```python
import torch

def _softmax(x, axis=-1):
    return torch.softmax(x, dim=axis)
def _gelu(x):
    return torch.nn.functional.gelu(x, approximate="tanh")
def _silu(x):
    return torch.nn.functional.silu(x)
def _sigmoid(x):
    return torch.sigmoid(x)
def _layernorm(x, axis=-1, eps=1e-5):
    m = x.mean(dim=axis, keepdim=True)
    v = x.var(dim=axis, keepdim=True, unbiased=False)
    return (x - m) / torch.sqrt(v + eps)
def _scatter_add(tgt, idx, src, axis=0):
    return tgt.clone().index_add_(axis, idx, src)
def _segment_sum(src, seg, n):
    out = torch.zeros((n,) + src.shape[1:], dtype=src.dtype)
    return out.index_add_(0, seg, src)
def _pad(x, pw):
    flat = tuple(v for pair in reversed(pw) for v in pair)
    return torch.nn.functional.pad(x, flat)

kernel_name = 'sequential_preLN__linear_kernel_hedgehog__glu_gated_geglu__prompt'

def setup_inputs():

    _w = (lambda a: (lambda t: t.to(torch.bfloat16) if t.is_floating_point() else t)(torch.as_tensor(a)))
    def _A_setup():
        B, S, D, H = 1, 2048, 640, 32
        Dh = D // H
        return {
            "x": _w((lambda *s: torch.randn(*s, dtype=torch.bfloat16) * 0.05)(B, S, D)),
            "wqkv": _w((lambda *s: torch.randn(*s, dtype=torch.bfloat16) * 0.2)(D, 3 * D)),
            "w_phi": _w((lambda *s: torch.randn(*s, dtype=torch.bfloat16) * 0.2)(H, Dh, Dh) * (Dh ** -0.5)),
            "wo": _w((lambda *s: torch.randn(*s, dtype=torch.bfloat16) * 0.05)(D, D)),
        }
    _A_in = _A_setup()

    def _B_setup():
        B, S, Dm, Df = 1, 2048, 640, 1536
        return {
            "x": _w((lambda *s: torch.randn(*s, dtype=torch.bfloat16) * 0.05)(B, S, Dm)),
            "w_gate": _w((lambda *s: torch.randn(*s, dtype=torch.bfloat16) * 0.4)(Dm, Df) * (Dm ** -0.5)),
            "w_up": _w((lambda *s: torch.randn(*s, dtype=torch.bfloat16) * 0.4)(Dm, Df) * (Dm ** -0.5)),
            "w_down": _w((lambda *s: torch.randn(*s, dtype=torch.bfloat16) * 0.4)(Df, Dm) * (Df ** -0.5)),
        }
    _B_in = _B_setup()

    return {
        "A_x": _A_in["x"],
        "A_wqkv": _A_in["wqkv"],
        "A_w_phi": _A_in["w_phi"],
        "A_wo": _A_in["wo"],
        "B_x": _B_in["x"],
        "B_w_gate": _B_in["w_gate"],
        "B_w_up": _B_in["w_up"],
        "B_w_down": _B_in["w_down"],
    }

def reference(A_x, A_wqkv, A_w_phi, A_wo, B_x, B_w_gate, B_w_up, B_w_down):
    def _A_ref(x, wqkv, w_phi, wo):
        B, S, D = x.shape
        H = 32; Dh = D // H
        qkv = (lambda x, *d: x.permute(*d))((x @ wqkv).reshape(B, S, 3, H, Dh), 2, 0, 3, 1, 4)
        q, k, v = qkv[0], qkv[1], qkv[2]
        pq = torch.einsum("bhsd,hdf->bhsf", q, w_phi)
        pk = torch.einsum("bhsd,hdf->bhsf", k, w_phi)
        phi_q = torch.concatenate([_softmax(pq, axis=-1), _softmax(-pq, axis=-1)], axis=-1)
        phi_k = torch.concatenate([_softmax(pk, axis=-1), _softmax(-pk, axis=-1)], axis=-1)
        kv = torch.einsum("bhsf,bhsd->bhfd", phi_k, v)
        z  = torch.einsum("bhsf->bhf", phi_k)
        num = torch.einsum("bhqf,bhfd->bhqd", phi_q, kv)
        den = torch.einsum("bhqf,bhf->bhq", phi_q, z)[..., None] + 1e-6
        o = num / den
        return (lambda x, *d: x.permute(*d))(o, 0, 2, 1, 3).reshape(B, S, D) @ wo

    def _B_ref(x, w_gate, w_up, w_down):
        g = torch.einsum("bsd,df->bsf", x, w_gate)
        u = torch.einsum("bsd,df->bsf", x, w_up)
        h = _gelu(g) * u
        return torch.einsum("bsf,fd->bsd", h, w_down)

    x = A_x
    h = x + _A_ref(_layernorm(x, axis=-1), A_wqkv, A_w_phi, A_wo)
    h2 = h + _B_ref(_layernorm(h, axis=-1), B_w_gate, B_w_up, B_w_down)
    return h2
```

```cpp
#include <hip/hip_runtime.h>
#include <hip/hip_bf16.h>
#include <stdint.h>
typedef unsigned short u16;
__device__ __forceinline__ float bf2f(u16 v){ return __uint_as_float(((unsigned)v)<<16); }
__device__ __forceinline__ u16 f2bf(float f){ unsigned u=__float_as_uint(f); u += 0x7fffu + ((u>>16)&1u); return (u16)(u>>16); }
constexpr int S=2048,D=640,H=32,DH=20,F=1536;

__global__ void ln_k(const u16* x, u16* y){
  int row=blockIdx.x; int l=threadIdx.x;
  float v[10]; float s=0;
  for(int i=0;i<10;i++){ v[i]=bf2f(x[row*D+l+64*i]); s+=v[i]; }
  for(int m=32;m>=1;m>>=1) s+=__shfl_xor(s,m);
  float mean=s/D; float q=0;
  for(int i=0;i<10;i++){ float d=v[i]-mean; q+=d*d; }
  for(int m=32;m>=1;m>>=1) q+=__shfl_xor(q,m);
  float r=1.f/sqrtf(q/D+1e-5f);
  for(int i=0;i<10;i++) y[row*D+l+64*i]=f2bf((v[i]-mean)*r);
}
template<int MODE> __global__ void gemm_k(const u16* A,const u16* B,const u16* B2,const u16* res,u16* C,int M,int N,int K){
  int n=blockIdx.x*64+threadIdx.x; int m=blockIdx.y*4+threadIdx.y;
  float a1=0,a2=0;
  for(int k=0;k<K;k++){ float a=bf2f(A[m*K+k]); a1+=a*bf2f(B[k*N+n]); if(MODE==2) a2+=a*bf2f(B2[k*N+n]); }
  if(MODE==0) C[m*N+n]=f2bf(a1);
  if(MODE==1) C[m*N+n]=f2bf(a1+bf2f(res[m*N+n]));
  if(MODE==2){ float g=a1; float t=tanhf(0.7978845608f*(g+0.044715f*g*g*g)); C[m*N+n]=f2bf(0.5f*g*(1.f+t)*a2); }
}
__global__ void phi_k(const u16* qkv,const u16* wphi,float* phi){
  int idx=blockIdx.x*blockDim.x+threadIdx.x;
  int h=idx%H, s=(idx/H)%S, w=idx/(H*S);
  float p[DH];
  for(int f=0;f<DH;f++){ float a=0; for(int d=0;d<DH;d++) a+=bf2f(qkv[s*3*D+w*D+h*DH+d])*bf2f(wphi[h*400+d*DH+f]); p[f]=a; }
  float mx=-1e30f,mn=1e30f; for(int f=0;f<DH;f++){mx=fmaxf(mx,p[f]);mn=fminf(mn,p[f]);}
  float s1=0,s2=0; for(int f=0;f<DH;f++){s1+=expf(p[f]-mx);s2+=expf(mn-p[f]);}
  for(int f=0;f<DH;f++){ phi[(size_t)idx*40+f]=expf(p[f]-mx)/s1; phi[(size_t)idx*40+20+f]=expf(mn-p[f])/s2; }
}
__global__ void kv_k(const u16* qkv,const float* phi,float* kv){
  int idx=blockIdx.x*blockDim.x+threadIdx.x; if(idx>=H*40*21) return;
  int d=idx%21,f=(idx/21)%40,h=idx/840; float a=0;
  for(int s=0;s<S;s++){ float pv=phi[((size_t)(S+s)*H+h)*40+f]; float v=d<20?bf2f(qkv[s*3*D+2*D+h*DH+d]):1.f; a+=pv*v; }
  kv[idx]=a;
}
__global__ void o_k(const float* phi,const float* kv,u16* o){
  int idx=blockIdx.x*blockDim.x+threadIdx.x; int c=idx%D,s=idx/D,h=c/DH,d=c%DH;
  float n=0,de=0; for(int f=0;f<40;f++){ float p=phi[((size_t)s*H+h)*40+f]; n+=p*kv[h*840+f*21+d]; de+=p*kv[h*840+f*21+20]; }
  o[idx]=f2bf(n/(de+1e-6f));
}
extern "C" void kernel_launch(void* const* d_in, const int* in_sizes, int n_in, void* d_out, int out_size, void* d_ws, size_t ws_size, hipStream_t stream) {
  const u16* x=(const u16*)d_in[0]; const u16* wqkv=(const u16*)d_in[1]; const u16* wphi=(const u16*)d_in[2]; const u16* wo=(const u16*)d_in[3];
  const u16* wg=(const u16*)d_in[5]; const u16* wu=(const u16*)d_in[6]; const u16* wd=(const u16*)d_in[7];
  char* ws=(char*)d_ws;
  u16* xn=(u16*)ws; ws+=S*D*2; u16* qkv=(u16*)ws; ws+=S*3*D*2; float* phi=(float*)ws; ws+=(size_t)2*S*H*40*4; float* kv=(float*)ws; ws+=H*840*4;
  u16* o=(u16*)ws; ws+=S*D*2; u16* h=(u16*)ws; ws+=S*D*2; u16* hn=(u16*)ws; ws+=S*D*2; u16* hh=(u16*)ws; ws+=S*F*2;
  ln_k<<<S,64,0,stream>>>(x,xn);
  gemm_k<0><<<dim3(3*D/64,S/4),dim3(64,4),0,stream>>>(xn,wqkv,nullptr,nullptr,qkv,S,3*D,D);
  phi_k<<<2*S*H/256,256,0,stream>>>(qkv,wphi,phi);
  kv_k<<<(H*840+63)/64,64,0,stream>>>(qkv,phi,kv);
  o_k<<<S*D/256,256,0,stream>>>(phi,kv,o);
  gemm_k<1><<<dim3(D/64,S/4),dim3(64,4),0,stream>>>(o,wo,nullptr,x,h,S,D,D);
  ln_k<<<S,64,0,stream>>>(h,hn);
  gemm_k<2><<<dim3(F/64,S/4),dim3(64,4),0,stream>>>(hn,wg,wu,nullptr,hh,S,F,D);
  gemm_k<1><<<dim3(D/64,S/4),dim3(64,4),0,stream>>>(hh,wd,nullptr,h,(u16*)d_out,S,D,F);
}
```

```cpp
#include <hip/hip_runtime.h>
#include <stddef.h>

__global__ __launch_bounds__(256, 1) void hh_block_kernel(const void* x, const void* wqkv, const void* wphi, const void* wo, const void* wg,
                                                          const void* wu, const void* wd, void* out, void* ws) {
  __shared__ __attribute__((aligned(16))) char lds[163840];
  unsigned long long ka = (unsigned long long)__builtin_amdgcn_kernarg_segment_ptr();
  unsigned blk = blockIdx.x, tid = threadIdx.x;
  asm volatile(
  "s_load_dwordx16 s[44:59], %0, 0x0\n"
  "s_load_dwordx2 s[60:61], %0, 0x40\n"
  "s_mov_b32 s24, %1\n"
  "v_lshrrev_b32 v3, 6, v0\n"
  "v_and_b32 v1, 63, v0\n"
  "s_and_b32 s27, s24, 7\n"
  "s_lshr_b32 s26, s24, 3\n"
  "v_readfirstlane_b32 s25, v3\n"
  "s_lshr_b32 s67, s27, 1\n"
  "s_lshl_b32 s67, s67, 2\n"
  "s_lshr_b32 s68, s26, 3\n"
  "s_add_u32 s40, s67, s68\n"
  "s_and_b32 s67, s27, 1\n"
  "s_lshl_b32 s67, s67, 3\n"
  "s_and_b32 s69, s26, 7\n"
  "s_add_u32 s41, s67, s69\n"
  "s_getpc_b64 s[36:37]\n"
  ".Lhh_pc:\n"
  "s_mov_b32 s38, .Lhh_end-.Lhh_pc\n"
  "s_mov_b32 s39, 0x20000\n"
  "s_and_b32 s37, s37, 0xffff\n"
  "s_lshl_b32 s67, s26, 2\n"
  "s_add_u32 s67, s67, s25\n"
  "s_lshl_b32 s67, s67, 9\n"
  "v_lshlrev_b32 v4, 7, v1\n"
  "v_cmp_gt_u32 vcc, 4, v1\n"
  "v_mov_b32 v5, 0x40000000\n"
  "s_nop 0\n"
  "v_cndmask_b32 v4, v5, v4, vcc\n"
  "buffer_load_dword v2, v4, s[36:39], s67 offen\n"
  "s_lshl_b32 s68, s68, 1\n"
  "s_add_u32 s70, s68, s69\n"
  "s_sub_u32 s67, s70, 5\n"
  "s_cmp_ge_u32 s70, 5\n"
  "s_cselect_b32 s70, s67, s70\n"
  "s_sub_u32 s67, s70, 5\n"
  "s_cmp_ge_u32 s70, 5\n"
  "s_cselect_b32 s70, s67, s70\n"
  "s_xor_b32 s42, s41, 1\n"
  "s_mov_b32 s87, 0x4b0\n"
  "s_sub_u32 s94, s42, s41\n"
  "s_mul_i32 s94, s94, 0x50\n"
  "s_sub_u32 s94, s94, 0xa50\n"
  "s_lshl_b32 s97, s25, 10\n"
  "s_mul_i32 s95, s40, 0x25800\n"
  "s_lshr_b32 s67, s41, 1\n"
  "s_mul_i32 s67, s67, 0xa0\n"
  "s_add_u32 s95, s95, s67\n"
  "s_and_b32 s96, s41, 1\n"
  "s_mul_i32 s96, s96, 0x50\n"
  "s_add_u32 s96, s96, 0x500\n"
  "v_mov_b32 v7, 0x40000000\n"
  "v_mul_u32_u24 v15, 0x1112, v0\n"
  "v_add_u32 v17, 0x100, v0\n"
  "v_add_u32 v20, 0x200, v0\n"
  "v_lshrrev_b32 v15, 16, v15\n"
  "v_mul_u32_u24 v18, 0x1112, v17\n"
  "v_mul_u32_u24 v21, 0x1112, v20\n"
  "v_mad_i32_i24 v16, v15, -15, v0\n"
  "v_lshrrev_b32 v18, 16, v18\n"
  "v_lshrrev_b32 v21, 16, v21\n"
  "v_mul_u32_u24 v15, 0xf00, v15\n"
  "v_mad_i32_i24 v19, v18, -15, v17\n"
  "v_mad_i32_i24 v22, v21, -15, v20\n"
  "v_cmp_lt_u32 s[88:89], 4, v16\n"
  "v_mul_u32_u24 v18, 0xf00, v18\n"
  "v_mul_u32_u24 v21, 0xf00, v21\n"
  "v_lshl_add_u32 v15, v16, 4, v15\n"
  "v_cmp_lt_u32 s[90:91], 4, v19\n"
  "v_cmp_lt_u32 s[92:93], 4, v22\n"
  "v_add_u32 v15, s96, v15\n"
  "v_lshl_add_u32 v18, v19, 4, v18\n"
  "v_lshl_add_u32 v21, v22, 4, v21\n"
  "v_cndmask_b32 v14, 0, 1, s[88:89]\n"
  "v_add_u32 v18, s96, v18\n"
  "v_add_u32 v21, s96, v21\n"
  "v_cmp_lt_u32 s[88:89], 9, v16\n"
  "v_cndmask_b32 v17, 0, 1, s[90:91]\n"
  "v_cndmask_b32 v20, 0, 1, s[92:93]\n"
  "v_mad_u32_u24 v15, v14, s87, v15\n"
  "v_cmp_lt_u32 s[90:91], 9, v19\n"
  "v_cmp_lt_u32 s[92:93], 9, v22\n"
  "v_cndmask_b32 v14, 0, 1, s[88:89]\n"
  "v_mad_u32_u24 v18, v17, s87, v18\n"
  "v_mad_u32_u24 v21, v20, s87, v21\n"
  "v_mad_i32_i24 v9, v14, s94, v15\n"
  "v_cndmask_b32 v17, 0, 1, s[90:91]\n"
  "v_cndmask_b32 v20, 0, 1, s[92:93]\n"
  "v_mad_i32_i24 v12, v17, s94, v18\n"
  "v_mad_i32_i24 v13, v20, s94, v21\n"
  "v_cmp_gt_u32 vcc, 0x58, v0\n"
  "s_nop 1\n"
  "v_cndmask_b32 v13, v7, v13, vcc\n"
  "s_mov_b32 s10, 0x258000\n"
  "s_mov_b32 s11, 0x20000\n"
  "s_waitcnt lgkmcnt(0)\n"
  "s_mov_b32 s8, s46\n"
  "s_and_b32 s9, s47, 0xffff\n"
  "s_add_u32 m0, s97, 0xc800\n"
  "s_nop 0\n"
  "buffer_load_dwordx4 v9, s[8:11], s95 offen lds\n"
  "s_add_u32 m0, s97, 0xd800\n"
  "s_nop 0\n"
  "buffer_load_dwordx4 v12, s[8:11], s95 offen lds\n"
  "s_add_u32 m0, s97, 0xe800\n"
  "s_nop 0\n"
  "buffer_load_dwordx4 v13, s[8:11], s95 offen lds\n"
  "s_mov_b32 s14, 0x1470000\n"
  "s_mov_b32 s15, 0x20000\n"
  "s_mov_b32 s12, s60\n"
  "s_and_b32 s13, s61, 0xffff\n"
  "buffer_load_dwordx2 v[10:11], off, s[12:15], 0 sc1\n"
  "s_mov_b32 s38, 0x6400\n"
  "s_mov_b32 s39, 0x20000\n"
  "s_mov_b32 s36, s48\n"
  "s_and_b32 s37, s49, 0xffff\n"
  "s_lshr_b32 s87, s41, 1\n"
  "s_mul_i32 s87, s87, 0xc80\n"
  "v_lshlrev_b32 v3, 4, v0\n"
  "s_add_u32 m0, s97, 0x25800\n"
  "s_nop 0\n"
  "buffer_load_dwordx4 v3, s[36:39], s87 offen lds\n"
  "v_lshrrev_b32 v3, 3, v1\n"
  "v_lshrrev_b32 v4, 4, v1\n"
  "v_and_b32 v5, 7, v1\n"
  "v_mul_u32_u24 v3, 0x500, v3\n"
  "v_xor_b32 v4, v4, v5\n"
  "v_lshl_add_u32 v9, v4, 4, v3\n"
  "v_xor_b32 v4, 4, v4\n"
  "v_lshl_add_u32 v12, v4, 4, v3\n"
  "v_add_u32 v13, 0x4800, v9\n"
  "v_add_u32 v12, 0x2400, v12\n"
  "v_add_u32 v14, 0x4800, v12\n"
  "s_lshl_b32 s68, s40, 2\n"
  "s_add_u32 s68, s68, s25\n"
  "s_mul_i32 s67, s68, 0xa000\n"
  "s_lshl_b32 s8, s25, 12\n"
  "s_mov_b32 s6, 0x280000\n"
  "s_mov_b32 s7, 0x20000\n"
  "s_mov_b32 s4, s44\n"
  "s_and_b32 s5, s45, 0xffff\n"
  "s_lshl_b32 s71, s70, 8\n"
  "s_add_u32 s71, s67, s71\n"
  "s_mul_i32 s82, s70, 0x2800\n"
  "s_add_u32 s70, s70, 1\n"
  "s_cmp_eq_u32 s70, 5\n"
  "s_cselect_b32 s70, 0, s70\n"
  "s_lshl_b32 s78, s70, 8\n"
  "s_add_u32 s78, s67, s78\n"
  "s_mul_i32 s83, s70, 0x2800\n"
  "s_add_u32 s70, s70, 1\n"
  "s_cmp_eq_u32 s70, 5\n"
  "s_cselect_b32 s70, 0, s70\n"
  "s_lshl_b32 s79, s70, 8\n"
  "s_add_u32 s79, s67, s79\n"
  "s_mul_i32 s84, s70, 0x2800\n"
  "s_add_u32 s70, s70, 1\n"
  "s_cmp_eq_u32 s70, 5\n"
  "s_cselect_b32 s70, 0, s70\n"
  "s_lshl_b32 s80, s70, 8\n"
  "s_add_u32 s80, s67, s80\n"
  "s_mul_i32 s85, s70, 0x2800\n"
  "s_add_u32 s70, s70, 1\n"
  "s_cmp_eq_u32 s70, 5\n"
  "s_cselect_b32 s70, 0, s70\n"
  "s_lshl_b32 s81, s70, 8\n"
  "s_add_u32 s81, s67, s81\n"
  "s_mul_i32 s86, s70, 0x2800\n"
  "s_add_u32 m0, s8, 0\n"
  "s_nop 0\n"
  "buffer_load_dwordx4 v9, s[4:7], s71 offen lds\n"
  "buffer_load_dwordx4 v12, s[4:7], s71 offen offset:1024 lds\n"
  "buffer_load_dwordx4 v13, s[4:7], s71 offen offset:2048 lds\n"
  "buffer_load_dwordx4 v14, s[4:7], s71 offen offset:3072 lds\n"
  "s_mul_i32 s9, s25, 0x1400\n"
  "s_mul_i32 s10, s41, 0x25800\n"
  "s_add_u32 s10, s10, 0x11f8000\n"
  "s_mov_b32 s18, 0xc8000\n"
  "s_mov_b32 s19, 0x20000\n"
  "s_mov_b32 s16, s50\n"
  "s_and_b32 s17, s51, 0xffff\n"
  "s_mov_b32 s22, 0x1e0000\n"
  "s_mov_b32 s23, 0x20000\n"
  "s_mov_b32 s20, s52\n"
  "s_and_b32 s21, s53, 0xffff\n"
  "s_mov_b32 s30, 0x1e0000\n"
  "s_mov_b32 s31, 0x20000\n"
  "s_mov_b32 s28, s56\n"
  "s_and_b32 s29, s57, 0xffff\n"
  "s_mov_b32 s34, 0x280000\n"
  "s_mov_b32 s35, 0x20000\n"
  "s_mov_b32 s32, s58\n"
  "s_and_b32 s33, s59, 0xffff\n"
  "s_and_b32 s67, s40, 1\n"
  "s_cmp_eq_u32 s67, 1\n"
  "s_cselect_b32 s20, s54, s52\n"
  "s_cselect_b32 s21, s55, s53\n"
  "s_and_b32 s21, s21, 0xffff\n"
  "s_lshr_b32 s67, s41, 3\n"
  "s_lshl_b32 s67, s67, 2\n"
  "s_and_b32 s68, s41, 3\n"
  "s_add_u32 s62, s67, s68\n"
  "s_lshr_b32 s43, s41, 2\n"
  "s_and_b32 s43, s43, 1\n"
  "s_mov_b32 s74, 0\n"
  "s_mov_b32 s75, -1\n"
  "s_mov_b32 s76, 0xffff0000\n"
  "s_mov_b32 s77, 0xffff0000\n"
  "s_lshl_b32 s67, s40, 4\n"
  "s_add_u32 s67, s67, s41\n"
  "s_lshl_b32 s67, s67, 5\n"
  "s_lshl_b32 s68, s25, 3\n"
  "s_add_u32 s65, s67, s68\n"
  "s_lshl_b32 s67, s41, 4\n"
  "s_add_u32 s67, s67, s40\n"
  "s_lshl_b32 s67, s67, 5\n"
  "s_add_u32 s66, s67, s68\n"
  "v_and_b32 v15, 15, v1\n"
  "v_lshrrev_b32 v16, 5, v1\n"
  "v_bfe_u32 v17, v1, 2, 2\n"
  "v_and_b32 v18, 3, v1\n"
  "v_lshrrev_b32 v19, 4, v1\n"
  "v_bfe_u32 v20, v1, 4, 1\n"
  "v_lshlrev_b32 v21, 4, v1\n"
  "v_mov_b32 v22, 0x40000000\n"
  "v_bfe_u32 v3, v1, 1, 3\n"
  "v_xor_b32 v3, v3, v19\n"
  "v_lshl_add_u32 v4, v15, 7, s8\n"
  "v_lshl_add_u32 v23, v3, 4, v4\n"
  "v_xor_b32 v3, 4, v3\n"
  "v_lshl_add_u32 v24, v3, 4, v4\n"
  "v_add_u32 v25, 0x21800, v23\n"
  "v_add_u32 v26, 0x21800, v24\n"
  "s_lshl_b32 s67, s26, 2\n"
  "s_add_u32 s67, s67, s25\n"
  "s_mul_i32 s67, s67, 25\n"
  "s_and_b32 s36, s27, 1\n"
  "s_mul_i32 s36, s36, 0x280\n"
  "v_add_u32 v3, s67, v1\n"
  "v_cmp_gt_u32 vcc, 25, v1\n"
  "v_mul_u32_u24 v4, 0x3334, v3\n"
  "v_lshrrev_b32 v4, 16, v4\n"
  "v_mad_i32_i24 v3, v4, -5, v3\n"
  "v_mul_u32_u24 v4, 0x500, v4\n"
  "v_lshl_add_u32 v3, v3, 7, v4\n"
  "v_cndmask_b32 v27, v22, v3, vcc\n"
  "s_cmp_ge_u32 s25, 2\n"
  "s_cselect_b32 s67, 0x40000000, 0\n"
  "s_cselect_b32 s38, 0, 0x2800\n"
  "s_add_u32 s68, s97, 0x2000\n"
  "s_cmp_ge_u32 s25, 2\n"
  "s_cselect_b32 s37, 0x27200, s68\n"
  "v_add_u32 v28, s67, v21\n"
  "v_lshl_add_u32 v3, v16, 3, v17\n"
  "v_mul_u32_u24 v3, 0xf0, v3\n"
  "v_lshl_add_u32 v3, v20, 5, v3\n"
  "v_lshl_add_u32 v3, v18, 3, v3\n"
  "s_lshl_b32 s67, s25, 6\n"
  "s_add_u32 s67, s67, 0xc800\n"
  "v_add_u32 v29, s67, v3\n"
  "s_lshl_b32 s67, s25, 1\n"
  "v_add_u32 v3, s67, v20\n"
  "v_cmp_lt_u32 vcc, 4, v3\n"
  "v_mov_b32 v4, 0x1400\n"
  "v_mov_b32 v5, 0xa00\n"
  "v_cndmask_b32 v4, v4, v5, vcc\n"
  "v_mov_b32 v5, 0x17c00\n"
  "v_cndmask_b32 v5, 0, v5, vcc\n"
  "v_lshl_add_u32 v5, v3, 10, v5\n"
  "v_lshl_add_u32 v5, v15, 4, v5\n"
  "v_cmp_eq_u32 vcc, 7, v3\n"
  "s_nop 1\n"
  "v_cndmask_b32 v6, 8, 7, vcc\n"
  "v_and_b32 v7, 8, v1\n"
  "v_cndmask_b32 v7, 0, v7, vcc\n"
  "v_cmp_eq_u32 vcc, 0, v7\n"
  "s_mul_i32 s67, s40, 5\n"
  "s_nop 0\n"
  "v_cndmask_b32 v5, v22, v5, vcc\n"
  "v_add_u32 v3, s67, v16\n"
  "v_lshrrev_b32 v7, 2, v3\n"
  "v_and_b32 v8, 3, v3\n"
  "v_mad_u32_u24 v30, v7, v4, v5\n"
  "v_lshlrev_b32 v8, v6, v8\n"
  "v_add_u32 v30, v30, v8\n"
  "v_add_u32 v3, 2, v3\n"
  "v_lshrrev_b32 v7, 2, v3\n"
  "v_and_b32 v8, 3, v3\n"
  "v_mad_u32_u24 v31, v7, v4, v5\n"
  "v_lshlrev_b32 v8, v6, v8\n"
  "v_add_u32 v31, v31, v8\n"
  "v_add_u32 v3, 2, v3\n"
  "v_lshrrev_b32 v7, 2, v3\n"
  "v_and_b32 v8, 3, v3\n"
  "v_mad_u32_u24 v32, v7, v4, v5\n"
  "v_lshlrev_b32 v8, v6, v8\n"
  "v_add_u32 v32, v32, v8\n"
  "v_cndmask_b32 v32, v32, v22, s[74:75]\n"
  "v_lshlrev_b32 v3, 2, v1\n"
  "v_cndmask_b32 v33, v3, v22, s[74:75]\n"
  "s_lshl_b32 s39, s41, 4\n"
  "s_add_u32 s39, s39, s40\n"
  "s_lshl_b32 s39, s39, 9\n"
  "s_lshl_b32 s67, s25, 7\n"
  "s_add_u32 s39, s39, s67\n"
  "s_add_u32 s39, s39, 0x1450000\n"
  "v_add_u32 v3, 0, v0\n"
  "v_add_u32 v5, 0x100, v0\n"
  "v_add_u32 v7, 0x200, v0\n"
  "v_mul_u32_u24 v4, 0x199a, v3\n"
  "v_mul_u32_u24 v6, 0x199a, v5\n"
  "v_mul_u32_u24 v8, 0x199a, v7\n"
  "v_lshrrev_b32 v4, 16, v4\n"
  "v_lshrrev_b32 v6, 16, v6\n"
  "v_lshrrev_b32 v8, 16, v8\n"
  "v_mad_i32_i24 v3, v4, -10, v3\n"
  "v_mad_i32_i24 v5, v6, -10, v5\n"
  "v_mad_i32_i24 v7, v8, -10, v7\n"
  "v_mul_u32_u24 v48, 0x500, v4\n"
  "v_mul_u32_u24 v49, 0x500, v6\n"
  "v_mul_u32_u24 v50, 0x500, v8\n"
  "v_lshl_add_u32 v48, v3, 4, v48\n"
  "v_lshl_add_u32 v49, v5, 4, v49\n"
  "v_lshl_add_u32 v50, v7, 4, v50\n"
  "v_add_u32 v3, 0x300, v0\n"
  "v_add_u32 v5, 0x400, v0\n"
  "v_mul_u32_u24 v4, 0x199a, v3\n"
  "v_mul_u32_u24 v6, 0x199a, v5\n"
  "v_lshrrev_b32 v4, 16, v4\n"
  "v_lshrrev_b32 v6, 16, v6\n"
  "v_mad_i32_i24 v3, v4, -10, v3\n"
  "v_mad_i32_i24 v5, v6, -10, v5\n"
  "v_mul_u32_u24 v51, 0x500, v4\n"
  "v_mul_u32_u24 v52, 0x500, v6\n"
  "v_lshl_add_u32 v51, v3, 4, v51\n"
  "v_lshl_add_u32 v52, v5, 4, v52\n"
  "s_mul_i32 s44, s62, 0xa0\n"
  "s_add_u32 s67, s97, 0x25000\n"
  "s_add_u32 s68, s97, 0xc000\n"
  "s_cmp_ge_u32 s25, 2\n"
  "s_cselect_b32 s45, s68, s67\n"
  "v_add_u32 v4, 0, v0\n"
  "v_mul_u32_u24 v5, 0x1556, v4\n"
  "v_lshrrev_b32 v5, 16, v5\n"
  "v_cmp_gt_u32 vcc, 0x50, v5\n"
  "v_mad_i32_i24 v4, v5, -12, v4\n"
  "v_mul_u32_u24 v5, 0xc00, v5\n"
  "v_lshl_add_u32 v5, v4, 4, v5\n"
  "v_cndmask_b32 v53, v22, v5, vcc\n"
  "v_add_u32 v4, 0x100, v0\n"
  "v_mul_u32_u24 v5, 0x1556, v4\n"
  "v_lshrrev_b32 v5, 16, v5\n"
  "v_cmp_gt_u32 vcc, 0x50, v5\n"
  "v_mad_i32_i24 v4, v5, -12, v4\n"
  "v_mul_u32_u24 v5, 0xc00, v5\n"
  "v_lshl_add_u32 v5, v4, 4, v5\n"
  "v_cndmask_b32 v54, v22, v5, vcc\n"
  "v_add_u32 v4, 0x200, v0\n"
  "v_mul_u32_u24 v5, 0x1556, v4\n"
  "v_lshrrev_b32 v5, 16, v5\n"
  "v_cmp_gt_u32 vcc, 0x50, v5\n"
  "v_mad_i32_i24 v4, v5, -12, v4\n"
  "v_mul_u32_u24 v5, 0xc00, v5\n"
  "v_lshl_add_u32 v5, v4, 4, v5\n"
  "v_cndmask_b32 v55, v22, v5, vcc\n"
  "v_add_u32 v4, 0x300, v0\n"
  "v_mul_u32_u24 v5, 0x1556, v4\n"
  "v_lshrrev_b32 v5, 16, v5\n"
  "v_cmp_gt_u32 vcc, 0x50, v5\n"
  "v_mad_i32_i24 v4, v5, -12, v4\n"
  "v_mul_u32_u24 v5, 0xc00, v5\n"
  "v_lshl_add_u32 v5, v4, 4, v5\n"
  "v_cndmask_b32 v56, v22, v5, vcc\n"
  "s_lshl_b32 s67, s43, 5\n"
  "s_sub_u32 s67, 63, s67\n"
  "v_add_u32 v4, 0, v0\n"
  "v_mul_u32_u24 v5, 0x199a, v4\n"
  "v_lshrrev_b32 v5, 16, v5\n"
  "v_cmp_gt_u32 vcc, 64, v5\n"
  "v_mad_i32_i24 v4, v5, -10, v4\n"
  "v_and_b32 v5, s67, v5\n"
  "v_mul_u32_u24 v5, 0x500, v5\n"
  "v_lshl_add_u32 v5, v4, 4, v5\n"
  "v_cndmask_b32 v57, v22, v5, vcc\n"
  "v_add_u32 v4, 0x100, v0\n"
  "v_mul_u32_u24 v5, 0x199a, v4\n"
  "v_lshrrev_b32 v5, 16, v5\n"
  "v_cmp_gt_u32 vcc, 64, v5\n"
  "v_mad_i32_i24 v4, v5, -10, v4\n"
  "v_and_b32 v5, s67, v5\n"
  "v_mul_u32_u24 v5, 0x500, v5\n"
  "v_lshl_add_u32 v5, v4, 4, v5\n"
  "v_cndmask_b32 v58, v22, v5, vcc\n"
  "v_add_u32 v4, 0x200, v0\n"
  "v_mul_u32_u24 v5, 0x199a, v4\n"
  "v_lshrrev_b32 v5, 16, v5\n"
  "v_cmp_gt_u32 vcc, 64, v5\n"
  "v_mad_i32_i24 v4, v5, -10, v4\n"
  "v_and_b32 v5, s67, v5\n"
  "v_mul_u32_u24 v5, 0x500, v5\n"
  "v_lshl_add_u32 v5, v4, 4, v5\n"
  "v_cndmask_b32 v59, v22, v5, vcc\n"
  "s_lshr_b32 s67, s40, 1\n"
  "s_mul_i32 s67, s67, 0x3c000\n"
  "s_mul_i32 s68, s41, 0xc0\n"
  "s_add_u32 s46, s67, s68\n"
  "s_mul_i32 s67, s40, 0x60\n"
  "s_lshl_b32 s68, s43, 6\n"
  "s_add_u32 s67, s67, s68\n"
  "s_mul_i32 s67, s67, 0x500\n"
  "s_mul_i32 s68, s62, 0xa0\n"
  "s_add_u32 s47, s67, s68\n"
  "v_lshlrev_b32 v62, 3, v1\n"
  "s_lshl_b32 s48, s41, 9\n"
  "s_add_u32 s48, s48, 0xf000\n"
  "v_mov_b32 v100, 0\n"
  "v_mov_b32 v101, 0\n"
  "v_mov_b32 v102, 0\n"
  "v_mov_b32 v103, 0\n"
  "v_mov_b32 v104, 0\n"
  "v_mov_b32 v105, 0\n"
  "v_mov_b32 v106, 0\n"
  "v_mov_b32 v107, 0\n"
  "s_mov_b32 s49, 0x3f803f80\n"
  "v_mov_b32 v34, 0\n"
  "v_mov_b32 v35, 0\n"
  "s_waitcnt vmcnt(4)\n"
  "s_barrier\n"
  "ds_read_b64_tr_b16 v[36:37], v29\n"
  "ds_read_b64_tr_b16 v[38:39], v29 offset:960\n"
  "ds_read_b64_tr_b16 v[40:41], v29 offset:3840\n"
  "ds_read_b64_tr_b16 v[42:43], v29 offset:4800\n"
  "ds_read_b64_tr_b16 v[44:45], v29 offset:7680\n"
  "ds_read_b64_tr_b16 v[46:47], v29 offset:8640\n"
  "s_waitcnt lgkmcnt(4)\n"
  "buffer_store_dwordx4 v[36:39], v30, s[12:15], s10 offen sc1\n"
  "v_dot2c_f32_bf16 v34, s49, v36\n"
  "v_dot2c_f32_bf16 v35, s49, v37\n"
  "v_dot2c_f32_bf16 v34, s49, v38\n"
  "v_dot2c_f32_bf16 v35, s49, v39\n"
  "s_waitcnt lgkmcnt(2)\n"
  "buffer_store_dwordx4 v[40:43], v31, s[12:15], s10 offen sc1\n"
  "v_dot2c_f32_bf16 v34, s49, v40\n"
  "v_dot2c_f32_bf16 v35, s49, v41\n"
  "v_dot2c_f32_bf16 v34, s49, v42\n"
  "v_dot2c_f32_bf16 v35, s49, v43\n"
  "s_waitcnt lgkmcnt(0)\n"
  "buffer_store_dwordx4 v[44:47], v32, s[12:15], s10 offen sc1\n"
  "v_dot2c_f32_bf16 v34, s49, v44\n"
  "v_dot2c_f32_bf16 v35, s49, v45\n"
  "v_dot2c_f32_bf16 v34, s49, v46\n"
  "v_dot2c_f32_bf16 v35, s49, v47\n"
  "s_nop 2\n"
  "v_add_f32 v34, v34, v35\n"
  "v_mov_b32 v35, v34\n"
  "s_nop 1\n"
  "v_permlane32_swap_b32 v34, v35\n"
  "v_add_f32 v34, v34, v35\n"
  "v_cvt_pk_bf16_f32 v35, v34, 0\n"
  "v_lshlrev_b32 v35, 16, v35\n"
  "v_sub_f32 v35, v34, v35\n"
  "v_cvt_pk_bf16_f32 v34, v34, v35\n"
  "buffer_store_dword v34, v33, s[12:15], s39 offen sc1\n"
  "s_add_u32 m0, s8, 0x4000\n"
  "s_add_u32 s69, s71, 0x80\n"
  "buffer_load_dwordx4 v9, s[4:7], s69 offen lds\n"
  "buffer_load_dwordx4 v12, s[4:7], s69 offen offset:1024 lds\n"
  "buffer_load_dwordx4 v13, s[4:7], s69 offen offset:2048 lds\n"
  "buffer_load_dwordx4 v14, s[4:7], s69 offen offset:3072 lds\n"
  "v_readfirstlane_b32 s72, v10\n"
  "v_readfirstlane_b32 s73, v11\n"
  "s_nop 2\n"
  "s_add_u32 s72, s72, 0x7f4a7c15\n"
  "s_addc_u32 s73, s73, 0x9e3779b9\n"
  "v_mov_b32 v10, s72\n"
  "v_mov_b32 v11, s73\n"
  "s_add_u32 s67, s97, 0x22400\n"
  "s_cmp_eq_u32 s25, 3\n"
  "s_cselect_b32 s67, 0x27200, s67\n"
  "v_add_u32 v60, s67, v21\n"
  "s_add_u32 s67, s97, 0x25000\n"
  "s_cmp_ge_u32 s25, 2\n"
  "s_cselect_b32 s67, 0x27200, s67\n"
  "v_add_u32 v61, s67, v21\n"
  "s_cmp_eq_u32 s25, 3\n"
  "s_nop 3\n"
  "s_cbranch_scc1 .Lhh_w3_1\n"
  "s_lshl_b32 s67, s25, 6\n"
  "s_add_u32 s67, s67, 0x1f400\n"
  "v_lshl_add_u32 v3, v18, 3, s67\n"
  "v_mad_u32_u24 v4, v19, 20, v17\n"
  "v_mul_u32_u24 v4, 0xc0, v4\n"
  "v_add_u32 v34, v3, v4\n"
  "v_mad_u32_u24 v4, v20, 40, v17\n"
  "v_mul_u32_u24 v4, 0xc0, v4\n"
  "v_lshl_add_u32 v4, v16, 5, v4\n"
  "v_add_u32 v35, v3, v4\n"
  "s_lshr_b32 s55, s40, 1\n"
  "s_and_b32 s56, s40, 1\n"
  "s_lshr_b32 s67, s55, 1\n"
  "s_mul_i32 s57, s67, 5\n"
  "s_and_b32 s59, s55, 1\n"
  "s_mul_i32 s53, s41, 0x3c000\n"
  "s_add_u32 s53, s53, 0x658000\n"
  "s_lshl_b32 s67, s25, 1\n"
  "s_add_u32 s67, s67, 0\n"
  "s_lshl_b32 s68, s67, 1\n"
  "s_mul_i32 s69, s67, 40\n"
  "s_cmp_lt_u32 s67, 2\n"
  "s_cselect_b32 s68, s68, s69\n"
  "s_cselect_b32 s58, 4, 2\n"
  "s_add_u32 s68, s68, s56\n"
  "s_add_u32 s69, s57, 4\n"
  "s_mul_i32 s69, s69, s58\n"
  "s_add_u32 s69, s69, s68\n"
  "s_lshl_b32 s69, s69, 1\n"
  "s_add_u32 s69, s69, s59\n"
  "s_lshl_b32 s60, s69, 9\n"
  "v_mov_b32 v3, s60\n"
  "s_lshl_b32 s69, s59, 1\n"
  "s_add_u32 s69, s69, s57\n"
  "s_mul_i32 s69, s69, s58\n"
  "s_add_u32 s69, s69, s68\n"
  "s_lshl_b32 s69, s69, 10\n"
  "s_add_u32 s39, s53, s69\n"
  "s_lshl_b32 s69, s58, 10\n"
  "s_add_u32 s50, s39, s69\n"
  "s_lshl_b32 s67, s25, 1\n"
  "s_add_u32 s67, s67, 1\n"
  "s_lshl_b32 s68, s67, 1\n"
  "s_mul_i32 s69, s67, 40\n"
  "s_cmp_lt_u32 s67, 2\n"
  "s_cselect_b32 s68, s68, s69\n"
  "s_cselect_b32 s58, 4, 2\n"
  "s_add_u32 s68, s68, s56\n"
  "s_add_u32 s69, s57, 4\n"
  "s_mul_i32 s69, s69, s58\n"
  "s_add_u32 s69, s69, s68\n"
  "s_lshl_b32 s69, s69, 1\n"
  "s_add_u32 s69, s69, s59\n"
  "s_lshl_b32 s60, s69, 9\n"
  "v_mov_b32 v4, s60\n"
  "s_lshl_b32 s69, s59, 1\n"
  "s_add_u32 s69, s69, s57\n"
  "s_mul_i32 s69, s69, s58\n"
  "s_add_u32 s69, s69, s68\n"
  "s_lshl_b32 s69, s69, 10\n"
  "s_add_u32 s51, s53, s69\n"
  "s_lshl_b32 s69, s58, 10\n"
  "s_add_u32 s52, s51, s69\n"
  "v_cndmask_b32 v30, v3, v4, s[74:75]\n"
  "v_lshl_add_u32 v30, v20, 8, v30\n"
  "v_lshl_add_u32 v30, v15, 4, v30\n"
  "v_mov_b32 v29, v21\n"
  "v_mov_b32 v31, 0x40000000\n"
  "v_mov_b32 v32, 0x40000000\n"
  "s_mul_i32 s69, s41, 0x1800\n"
  "s_mul_i32 s67, s55, 0x300\n"
  "s_add_u32 s69, s69, s67\n"
  "s_mul_i32 s68, s56, 0x180\n"
  "s_add_u32 s69, s69, s68\n"
  "s_lshl_b32 s67, s25, 7\n"
  "s_add_u32 s69, s69, s67\n"
  "s_add_u32 s54, s69, 0x640000\n"
  "v_lshlrev_b32 v3, 2, v1\n"
  "v_cndmask_b32 v33, v3, v22, s[74:75]\n"
  "s_nop 4\n"
  "s_branch .Lhh_wd_2\n"
  ".Lhh_w3_1:\n"
  "v_lshl_add_u32 v3, v19, 2, v17\n"
  "v_mul_u32_u24 v3, 0xa0, v3\n"
  "v_mad_u32_u24 v3, v18, 40, v3\n"
  "v_add_u32 v34, 0x23000, v3\n"
  "s_and_b32 s67, s40, 7\n"
  "s_lshr_b32 s67, s67, 1\n"
  "s_mul_i32 s67, s67, 20\n"
  "s_and_b32 s69, s40, 1\n"
  "s_mul_i32 s69, s69, 5\n"
  "s_add_u32 s67, s67, s69\n"
  "s_mul_i32 s68, s43, 0xa0\n"
  "s_add_u32 s67, s67, s68\n"
  "s_mul_i32 s59, s43, 0x40000000\n"
  "s_lshr_b32 s68, s40, 3\n"
  "s_mul_i32 s68, s68, 10\n"
  "s_add_u32 s67, s67, s68\n"
  "s_lshl_b32 s67, s67, 10\n"
  "s_mul_i32 s39, s62, 0x3c000\n"
  "s_add_u32 s39, s39, s67\n"
  "s_add_u32 s39, s39, 0xa18000\n"
  "s_add_u32 s50, s39, 0x1000\n"
  "s_add_u32 s51, s39, 0x14000\n"
  "s_add_u32 s52, s39, 0x15000\n"
  "s_mov_b32 s53, 0\n"
  "s_mov_b32 s54, 0\n"
  "v_mov_b32 v29, 0x40000000\n"
  "v_mov_b32 v30, 0x40000000\n"
  "v_mov_b32 v33, 0x40000000\n"
  "v_mov_b32 v31, v21\n"
  "v_add_u32 v32, s59, v21\n"
  "s_nop 4\n"
  ".Lhh_wd_2:\n"
  "v_lshlrev_b32 v41, 11, v19\n"
  "v_lshl_add_u32 v41, v15, 2, v41\n"
  "v_add_u32 v41, 0x1b800, v41\n"
  "v_add_u32 v42, 0xc800, v21\n"
  "v_add_u32 v43, 0x19000, v21\n"
  "v_and_b32 v3, 7, v1\n"
  "v_lshlrev_b32 v3, 4, v3\n"
  "v_lshl_add_u32 v44, v19, 7, v3\n"
  "s_mul_i32 s67, s25, 0x2900\n"
  "s_add_u32 s67, s67, 0xc800\n"
  "v_mul_u32_u24 v3, 0x148, v15\n"
  "v_add_u32 v3, s67, v3\n"
  "v_lshlrev_b32 v4, 3, v19\n"
  "v_mul_u32_u24 v36, 0x50, v16\n"
  "v_lshl_add_u32 v36, v20, 3, v36\n"
  "v_add_u32 v36, v36, v3\n"
  "s_mov_b32 s67, 0x8005828\n"
  "v_bfe_u32 v45, s67, v4, 8\n"
  "v_add_u32 v45, v45, v3\n"
  "s_mov_b32 s67, 0x30201810\n"
  "v_bfe_u32 v46, s67, v4, 8\n"
  "v_add_u32 v46, v46, v3\n"
  "s_mov_b32 s67, 0x50484038\n"
  "v_bfe_u32 v47, s67, v4, 8\n"
  "v_add_u32 v47, v47, v3\n"
  "v_lshl_add_u32 v3, v16, 3, v17\n"
  "v_mul_u32_u24 v3, 0x148, v3\n"
  "v_lshl_add_u32 v3, v20, 5, v3\n"
  "v_lshl_add_u32 v3, v18, 3, v3\n"
  "s_lshr_b32 s67, s25, 1\n"
  "s_and_b32 s56, s25, 1\n"
  "s_mul_i32 s69, s67, 0x50\n"
  "s_lshl_b32 s70, s56, 6\n"
  "s_add_u32 s69, s69, s70\n"
  "s_add_u32 s69, s69, 0xc800\n"
  "s_mul_i32 s70, s67, 48\n"
  "s_add_u32 s70, s70, 0xc8a0\n"
  "v_add_u32 v37, s69, v3\n"
  "v_add_u32 v38, s70, v3\n"
  "s_lshl_b32 s55, s41, 4\n"
  "s_add_u32 s55, s55, s40\n"
  "s_lshl_b32 s55, s55, 12\n"
  "s_mul_i32 s69, s67, 0x780\n"
  "s_add_u32 s55, s55, s69\n"
  "s_add_u32 s55, s55, 0x20000\n"
  "v_and_b32 v5, 31, v1\n"
  "v_cmp_gt_u32 vcc, 24, v5\n"
  "v_mad_u32_u24 v3, v16, 24, v5\n"
  "v_mov_b32 v4, 0x4000000\n"
  "v_cndmask_b32 v3, v4, v3, vcc\n"
  "v_lshlrev_b32 v39, 4, v3\n"
  "v_lshlrev_b32 v40, 3, v3\n"
  "v_mul_u32_u24 v108, 40, v17\n"
  "v_and_b32 v109, 1, v18\n"
  "v_lshl_add_u32 v109, v109, 4, v108\n"
  "v_lshlrev_b32 v110, 3, v19\n"
  "s_and_b32 s57, s41, 1\n"
  "s_mul_i32 s57, s57, 0x640\n"
  "s_add_u32 s57, s57, 0x25800\n"
  "v_add_u32 v109, s57, v109\n"
  "v_mov_b32 v108, 0xa0\n"
  "s_mov_b32 s57, 0x3020100\n"
  "v_bfe_u32 v63, s57, v110, 8\n"
  "v_mad_u32_u24 v63, v63, v108, v109\n"
  "s_mov_b32 s57, 0x7060504\n"
  "v_bfe_u32 v66, s57, v110, 8\n"
  "v_mad_u32_u24 v66, v66, v108, v109\n"
  "s_mov_b32 s57, 0x9090908\n"
  "v_bfe_u32 v67, s57, v110, 8\n"
  "v_mad_u32_u24 v67, v67, v108, v109\n"
  "s_mov_b32 s58, 0xff00ff\n"
  "s_mov_b32 s59, 0xff00ff\n"
  "s_mov_b32 s60, 0xff0000ff\n"
  "s_mov_b32 s61, 0xff00ff00\n"
  "s_mov_b32 s88, 0xff00ff00\n"
  "s_mov_b32 s89, 0\n"
  "v_and_b32 v108, 1, v18\n"
  "v_lshlrev_b32 v108, 4, v108\n"
  "s_and_b32 s57, s41, 1\n"
  "s_mul_i32 s57, s57, 0xfffff380\n"
  "s_add_u32 s57, s57, 0x640\n"
  "s_add_u32 s64, s66, 0xf000\n"
  "s_waitcnt vmcnt(4)\n"
  "s_mov_b64 exec, 1\n"
  "buffer_store_dwordx2 v[10:11], off, s[12:15], s64 sc1\n"
  "s_mov_b64 exec, -1\n"
  "ds_read_b128 v[68:71], v23\n"
  "ds_read_b128 v[72:75], v23 offset:2048\n"
  "ds_read_b128 v[76:79], v24\n"
  "ds_read_b128 v[80:83], v24 offset:2048\n"
  "ds_read_b128 a[0:3], v23\n"
  "ds_read_b128 a[4:7], v23 offset:2048\n"
  "ds_read_b128 a[8:11], v24\n"
  "ds_read_b128 a[12:15], v24 offset:2048\n"
  "s_add_u32 m0, s8, 0x8000\n"
  "s_nop 0\n"
  "buffer_load_dwordx4 v9, s[4:7], s78 offen lds\n"
  "buffer_load_dwordx4 v12, s[4:7], s78 offen offset:1024 lds\n"
  "buffer_load_dwordx4 v13, s[4:7], s78 offen offset:2048 lds\n"
  "buffer_load_dwordx4 v14, s[4:7], s78 offen offset:3072 lds\n"
  "s_add_u32 m0, s8, 0x21800\n"
  "s_add_u32 s69, s78, 0x80\n"
  "buffer_load_dwordx4 v9, s[4:7], s69 offen lds\n"
  "buffer_load_dwordx4 v12, s[4:7], s69 offen offset:1024 lds\n"
  "buffer_load_dwordx4 v13, s[4:7], s69 offen offset:2048 lds\n"
  "buffer_load_dwordx4 v14, s[4:7], s69 offen offset:3072 lds\n"
  "ds_read_b64_tr_b16 v[112:113], v63\n"
  "ds_read_b64_tr_b16 v[114:115], v66\n"
  "ds_read_b64_tr_b16 v[116:117], v67\n"
  "ds_read_b64_tr_b16 v[120:121], v63 offset:8\n"
  "ds_read_b64_tr_b16 v[122:123], v66 offset:8\n"
  "ds_read_b64_tr_b16 v[124:125], v67 offset:8\n"
  "v_sub_u32 v63, v63, v108\n"
  "v_sub_u32 v66, v66, v108\n"
  "v_sub_u32 v67, v67, v108\n"
  "ds_read_b64_tr_b16 v[128:129], v63 offset:32\n"
  "ds_read_b64_tr_b16 v[130:131], v66 offset:32\n"
  "ds_read_b64_tr_b16 v[132:133], v67 offset:32\n"
  "v_add3_u32 v63, v63, v108, s57\n"
  "v_add3_u32 v66, v66, v108, s57\n"
  "v_add3_u32 v67, v67, v108, s57\n"
  "s_waitcnt lgkmcnt(8)\n"
  "v_cndmask_b32 v112, 0, v112, s[58:59]\n"
  "v_cndmask_b32 v113, 0, v113, s[58:59]\n"
  "s_waitcnt lgkmcnt(7)\n"
  "v_cndmask_b32 v114, 0, v114, s[60:61]\n"
  "v_cndmask_b32 v115, 0, v115, s[60:61]\n"
  "s_waitcnt lgkmcnt(6)\n"
  "v_cndmask_b32 v116, 0, v116, s[88:89]\n"
  "v_cndmask_b32 v117, 0, v117, s[88:89]\n"
  "v_accvgpr_write_b32 a200, v112\n"
  "v_accvgpr_write_b32 a201, v113\n"
  "v_accvgpr_write_b32 a202, v114\n"
  "v_accvgpr_write_b32 a203, v115\n"
  "v_accvgpr_write_b32 a204, v116\n"
  "v_accvgpr_write_b32 a205, v117\n"
  "v_accvgpr_write_b32 a206, 0\n"
  "v_accvgpr_write_b32 a207, 0\n"
  "s_waitcnt lgkmcnt(5)\n"
  "v_cndmask_b32 v120, 0, v120, s[58:59]\n"
  "v_cndmask_b32 v121, 0, v121, s[58:59]\n"
  "s_waitcnt lgkmcnt(4)\n"
  "v_cndmask_b32 v122, 0, v122, s[60:61]\n"
  "v_cndmask_b32 v123, 0, v123, s[60:61]\n"
  "s_waitcnt lgkmcnt(3)\n"
  "v_cndmask_b32 v124, 0, v124, s[88:89]\n"
  "v_cndmask_b32 v125, 0, v125, s[88:89]\n"
  "v_accvgpr_write_b32 a208, v120\n"
  "v_accvgpr_write_b32 a209, v121\n"
  "s_waitcnt vmcnt(9)\n"
  "s_waitcnt lgkmcnt(0)\n"
  "ds_read_b128 v[84:87], v23 offset:16384\n"
  "ds_read_b128 v[88:91], v23 offset:18432\n"
  "ds_read_b128 v[92:95], v24 offset:16384\n"
  "ds_read_b128 v[96:99], v24 offset:18432\n"
  "ds_read_b128 a[16:19], v23 offset:16384\n"
  "ds_read_b128 a[20:23], v23 offset:18432\n"
  "ds_read_b128 a[24:27], v24 offset:16384\n"
  "ds_read_b128 a[28:31], v24 offset:18432\n"
  "s_add_u32 m0, s8, 0\n"
  "s_nop 0\n"
  "buffer_load_dwordx4 v9, s[4:7], s79 offen lds\n"
  "buffer_load_dwordx4 v12, s[4:7], s79 offen offset:1024 lds\n"
  "buffer_load_dwordx4 v13, s[4:7], s79 offen offset:2048 lds\n"
  "buffer_load_dwordx4 v14, s[4:7], s79 offen offset:3072 lds\n"
  "v_dot2c_f32_bf16 v100, v68, v68\n"
  "v_dot2c_f32_bf16 v102, s49, v68\n"
  "v_dot2c_f32_bf16 v101, v72, v72\n"
  "v_dot2c_f32_bf16 v103, s49, v72\n"
  "v_dot2c_f32_bf16 v104, v69, v69\n"
  "v_dot2c_f32_bf16 v106, s49, v69\n"
  "v_dot2c_f32_bf16 v105, v73, v73\n"
  "v_dot2c_f32_bf16 v107, s49, v73\n"
  "v_dot2c_f32_bf16 v100, v70, v70\n"
  "v_dot2c_f32_bf16 v102, s49, v70\n"
  "v_dot2c_f32_bf16 v101, v74, v74\n"
  "v_dot2c_f32_bf16 v103, s49, v74\n"
  "v_dot2c_f32_bf16 v104, v71, v71\n"
  "v_dot2c_f32_bf16 v106, s49, v71\n"
  "v_dot2c_f32_bf16 v105, v75, v75\n"
  "v_dot2c_f32_bf16 v107, s49, v75\n"
  "v_dot2c_f32_bf16 v100, v76, v76\n"
  "v_dot2c_f32_bf16 v102, s49, v76\n"
  "v_dot2c_f32_bf16 v101, v80, v80\n"
  "v_dot2c_f32_bf16 v103, s49, v80\n"
  "v_dot2c_f32_bf16 v104, v77, v77\n"
  "v_dot2c_f32_bf16 v106, s49, v77\n"
  "v_dot2c_f32_bf16 v105, v81, v81\n"
  "v_dot2c_f32_bf16 v107, s49, v81\n"
  "v_dot2c_f32_bf16 v100, v78, v78\n"
  "v_dot2c_f32_bf16 v102, s49, v78\n"
  "v_dot2c_f32_bf16 v101, v82, v82\n"
  "v_dot2c_f32_bf16 v103, s49, v82\n"
  "v_dot2c_f32_bf16 v104, v79, v79\n"
  "v_dot2c_f32_bf16 v106, s49, v79\n"
  "v_dot2c_f32_bf16 v105, v83, v83\n"
  "v_dot2c_f32_bf16 v107, s49, v83\n"
  "v_accvgpr_write_b32 a210, v122\n"
  "v_accvgpr_write_b32 a211, v123\n"
  "v_accvgpr_write_b32 a212, v124\n"
  "v_accvgpr_write_b32 a213, v125\n"
  "v_accvgpr_write_b32 a214, 0\n"
  "v_accvgpr_write_b32 a215, 0\n"
  "v_cndmask_b32 v128, 0, v128, s[58:59]\n"
  "v_cndmask_b32 v129, 0, v129, s[58:59]\n"
  "v_cndmask_b32 v130, 0, v130, s[60:61]\n"
  "v_cndmask_b32 v131, 0, v131, s[60:61]\n"
  "v_cndmask_b32 v132, 0, v132, s[88:89]\n"
  "v_cndmask_b32 v133, 0, v133, s[88:89]\n"
  "v_accvgpr_write_b32 a216, v128\n"
  "v_accvgpr_write_b32 a217, v129\n"
  "v_accvgpr_write_b32 a218, v130\n"
  "v_accvgpr_write_b32 a219, v131\n"
  "v_accvgpr_write_b32 a220, v132\n"
  "v_accvgpr_write_b32 a221, v133\n"
  "v_accvgpr_write_b32 a222, 0\n"
  "v_accvgpr_write_b32 a223, 0\n"
  "ds_read_b64_tr_b16 v[112:113], v63\n"
  "ds_read_b64_tr_b16 v[114:115], v66\n"
  "ds_read_b64_tr_b16 v[116:117], v67\n"
  "ds_read_b64_tr_b16 v[120:121], v63 offset:8\n"
  "ds_read_b64_tr_b16 v[122:123], v66 offset:8\n"
  "ds_read_b64_tr_b16 v[124:125], v67 offset:8\n"
  "v_sub_u32 v63, v63, v108\n"
  "v_sub_u32 v66, v66, v108\n"
  "v_sub_u32 v67, v67, v108\n"
  "ds_read_b64_tr_b16 v[128:129], v63 offset:32\n"
  "ds_read_b64_tr_b16 v[130:131], v66 offset:32\n"
  "ds_read_b64_tr_b16 v[132:133], v67 offset:32\n"
  "s_waitcnt lgkmcnt(8)\n"
  "v_cndmask_b32 v112, 0, v112, s[58:59]\n"
  "v_cndmask_b32 v113, 0, v113, s[58:59]\n"
  "s_waitcnt lgkmcnt(7)\n"
  "v_cndmask_b32 v114, 0, v114, s[60:61]\n"
  "v_cndmask_b32 v115, 0, v115, s[60:61]\n"
  "s_waitcnt lgkmcnt(6)\n"
  "v_cndmask_b32 v116, 0, v116, s[88:89]\n"
  "s_waitcnt vmcnt(8)\n"
  "s_waitcnt lgkmcnt(0)\n"
  "ds_read_b128 v[68:71], v23 offset:32768\n"
  "ds_read_b128 v[72:75], v23 offset:34816\n"
  "ds_read_b128 v[76:79], v24 offset:32768\n"
  "ds_read_b128 v[80:83], v24 offset:34816\n"
  "ds_read_b128 a[32:35], v23 offset:32768\n"
  "ds_read_b128 a[36:39], v23 offset:34816\n"
  "ds_read_b128 a[40:43], v24 offset:32768\n"
  "ds_read_b128 a[44:47], v24 offset:34816\n"
  "s_add_u32 m0, s8, 0x4000\n"
  "s_add_u32 s69, s79, 0x80\n"
  "buffer_load_dwordx4 v9, s[4:7], s69 offen lds\n"
  "buffer_load_dwordx4 v12, s[4:7], s69 offen offset:1024 lds\n"
  "buffer_load_dwordx4 v13, s[4:7], s69 offen offset:2048 lds\n"
  "buffer_load_dwordx4 v14, s[4:7], s69 offen offset:3072 lds\n"
  "v_dot2c_f32_bf16 v100, v84, v84\n"
  "v_dot2c_f32_bf16 v102, s49, v84\n"
  "v_dot2c_f32_bf16 v101, v88, v88\n"
  "v_dot2c_f32_bf16 v103, s49, v88\n"
  "v_dot2c_f32_bf16 v104, v85, v85\n"
  "v_dot2c_f32_bf16 v106, s49, v85\n"
  "v_dot2c_f32_bf16 v105, v89, v89\n"
  "v_dot2c_f32_bf16 v107, s49, v89\n"
  "v_dot2c_f32_bf16 v100, v86, v86\n"
  "v_dot2c_f32_bf16 v102, s49, v86\n"
  "v_dot2c_f32_bf16 v101, v90, v90\n"
  "v_dot2c_f32_bf16 v103, s49, v90\n"
  "v_dot2c_f32_bf16 v104, v87, v87\n"
  "v_dot2c_f32_bf16 v106, s49, v87\n"
  "v_dot2c_f32_bf16 v105, v91, v91\n"
  "v_dot2c_f32_bf16 v107, s49, v91\n"
  "v_dot2c_f32_bf16 v100, v92, v92\n"
  "v_dot2c_f32_bf16 v102, s49, v92\n"
  "v_dot2c_f32_bf16 v101, v96, v96\n"
  "v_dot2c_f32_bf16 v103, s49, v96\n"
  "v_dot2c_f32_bf16 v104, v93, v93\n"
  "v_dot2c_f32_bf16 v106, s49, v93\n"
  "v_dot2c_f32_bf16 v105, v97, v97\n"
  "v_dot2c_f32_bf16 v107, s49, v97\n"
  "v_dot2c_f32_bf16 v100, v94, v94\n"
  "v_dot2c_f32_bf16 v102, s49, v94\n"
  "v_dot2c_f32_bf16 v101, v98, v98\n"
  "v_dot2c_f32_bf16 v103, s49, v98\n"
  "v_dot2c_f32_bf16 v104, v95, v95\n"
  "v_dot2c_f32_bf16 v106, s49, v95\n"
  "v_dot2c_f32_bf16 v105, v99, v99\n"
  "v_dot2c_f32_bf16 v107, s49, v99\n"
  "v_cndmask_b32 v117, 0, v117, s[88:89]\n"
  "v_accvgpr_write_b32 a224, v112\n"
  "v_accvgpr_write_b32 a225, v113\n"
  "v_accvgpr_write_b32 a226, v114\n"
  "v_accvgpr_write_b32 a227, v115\n"
  "v_accvgpr_write_b32 a228, v116\n"
  "v_accvgpr_write_b32 a229, v117\n"
  "v_accvgpr_write_b32 a230, 0\n"
  "v_accvgpr_write_b32 a231, 0\n"
  "v_cndmask_b32 v120, 0, v120, s[58:59]\n"
  "v_cndmask_b32 v121, 0, v121, s[58:59]\n"
  "v_cndmask_b32 v122, 0, v122, s[60:61]\n"
  "v_cndmask_b32 v123, 0, v123, s[60:61]\n"
  "v_cndmask_b32 v124, 0, v124, s[88:89]\n"
  "v_cndmask_b32 v125, 0, v125, s[88:89]\n"
  "v_accvgpr_write_b32 a232, v120\n"
  "v_accvgpr_write_b32 a233, v121\n"
  "v_accvgpr_write_b32 a234, v122\n"
  "v_accvgpr_write_b32 a235, v123\n"
  "v_accvgpr_write_b32 a236, v124\n"
  "v_accvgpr_write_b32 a237, v125\n"
  "v_accvgpr_write_b32 a238, 0\n"
  "v_accvgpr_write_b32 a239, 0\n"
  "v_cndmask_b32 v128, 0, v128, s[58:59]\n"
  "v_cndmask_b32 v129, 0, v129, s[58:59]\n"
  "v_cndmask_b32 v130, 0, v130, s[60:61]\n"
  "v_cndmask_b32 v131, 0, v131, s[60:61]\n"
  "v_cndmask_b32 v132, 0, v132, s[88:89]\n"
  "v_cndmask_b32 v133, 0, v133, s[88:89]\n"
  "v_accvgpr_write_b32 a240, v128\n"
  "v_accvgpr_write_b32 a241, v129\n"
  "v_accvgpr_write_b32 a242, v130\n"
  "v_accvgpr_write_b32 a243, v131\n"
  "v_accvgpr_write_b32 a244, v132\n"
  "v_accvgpr_write_b32 a245, v133\n"
  "v_accvgpr_write_b32 a246, 0\n"
  "v_accvgpr_write_b32 a247, 0\n"
  "s_waitcnt vmcnt(8)\n"
  "s_waitcnt lgkmcnt(0)\n"
  "ds_read_b128 v[84:87], v25\n"
  "ds_read_b128 v[88:91], v25 offset:2048\n"
  "ds_read_b128 v[92:95], v26\n"
  "ds_read_b128 v[96:99], v26 offset:2048\n"
  "ds_read_b128 a[48:51], v25\n"
  "ds_read_b128 a[52:55], v25 offset:2048\n"
  "ds_read_b128 a[56:59], v26\n"
  "ds_read_b128 a[60:63], v26 offset:2048\n"
  "s_add_u32 m0, s8, 0x8000\n"
  "s_nop 0\n"
  "buffer_load_dwordx4 v9, s[4:7], s80 offen lds\n"
  "buffer_load_dwordx4 v12, s[4:7], s80 offen offset:1024 lds\n"
  "buffer_load_dwordx4 v13, s[4:7], s80 offen offset:2048 lds\n"
  "buffer_load_dwordx4 v14, s[4:7], s80 offen offset:3072 lds\n"
  "v_dot2c_f32_bf16 v100, v68, v68\n"
  "v_dot2c_f32_bf16 v102, s49, v68\n"
  "v_dot2c_f32_bf16 v101, v72, v72\n"
  "v_dot2c_f32_bf16 v103, s49, v72\n"
  "v_dot2c_f32_bf16 v104, v69, v69\n"
  "v_dot2c_f32_bf16 v106, s49, v69\n"
  "v_dot2c_f32_bf16 v105, v73, v73\n"
  "v_dot2c_f32_bf16 v107, s49, v73\n"
  "v_dot2c_f32_bf16 v100, v70, v70\n"
  "v_dot2c_f32_bf16 v102, s49, v70\n"
  "v_dot2c_f32_bf16 v101, v74, v74\n"
  "v_dot2c_f32_bf16 v103, s49, v74\n"
  "v_dot2c_f32_bf16 v104, v71, v71\n"
  "v_dot2c_f32_bf16 v106, s49, v71\n"
  "v_dot2c_f32_bf16 v105, v75, v75\n"
  "v_dot2c_f32_bf16 v107, s49, v75\n"
  "v_dot2c_f32_bf16 v100, v76, v76\n"
  "v_dot2c_f32_bf16 v102, s49, v76\n"
  "v_dot2c_f32_bf16 v101, v80, v80\n"
  "v_dot2c_f32_bf16 v103, s49, v80\n"
  "v_dot2c_f32_bf16 v104, v77, v77\n"
  "v_dot2c_f32_bf16 v106, s49, v77\n"
  "v_dot2c_f32_bf16 v105, v81, v81\n"
  "v_dot2c_f32_bf16 v107, s49, v81\n"
  "v_dot2c_f32_bf16 v100, v78, v78\n"
  "v_dot2c_f32_bf16 v102, s49, v78\n"
  "v_dot2c_f32_bf16 v101, v82, v82\n"
  "v_dot2c_f32_bf16 v103, s49, v82\n"
  "v_dot2c_f32_bf16 v104, v79, v79\n"
  "v_dot2c_f32_bf16 v106, s49, v79\n"
  "v_dot2c_f32_bf16 v105, v83, v83\n"
  "v_dot2c_f32_bf16 v107, s49, v83\n"
  "s_waitcnt vmcnt(8)\n"
  "s_waitcnt lgkmcnt(0)\n"
  "ds_read_b128 v[68:71], v23\n"
  "ds_read_b128 v[72:75], v23 offset:2048\n"
  "ds_read_b128 v[76:79], v24\n"
  "ds_read_b128 v[80:83], v24 offset:2048\n"
  "ds_read_b128 a[64:67], v23\n"
  "ds_read_b128 a[68:71], v23 offset:2048\n"
  "ds_read_b128 a[72:75], v24\n"
  "ds_read_b128 a[76:79], v24 offset:2048\n"
  "s_add_u32 m0, s8, 0x21800\n"
  "s_add_u32 s69, s80, 0x80\n"
  "buffer_load_dwordx4 v9, s[4:7], s69 offen lds\n"
  "buffer_load_dwordx4 v12, s[4:7], s69 offen offset:1024 lds\n"
  "buffer_load_dwordx4 v13, s[4:7], s69 offen offset:2048 lds\n"
  "buffer_load_dwordx4 v14, s[4:7], s69 offen offset:3072 lds\n"
  "buffer_load_dwordx2 v[64:65], v62, s[12:15], s48 offen sc1\n"
  "v_dot2c_f32_bf16 v100, v84, v84\n"
  "v_dot2c_f32_bf16 v102, s49, v84\n"
  "v_dot2c_f32_bf16 v101, v88, v88\n"
  "v_dot2c_f32_bf16 v103, s49, v88\n"
  "v_dot2c_f32_bf16 v104, v85, v85\n"
  "v_dot2c_f32_bf16 v106, s49, v85\n"
  "v_dot2c_f32_bf16 v105, v89, v89\n"
  "v_dot2c_f32_bf16 v107, s49, v89\n"
  "v_dot2c_f32_bf16 v100, v86, v86\n"
  "v_dot2c_f32_bf16 v102, s49, v86\n"
  "v_dot2c_f32_bf16 v101, v90, v90\n"
  "v_dot2c_f32_bf16 v103, s49, v90\n"
  "v_dot2c_f32_bf16 v104, v87, v87\n"
  "v_dot2c_f32_bf16 v106, s49, v87\n"
  "v_dot2c_f32_bf16 v105, v91, v91\n"
  "v_dot2c_f32_bf16 v107, s49, v91\n"
  "v_dot2c_f32_bf16 v100, v92, v92\n"
  "v_dot2c_f32_bf16 v102, s49, v92\n"
  "v_dot2c_f32_bf16 v101, v96, v96\n"
  "v_dot2c_f32_bf16 v103, s49, v96\n"
  "v_dot2c_f32_bf16 v104, v93, v93\n"
  "v_dot2c_f32_bf16 v106, s49, v93\n"
  "v_dot2c_f32_bf16 v105, v97, v97\n"
  "v_dot2c_f32_bf16 v107, s49, v97\n"
  "v_dot2c_f32_bf16 v100, v94, v94\n"
  "v_dot2c_f32_bf16 v102, s49, v94\n"
  "v_dot2c_f32_bf16 v101, v98, v98\n"
  "v_dot2c_f32_bf16 v103, s49, v98\n"
  "v_dot2c_f32_bf16 v104, v95, v95\n"
  "v_dot2c_f32_bf16 v106, s49, v95\n"
  "v_dot2c_f32_bf16 v105, v99, v99\n"
  "v_dot2c_f32_bf16 v107, s49, v99\n"
  "s_waitcnt vmcnt(9)\n"
  "s_waitcnt lgkmcnt(0)\n"
  "ds_read_b128 v[84:87], v23 offset:16384\n"
  "ds_read_b128 v[88:91], v23 offset:18432\n"
  "ds_read_b128 v[92:95], v24 offset:16384\n"
  "ds_read_b128 v[96:99], v24 offset:18432\n"
  "ds_read_b128 a[80:83], v23 offset:16384\n"
  "ds_read_b128 a[84:87], v23 offset:18432\n"
  "ds_read_b128 a[88:91], v24 offset:16384\n"
  "ds_read_b128 a[92:95], v24 offset:18432\n"
  "s_add_u32 m0, s8, 0\n"
  "s_nop 0\n"
  "buffer_load_dwordx4 v9, s[4:7], s81 offen lds\n"
  "buffer_load_dwordx4 v12, s[4:7], s81 offen offset:1024 lds\n"
  "buffer_load_dwordx4 v13, s[4:7], s81 offen offset:2048 lds\n"
  "buffer_load_dwordx4 v14, s[4:7], s81 offen offset:3072 lds\n"
  "v_dot2c_f32_bf16 v100, v68, v68\n"
  "v_dot2c_f32_bf16 v102, s49, v68\n"
  "v_dot2c_f32_bf16 v101, v72, v72\n"
  "v_dot2c_f32_bf16 v103, s49, v72\n"
  "v_dot2c_f32_bf16 v104, v69, v69\n"
  "v_dot2c_f32_bf16 v106, s49, v69\n"
  "v_dot2c_f32_bf16 v105, v73, v73\n"
  "v_dot2c_f32_bf16 v107, s49, v73\n"
  "v_dot2c_f32_bf16 v100, v70, v70\n"
  "v_dot2c_f32_bf16 v102, s49, v70\n"
  "v_dot2c_f32_bf16 v101, v74, v74\n"
  "v_dot2c_f32_bf16 v103, s49, v74\n"
  "v_dot2c_f32_bf16 v104, v71, v71\n"
  "v_dot2c_f32_bf16 v106, s49, v71\n"
  "v_dot2c_f32_bf16 v105, v75, v75\n"
  "v_dot2c_f32_bf16 v107, s49, v75\n"
  "v_dot2c_f32_bf16 v100, v76, v76\n"
  "v_dot2c_f32_bf16 v102, s49, v76\n"
  "v_dot2c_f32_bf16 v101, v80, v80\n"
  "v_dot2c_f32_bf16 v103, s49, v80\n"
  "v_dot2c_f32_bf16 v104, v77, v77\n"
  "v_dot2c_f32_bf16 v106, s49, v77\n"
  "v_dot2c_f32_bf16 v105, v81, v81\n"
  "v_dot2c_f32_bf16 v107, s49, v81\n"
  "v_dot2c_f32_bf16 v100, v78, v78\n"
  "v_dot2c_f32_bf16 v102, s49, v78\n"
  "v_dot2c_f32_bf16 v101, v82, v82\n"
  "v_dot2c_f32_bf16 v103, s49, v82\n"
  "v_dot2c_f32_bf16 v104, v79, v79\n"
  "v_dot2c_f32_bf16 v106, s49, v79\n"
  "v_dot2c_f32_bf16 v105, v83, v83\n"
  "v_dot2c_f32_bf16 v107, s49, v83\n"
  "s_waitcnt vmcnt(9)\n"
  "s_waitcnt lgkmcnt(0)\n"
  "ds_read_b128 v[68:71], v23 offset:32768\n"
  "ds_read_b128 v[72:75], v23 offset:34816\n"
  "ds_read_b128 v[76:79], v24 offset:32768\n"
  "ds_read_b128 v[80:83], v24 offset:34816\n"
  "ds_read_b128 a[96:99], v23 offset:32768\n"
  "ds_read_b128 a[100:103], v23 offset:34816\n"
  "ds_read_b128 a[104:107], v24 offset:32768\n"
  "ds_read_b128 a[108:111], v24 offset:34816\n"
  "s_add_u32 m0, s8, 0x4000\n"
  "s_add_u32 s69, s81, 0x80\n"
  "buffer_load_dwordx4 v9, s[4:7], s69 offen lds\n"
  "buffer_load_dwordx4 v12, s[4:7], s69 offen offset:1024 lds\n"
  "buffer_load_dwordx4 v13, s[4:7], s69 offen offset:2048 lds\n"
  "buffer_load_dwordx4 v14, s[4:7], s69 offen offset:3072 lds\n"
  "s_waitcnt vmcnt(8)\n"
  "v_cmp_ne_u64 vcc, s[72:73], v[64:65]\n"
  "s_nop 3\n"
  "s_cbranch_vccz .Lhh_done_4x\n"
  "s_mov_b32 s63, 0x10000\n"
  ".Lhh_poll_3:\n"
  "buffer_load_dwordx2 v[64:65], v62, s[12:15], s48 offen sc1\n"
  "s_waitcnt vmcnt(0)\n"
  "v_cmp_ne_u64 vcc, s[72:73], v[64:65]\n"
  "s_nop 4\n"
  "s_cbranch_vccz .Lhh_done_4\n"
  "s_sleep 1\n"
  "s_sub_u32 s63, s63, 1\n"
  "s_cbranch_scc0 .Lhh_poll_3\n"
  ".Lhh_done_4:\n"
  ".Lhh_done_4x:\n"
  "s_lshl_b32 s70, s82, 1\n"
  "s_add_u32 s70, s70, s9\n"
  "s_add_u32 m0, s9, 0xc800\n"
  "s_add_u32 s11, s70, s10\n"
  "buffer_load_dwordx4 v21, s[12:15], s11 offen sc1 lds\n"
  "buffer_load_dwordx4 v21, s[12:15], s11 offen offset:1024 sc1 lds\n"
  "buffer_load_dwordx4 v21, s[12:15], s11 offen offset:2048 sc1 lds\n"
  "buffer_load_dwordx4 v21, s[12:15], s11 offen offset:3072 sc1 lds\n"
  "s_add_u32 m0, m0, 0x1000\n"
  "s_add_u32 s11, s11, 0x1000\n"
  "buffer_load_dwordx4 v21, s[12:15], s11 offen sc1 lds\n"
  "s_lshl_b32 s67, s25, 11\n"
  "s_add_u32 m0, s67, 0x1b800\n"
  "s_lshl_b32 s68, s41, 13\n"
  "s_add_u32 s67, s67, s68\n"
  "s_add_u32 s67, s67, 0x1450000\n"
  "buffer_load_dwordx4 v21, s[12:15], s67 offen sc1 lds\n"
  "buffer_load_dwordx4 v21, s[12:15], s67 offen offset:1024 sc1 lds\n"
  "v_dot2c_f32_bf16 v100, v84, v84\n"
  "v_dot2c_f32_bf16 v102, s49, v84\n"
  "v_dot2c_f32_bf16 v101, v88, v88\n"
  "v_dot2c_f32_bf16 v103, s49, v88\n"
  "v_dot2c_f32_bf16 v104, v85, v85\n"
  "v_dot2c_f32_bf16 v106, s49, v85\n"
  "v_dot2c_f32_bf16 v105, v89, v89\n"
  "v_dot2c_f32_bf16 v107, s49, v89\n"
  "v_dot2c_f32_bf16 v100, v86, v86\n"
  "v_dot2c_f32_bf16 v102, s49, v86\n"
  "v_dot2c_f32_bf16 v101, v90, v90\n"
  "v_dot2c_f32_bf16 v103, s49, v90\n"
  "v_dot2c_f32_bf16 v104, v87, v87\n"
  "v_dot2c_f32_bf16 v106, s49, v87\n"
  "v_dot2c_f32_bf16 v105, v91, v91\n"
  "v_dot2c_f32_bf16 v107, s49, v91\n"
  "v_dot2c_f32_bf16 v100, v92, v92\n"
  "v_dot2c_f32_bf16 v102, s49, v92\n"
  "v_dot2c_f32_bf16 v101, v96, v96\n"
  "v_dot2c_f32_bf16 v103, s49, v96\n"
  "v_dot2c_f32_bf16 v104, v93, v93\n"
  "v_dot2c_f32_bf16 v106, s49, v93\n"
  "v_dot2c_f32_bf16 v105, v97, v97\n"
  "v_dot2c_f32_bf16 v107, s49, v97\n"
  "v_dot2c_f32_bf16 v100, v94, v94\n"
  "v_dot2c_f32_bf16 v102, s49, v94\n"
  "v_dot2c_f32_bf16 v101, v98, v98\n"
  "v_dot2c_f32_bf16 v103, s49, v98\n"
  "v_dot2c_f32_bf16 v104, v95, v95\n"
  "v_dot2c_f32_bf16 v106, s49, v95\n"
  "v_dot2c_f32_bf16 v105, v99, v99\n"
  "v_dot2c_f32_bf16 v107, s49, v99\n"
  "s_waitcnt lgkmcnt(0)\n"
  "ds_read_b128 v[84:87], v25\n"
  "ds_read_b128 v[88:91], v25 offset:2048\n"
  "ds_read_b128 v[92:95], v26\n"
  "ds_read_b128 v[96:99], v26 offset:2048\n"
  "ds_read_b128 a[112:115], v25\n"
  "ds_read_b128 a[116:119], v25 offset:2048\n"
  "ds_read_b128 a[120:123], v26\n"
  "ds_read_b128 a[124:127], v26 offset:2048\n"
  "v_dot2c_f32_bf16 v100, v68, v68\n"
  "v_dot2c_f32_bf16 v102, s49, v68\n"
  "v_dot2c_f32_bf16 v101, v72, v72\n"
  "v_dot2c_f32_bf16 v103, s49, v72\n"
  "v_dot2c_f32_bf16 v104, v69, v69\n"
  "v_dot2c_f32_bf16 v106, s49, v69\n"
  "v_dot2c_f32_bf16 v105, v73, v73\n"
  "v_dot2c_f32_bf16 v107, s49, v73\n"
  "v_dot2c_f32_bf16 v100, v70, v70\n"
  "v_dot2c_f32_bf16 v102, s49, v70\n"
  "v_dot2c_f32_bf16 v101, v74, v74\n"
  "v_dot2c_f32_bf16 v103, s49, v74\n"
  "v_dot2c_f32_bf16 v104, v71, v71\n"
  "v_dot2c_f32_bf16 v106, s49, v71\n"
  "v_dot2c_f32_bf16 v105, v75, v75\n"
  "v_dot2c_f32_bf16 v107, s49, v75\n"
  "v_dot2c_f32_bf16 v100, v76, v76\n"
  "v_dot2c_f32_bf16 v102, s49, v76\n"
  "v_dot2c_f32_bf16 v101, v80, v80\n"
  "v_dot2c_f32_bf16 v103, s49, v80\n"
  "v_dot2c_f32_bf16 v104, v77, v77\n"
  "v_dot2c_f32_bf16 v106, s49, v77\n"
  "v_dot2c_f32_bf16 v105, v81, v81\n"
  "v_dot2c_f32_bf16 v107, s49, v81\n"
  "v_dot2c_f32_bf16 v100, v78, v78\n"
  "v_dot2c_f32_bf16 v102, s49, v78\n"
  "v_dot2c_f32_bf16 v101, v82, v82\n"
  "v_dot2c_f32_bf16 v103, s49, v82\n"
  "v_dot2c_f32_bf16 v104, v79, v79\n"
  "v_dot2c_f32_bf16 v106, s49, v79\n"
  "v_dot2c_f32_bf16 v105, v83, v83\n"
  "v_dot2c_f32_bf16 v107, s49, v83\n"
  "s_waitcnt vmcnt(11)\n"
  "s_waitcnt lgkmcnt(0)\n"
  "ds_read_b128 v[68:71], v23\n"
  "ds_read_b128 v[72:75], v23 offset:2048\n"
  "ds_read_b128 v[76:79], v24\n"
  "ds_read_b128 v[80:83], v24 offset:2048\n"
  "ds_read_b128 a[128:131], v23\n"
  "ds_read_b128 a[132:135], v23 offset:2048\n"
  "ds_read_b128 a[136:139], v24\n"
  "ds_read_b128 a[140:143], v24 offset:2048\n"
  "s_lshl_b32 s70, s83, 1\n"
  "s_add_u32 s70, s70, s9\n"
  "s_add_u32 m0, s9, 0x11800\n"
  "s_add_u32 s11, s70, s10\n"
  "buffer_load_dwordx4 v21, s[12:15], s11 offen sc1 lds\n"
  "buffer_load_dwordx4 v21, s[12:15], s11 offen offset:1024 sc1 lds\n"
  "buffer_load_dwordx4 v21, s[12:15], s11 offen offset:2048 sc1 lds\n"
  "buffer_load_dwordx4 v21, s[12:15], s11 offen offset:3072 sc1 lds\n"
  "s_add_u32 m0, m0, 0x1000\n"
  "s_add_u32 s11, s11, 0x1000\n"
  "buffer_load_dwordx4 v21, s[12:15], s11 offen sc1 lds\n"
  "v_dot2c_f32_bf16 v100, v84, v84\n"
  "v_dot2c_f32_bf16 v102, s49, v84\n"
  "v_dot2c_f32_bf16 v101, v88, v88\n"
  "v_dot2c_f32_bf16 v103, s49, v88\n"
  "v_dot2c_f32_bf16 v104, v85, v85\n"
  "v_dot2c_f32_bf16 v106, s49, v85\n"
  "v_dot2c_f32_bf16 v105, v89, v89\n"
  "v_dot2c_f32_bf16 v107, s49, v89\n"
  "v_dot2c_f32_bf16 v100, v86, v86\n"
  "v_dot2c_f32_bf16 v102, s49, v86\n"
  "v_dot2c_f32_bf16 v101, v90, v90\n"
  "v_dot2c_f32_bf16 v103, s49, v90\n"
  "v_dot2c_f32_bf16 v104, v87, v87\n"
  "v_dot2c_f32_bf16 v106, s49, v87\n"
  "v_dot2c_f32_bf16 v105, v91, v91\n"
  "v_dot2c_f32_bf16 v107, s49, v91\n"
  "v_dot2c_f32_bf16 v100, v92, v92\n"
  "v_dot2c_f32_bf16 v102, s49, v92\n"
  "v_dot2c_f32_bf16 v101, v96, v96\n"
  "v_dot2c_f32_bf16 v103, s49, v96\n"
  "v_dot2c_f32_bf16 v104, v93, v93\n"
  "v_dot2c_f32_bf16 v106, s49, v93\n"
  "v_dot2c_f32_bf16 v105, v97, v97\n"
  "v_dot2c_f32_bf16 v107, s49, v97\n"
  "v_dot2c_f32_bf16 v100, v94, v94\n"
  "v_dot2c_f32_bf16 v102, s49, v94\n"
  "v_dot2c_f32_bf16 v101, v98, v98\n"
  "v_dot2c_f32_bf16 v103, s49, v98\n"
  "v_dot2c_f32_bf16 v104, v95, v95\n"
  "v_dot2c_f32_bf16 v106, s49, v95\n"
  "v_dot2c_f32_bf16 v105, v99, v99\n"
  "v_dot2c_f32_bf16 v107, s49, v99\n"
  "s_waitcnt vmcnt(12)\n"
  "s_waitcnt lgkmcnt(0)\n"
  "ds_read_b128 v[84:87], v23 offset:16384\n"
  "ds_read_b128 v[88:91], v23 offset:18432\n"
  "ds_read_b128 v[92:95], v24 offset:16384\n"
  "ds_read_b128 v[96:99], v24 offset:18432\n"
  "ds_read_b128 a[144:147], v23 offset:16384\n"
  "ds_read_b128 a[148:151], v23 offset:18432\n"
  "ds_read_b128 a[152:155], v24 offset:16384\n"
  "ds_read_b128 a[156:159], v24 offset:18432\n"
  "v_dot2c_f32_bf16 v100, v68, v68\n"
  "v_dot2c_f32_bf16 v102, s49, v68\n"
  "v_dot2c_f32_bf16 v101, v72, v72\n"
  "v_dot2c_f32_bf16 v103, s49, v72\n"
  "v_dot2c_f32_bf16 v104, v69, v69\n"
  "v_dot2c_f32_bf16 v106, s49, v69\n"
  "v_dot2c_f32_bf16 v105, v73, v73\n"
  "v_dot2c_f32_bf16 v107, s49, v73\n"
  "v_dot2c_f32_bf16 v100, v70, v70\n"
  "v_dot2c_f32_bf16 v102, s49, v70\n"
  "v_dot2c_f32_bf16 v101, v74, v74\n"
  "v_dot2c_f32_bf16 v103, s49, v74\n"
  "v_dot2c_f32_bf16 v104, v71, v71\n"
  "v_dot2c_f32_bf16 v106, s49, v71\n"
  "v_dot2c_f32_bf16 v105, v75, v75\n"
  "v_dot2c_f32_bf16 v107, s49, v75\n"
  "v_dot2c_f32_bf16 v100, v76, v76\n"
  "v_dot2c_f32_bf16 v102, s49, v76\n"
  "v_dot2c_f32_bf16 v101, v80, v80\n"
  "v_dot2c_f32_bf16 v103, s49, v80\n"
  "v_dot2c_f32_bf16 v104, v77, v77\n"
  "v_dot2c_f32_bf16 v106, s49, v77\n"
  "v_dot2c_f32_bf16 v105, v81, v81\n"
  "v_dot2c_f32_bf16 v107, s49, v81\n"
  "v_dot2c_f32_bf16 v100, v78, v78\n"
  "v_dot2c_f32_bf16 v102, s49, v78\n"
  "v_dot2c_f32_bf16 v101, v82, v82\n"
  "v_dot2c_f32_bf16 v103, s49, v82\n"
  "v_dot2c_f32_bf16 v104, v79, v79\n"
  "v_dot2c_f32_bf16 v106, s49, v79\n"
  "v_dot2c_f32_bf16 v105, v83, v83\n"
  "v_dot2c_f32_bf16 v107, s49, v83\n"
  "s_waitcnt lgkmcnt(7)\n"
  "v_dot2c_f32_bf16 v100, v84, v84\n"
  "v_dot2c_f32_bf16 v102, s49, v84\n"
  "s_waitcnt lgkmcnt(6)\n"
  "v_dot2c_f32_bf16 v101, v88, v88\n"
  "v_dot2c_f32_bf16 v103, s49, v88\n"
  "v_dot2c_f32_bf16 v104, v85, v85\n"
  "v_dot2c_f32_bf16 v106, s49, v85\n"
  "v_dot2c_f32_bf16 v105, v89, v89\n"
  "v_dot2c_f32_bf16 v107, s49, v89\n"
  "v_dot2c_f32_bf16 v100, v86, v86\n"
  "v_dot2c_f32_bf16 v102, s49, v86\n"
  "v_dot2c_f32_bf16 v101, v90, v90\n"
  "v_dot2c_f32_bf16 v103, s49, v90\n"
  "v_dot2c_f32_bf16 v104, v87, v87\n"
  "v_dot2c_f32_bf16 v106, s49, v87\n"
  "v_dot2c_f32_bf16 v105, v91, v91\n"
  "v_dot2c_f32_bf16 v107, s49, v91\n"
  "s_waitcnt lgkmcnt(5)\n"
  "v_dot2c_f32_bf16 v100, v92, v92\n"
  "v_dot2c_f32_bf16 v102, s49, v92\n"
  "s_waitcnt lgkmcnt(4)\n"
  "v_dot2c_f32_bf16 v101, v96, v96\n"
  "v_dot2c_f32_bf16 v103, s49, v96\n"
  "v_dot2c_f32_bf16 v104, v93, v93\n"
  "v_dot2c_f32_bf16 v106, s49, v93\n"
  "v_dot2c_f32_bf16 v105, v97, v97\n"
  "v_dot2c_f32_bf16 v107, s49, v97\n"
  "v_dot2c_f32_bf16 v100, v94, v94\n"
  "v_dot2c_f32_bf16 v102, s49, v94\n"
  "v_dot2c_f32_bf16 v101, v98, v98\n"
  "v_dot2c_f32_bf16 v103, s49, v98\n"
  "v_dot2c_f32_bf16 v104, v95, v95\n"
  "v_dot2c_f32_bf16 v106, s49, v95\n"
  "v_dot2c_f32_bf16 v105, v99, v99\n"
  "v_dot2c_f32_bf16 v107, s49, v99\n"
  "v_add_f32 v100, v100, v104\n"
  "s_nop 0\n"
  "v_add_f32 v101, v101, v105\n"
  "v_add_f32 v102, v102, v106\n"
  "v_add_f32 v103, v103, v107\n"
  "v_mov_b32 v104, v100\n"
  "v_mov_b32 v105, v101\n"
  "v_mov_b32 v106, v102\n"
  "v_mov_b32 v107, v103\n"
  "v_permlane16_swap_b32 v100, v104\n"
  "v_permlane16_swap_b32 v101, v105\n"
  "v_permlane16_swap_b32 v102, v106\n"
  "v_permlane16_swap_b32 v103, v107\n"
  "v_add_f32 v100, v100, v104\n"
  "v_add_f32 v101, v101, v105\n"
  "v_add_f32 v102, v102, v106\n"
  "v_add_f32 v103, v103, v107\n"
  "v_mov_b32 v104, v100\n"
  "v_mov_b32 v105, v101\n"
  "v_mov_b32 v106, v102\n"
  "v_mov_b32 v107, v103\n"
  "v_permlane32_swap_b32 v100, v104\n"
  "v_permlane32_swap_b32 v101, v105\n"
  "v_permlane32_swap_b32 v102, v106\n"
  "v_permlane32_swap_b32 v103, v107\n"
  "v_add_f32 v100, v100, v104\n"
  "v_add_f32 v101, v101, v105\n"
  "v_add_f32 v102, v102, v106\n"
  "v_add_f32 v103, v103, v107\n"
  "v_mul_f32 v102, 0xbacccccd, v102\n"
  "v_mul_f32 v103, 0xbacccccd, v103\n"
  "v_mul_f32 v100, 0x3acccccd, v100\n"
  "v_mul_f32 v101, 0x3acccccd, v101\n"
  "v_fma_f32 v100, -v102, v102, v100\n"
  "v_fma_f32 v101, -v103, v103, v101\n"
  "v_cvt_pk_bf16_f32 v68, v102, v102\n"
  "v_cvt_pk_bf16_f32 v72, v103, v103\n"
  "v_max_f32 v100, 0, v100\n"
  "v_max_f32 v101, 0, v101\n"
  "v_lshlrev_b32 v104, 16, v68\n"
  "v_lshlrev_b32 v105, 16, v72\n"
  "v_add_f32 v100, 0x3727c5ac, v100\n"
  "v_add_f32 v101, 0x3727c5ac, v101\n"
  "v_sub_f32 v104, v102, v104\n"
  "v_sub_f32 v105, v103, v105\n"
  "v_rsq_f32 v12, v100\n"
  "v_rsq_f32 v24, v101\n"
  "v_cvt_pk_bf16_f32 v76, v104, v104\n"
  "v_cvt_pk_bf16_f32 v80, v105, v105\n"
  "v_mov_b32 v69, v68\n"
  "v_mov_b32 v73, v72\n"
  "v_mov_b32 v70, v68\n"
  "v_mov_b32 v74, v72\n"
  "v_mov_b32 v71, v68\n"
  "v_mov_b32 v75, v72\n"
  "v_mov_b32 v77, v76\n"
  "v_mov_b32 v81, v80\n"
  "v_mov_b32 v78, v76\n"
  "v_mov_b32 v82, v80\n"
  "v_mov_b32 v79, v76\n"
  "v_mov_b32 v83, v80\n"
  "v_mul_f32 v66, 0x3fb8aa3b, v12\n"
  "v_mul_f32 v84, 0x3fb8aa3b, v24\n"
  "s_waitcnt lgkmcnt(0)\n"
  "s_waitcnt vmcnt(5)\n"
  "s_barrier\n"
  "ds_read_b128 v[128:131], v42\n"
  "ds_read_b128 v[132:135], v42 offset:1024\n"
  "ds_read_b128 v[136:139], v42 offset:2048\n"
  "ds_read_b128 v[140:143], v42 offset:3072\n"
  "ds_read_b128 v[144:147], v42 offset:4096\n"
  "s_waitcnt lgkmcnt(0)\n"
  "v_mfma_f32_16x16x32_bf16 v[88:91], v[128:131], a[0:3], 0\n"
  "ds_read_b128 v[148:151], v42 offset:5120\n"
  "v_mfma_f32_16x16x32_bf16 v[92:95], v[128:131], a[4:7], 0\n"
  "ds_read_b128 v[152:155], v42 offset:6144\n"
  "v_mfma_f32_16x16x32_bf16 v[96:99], v[132:135], a[0:3], 0\n"
  "ds_read_b128 v[156:159], v42 offset:7168\n"
  "v_mfma_f32_16x16x32_bf16 v[100:103], v[132:135], a[4:7], 0\n"
  "ds_read_b128 v[160:163], v42 offset:8192\n"
  "v_mfma_f32_16x16x32_bf16 v[104:107], v[136:139], a[0:3], 0\n"
  "ds_read_b128 v[164:167], v42 offset:9216\n"
  "v_mfma_f32_16x16x32_bf16 v[108:111], v[136:139], a[4:7], 0\n"
  "ds_read_b32 v168, v41\n"
  "s_lshl_b32 s70, s84, 1\n"
  "s_add_u32 s70, s70, s9\n"
  "ds_read_b32 v169, v41 offset:512\n"
  "v_mfma_f32_16x16x32_bf16 v[112:115], v[140:143], a[0:3], 0\n"
  "ds_read_b32 v170, v41 offset:1024\n"
  "ds_read_b32 v171, v41 offset:1536\n"
  "s_add_u32 m0, s9, 0x16800\n"
  "s_add_u32 s11, s70, s10\n"
  "buffer_load_dwordx4 v21, s[12:15], s11 offen sc1 lds\n"
  "v_mfma_f32_16x16x32_bf16 v[116:119], v[140:143], a[4:7], 0\n"
  "ds_read_b32 v172, v41 offset:64\n"
  "ds_read_b32 v173, v41 offset:576\n"
  "ds_read_b32 v174, v41 offset:1088\n"
  "v_mfma_f32_16x16x32_bf16 v[120:123], v[144:147], a[0:3], 0\n"
  "ds_read_b32 v175, v41 offset:1600\n"
  "buffer_load_dwordx4 v21, s[12:15], s11 offen offset:1024 sc1 lds\n"
  "ds_read_b32 v176, v41 offset:128\n"
  "v_mfma_f32_16x16x32_bf16 v[124:127], v[144:147], a[4:7], 0\n"
  "ds_read_b32 v177, v41 offset:640\n"
  "ds_read_b32 v178, v41 offset:1152\n"
  "s_waitcnt lgkmcnt(11)\n"
  "v_mfma_f32_16x16x32_bf16 v[88:91], v[148:151], a[8:11], v[88:91]\n"
  "ds_read_b128 v[128:131], v42 offset:10240\n"
  "v_mfma_f32_16x16x32_bf16 v[92:95], v[148:151], a[12:15], v[92:95]\n"
  "ds_read_b128 v[132:135], v42 offset:11264\n"
  "v_mfma_f32_16x16x32_bf16 v[96:99], v[152:155], a[8:11], v[96:99]\n"
  "ds_read_b128 v[136:139], v42 offset:12288\n"
  "v_mfma_f32_16x16x32_bf16 v[100:103], v[152:155], a[12:15], v[100:103]\n"
  "ds_read_b128 v[140:143], v42 offset:13312\n"
  "v_mfma_f32_16x16x32_bf16 v[104:107], v[156:159], a[8:11], v[104:107]\n"
  "ds_read_b128 v[144:147], v42 offset:14336\n"
  "v_mfma_f32_16x16x32_bf16 v[108:111], v[156:159], a[12:15], v[108:111]\n"
  "ds_read_b32 v179, v41 offset:1664\n"
  "buffer_load_dwordx4 v21, s[12:15], s11 offen offset:2048 sc1 lds\n"
  "ds_read_b32 v180, v41 offset:192\n"
  "v_mfma_f32_16x16x32_bf16 v[112:115], v[160:163], a[8:11], v[112:115]\n"
  "ds_read_b32 v181, v41 offset:704\n"
  "ds_read_b32 v182, v41 offset:1216\n"
  "buffer_load_dwordx4 v21, s[12:15], s11 offen offset:3072 sc1 lds\n"
  "v_mfma_f32_16x16x32_bf16 v[116:119], v[160:163], a[12:15], v[116:119]\n"
  "ds_read_b32 v183, v41 offset:1728\n"
  "ds_read_b32 v184, v41 offset:256\n"
  "ds_read_b32 v185, v41 offset:768\n"
  "v_mfma_f32_16x16x32_bf16 v[120:123], v[164:167], a[8:11], v[120:123]\n"
  "ds_read_b32 v186, v41 offset:1280\n"
  "s_add_u32 m0, m0, 0x1000\n"
  "s_add_u32 s11, s11, 0x1000\n"
  "buffer_load_dwordx4 v21, s[12:15], s11 offen sc1 lds\n"
  "ds_read_b32 v187, v41 offset:1792\n"
  "v_mfma_f32_16x16x32_bf16 v[124:127], v[164:167], a[12:15], v[124:127]\n"
  "ds_read_b32 v216, v41 offset:320\n"
  "ds_read_b32 v217, v41 offset:832\n"
  "s_waitcnt lgkmcnt(11)\n"
  "v_mfma_f32_16x16x32_bf16 v[88:91], v[128:131], a[16:19], v[88:91]\n"
  "ds_read_b128 v[148:151], v42 offset:15360\n"
  "v_mfma_f32_16x16x32_bf16 v[92:95], v[128:131], a[20:23], v[92:95]\n"
  "ds_read_b128 v[152:155], v42 offset:16384\n"
  "v_mfma_f32_16x16x32_bf16 v[96:99], v[132:135], a[16:19], v[96:99]\n"
  "ds_read_b128 v[156:159], v42 offset:17408\n"
  "v_mfma_f32_16x16x32_bf16 v[100:103], v[132:135], a[20:23], v[100:103]\n"
  "ds_read_b128 v[160:163], v42 offset:18432\n"
  "v_mfma_f32_16x16x32_bf16 v[104:107], v[136:139], a[16:19], v[104:107]\n"
  "ds_read_b128 v[164:167], v42 offset:19456\n"
  "v_mfma_f32_16x16x32_bf16 v[108:111], v[136:139], a[20:23], v[108:111]\n"
  "ds_read_b32 v218, v41 offset:1344\n"
  "ds_read_b32 v219, v41 offset:1856\n"
  "v_mfma_f32_16x16x32_bf16 v[112:115], v[140:143], a[16:19], v[112:115]\n"
  "ds_read_b32 v220, v41 offset:384\n"
  "ds_read_b32 v221, v41 offset:896\n"
  "v_mfma_f32_16x16x32_bf16 v[116:119], v[140:143], a[20:23], v[116:119]\n"
  "ds_read_b32 v222, v41 offset:1408\n"
  "ds_read_b32 v223, v41 offset:1920\n"
  "v_mfma_f32_16x16x32_bf16 v[120:123], v[144:147], a[16:19], v[120:123]\n"
  "ds_read_b32 v224, v41 offset:448\n"
  "ds_read_b32 v225, v41 offset:960\n"
  "v_mfma_f32_16x16x32_bf16 v[124:127], v[144:147], a[20:23], v[124:127]\n"
  "ds_read_b32 v226, v41 offset:1472\n"
  "ds_read_b32 v227, v41 offset:1984\n"
  "s_waitcnt lgkmcnt(0)\n"
  "s_waitcnt vmcnt(5)\n"
  "s_barrier\n"
  "v_mfma_f32_16x16x32_bf16 v[88:91], v[148:151], a[24:27], v[88:91]\n"
  "ds_read_b128 v[128:131], v42 offset:20480\n"
  "v_mfma_f32_16x16x32_bf16 v[92:95], v[148:151], a[28:31], v[92:95]\n"
  "ds_read_b128 v[132:135], v42 offset:21504\n"
  "v_mfma_f32_16x16x32_bf16 v[96:99], v[152:155], a[24:27], v[96:99]\n"
  "ds_read_b128 v[136:139], v42 offset:22528\n"
  "v_mfma_f32_16x16x32_bf16 v[100:103], v[152:155], a[28:31], v[100:103]\n"
  "ds_read_b128 v[140:143], v42 offset:23552\n"
  "v_mfma_f32_16x16x32_bf16 v[104:107], v[156:159], a[24:27], v[104:107]\n"
  "ds_read_b128 v[144:147], v42 offset:24576\n"
  "v_mfma_f32_16x16x32_bf16 v[108:111], v[156:159], a[28:31], v[108:111]\n"
  "s_lshl_b32 s70, s85, 1\n"
  "s_add_u32 s70, s70, s9\n"
  "v_mfma_f32_16x16x32_bf16 v[112:115], v[160:163], a[24:27], v[112:115]\n"
  "v_mfma_f32_16x16x32_bf16 v[116:119], v[160:163], a[28:31], v[116:119]\n"
  "s_add_u32 m0, s9, 0x1b800\n"
  "s_add_u32 s11, s70, s10\n"
  "buffer_load_dwordx4 v21, s[12:15], s11 offen sc1 lds\n"
  "v_mfma_f32_16x16x32_bf16 v[120:123], v[164:167], a[24:27], v[120:123]\n"
  "v_mfma_f32_16x16x32_bf16 v[124:127], v[164:167], a[28:31], v[124:127]\n"
  "s_waitcnt lgkmcnt(0)\n"
  "v_mfma_f32_16x16x32_bf16 v[88:91], v[128:131], a[32:35], v[88:91]\n"
  "ds_read_b128 v[148:151], v42 offset:25600\n"
  "v_mfma_f32_16x16x32_bf16 v[92:95], v[128:131], a[36:39], v[92:95]\n"
  "ds_read_b128 v[152:155], v42 offset:26624\n"
  "v_mfma_f32_16x16x32_bf16 v[96:99], v[132:135], a[32:35], v[96:99]\n"
  "ds_read_b128 v[156:159], v42 offset:27648\n"
  "v_mfma_f32_16x16x32_bf16 v[100:103], v[132:135], a[36:39], v[100:103]\n"
  "ds_read_b128 v[160:163], v42 offset:28672\n"
  "v_mfma_f32_16x16x32_bf16 v[104:107], v[136:139], a[32:35], v[104:107]\n"
  "ds_read_b128 v[164:167], v42 offset:29696\n"
  "v_mfma_f32_16x16x32_bf16 v[108:111], v[136:139], a[36:39], v[108:111]\n"
  "buffer_load_dwordx4 v21, s[12:15], s11 offen offset:1024 sc1 lds\n"
  "v_mfma_f32_16x16x32_bf16 v[112:115], v[140:143], a[32:35], v[112:115]\n"
  "v_mfma_f32_16x16x32_bf16 v[116:119], v[140:143], a[36:39], v[116:119]\n"
  "buffer_load_dwordx4 v21, s[12:15], s11 offen offset:2048 sc1 lds\n"
  "v_mfma_f32_16x16x32_bf16 v[120:123], v[144:147], a[32:35], v[120:123]\n"
  "v_mfma_f32_16x16x32_bf16 v[124:127], v[144:147], a[36:39], v[124:127]\n"
  "s_waitcnt lgkmcnt(0)\n"
  "v_mfma_f32_16x16x32_bf16 v[88:91], v[148:151], a[40:43], v[88:91]\n"
  "ds_read_b128 v[128:131], v42 offset:30720\n"
  "v_mfma_f32_16x16x32_bf16 v[92:95], v[148:151], a[44:47], v[92:95]\n"
  "ds_read_b128 v[132:135], v42 offset:31744\n"
  "v_mfma_f32_16x16x32_bf16 v[96:99], v[152:155], a[40:43], v[96:99]\n"
  "ds_read_b128 v[136:139], v42 offset:32768\n"
  "v_mfma_f32_16x16x32_bf16 v[100:103], v[152:155], a[44:47], v[100:103]\n"
  "ds_read_b128 v[140:143], v42 offset:33792\n"
  "v_mfma_f32_16x16x32_bf16 v[104:107], v[156:159], a[40:43], v[104:107]\n"
  "ds_read_b128 v[144:147], v42 offset:34816\n"
  "v_mfma_f32_16x16x32_bf16 v[108:111], v[156:159], a[44:47], v[108:111]\n"
  "buffer_load_dwordx4 v21, s[12:15], s11 offen offset:3072 sc1 lds\n"
  "v_mfma_f32_16x16x32_bf16 v[112:115], v[160:163], a[40:43], v[112:115]\n"
  "s_add_u32 m0, m0, 0x1000\n"
  "s_add_u32 s11, s11, 0x1000\n"
  "buffer_load_dwordx4 v21, s[12:15], s11 offen sc1 lds\n"
  "v_mfma_f32_16x16x32_bf16 v[116:119], v[160:163], a[44:47], v[116:119]\n"
  "v_mfma_f32_16x16x32_bf16 v[120:123], v[164:167], a[40:43], v[120:123]\n"
  "s_lshl_b32 s70, s86, 1\n"
  "s_add_u32 s70, s70, s9\n"
  "v_mfma_f32_16x16x32_bf16 v[124:127], v[164:167], a[44:47], v[124:127]\n"
  "s_waitcnt lgkmcnt(0)\n"
  "v_mfma_f32_16x16x32_bf16 v[88:91], v[128:131], a[48:51], v[88:91]\n"
  "ds_read_b128 v[148:151], v42 offset:35840\n"
  "v_mfma_f32_16x16x32_bf16 v[92:95], v[128:131], a[52:55], v[92:95]\n"
  "ds_read_b128 v[152:155], v42 offset:36864\n"
  "v_mfma_f32_16x16x32_bf16 v[96:99], v[132:135], a[48:51], v[96:99]\n"
  "ds_read_b128 v[156:159], v42 offset:37888\n"
  "v_mfma_f32_16x16x32_bf16 v[100:103], v[132:135], a[52:55], v[100:103]\n"
  "ds_read_b128 v[160:163], v42 offset:38912\n"
  "v_mfma_f32_16x16x32_bf16 v[104:107], v[136:139], a[48:51], v[104:107]\n"
  "ds_read_b128 v[164:167], v42 offset:39936\n"
  "v_mfma_f32_16x16x32_bf16 v[108:111], v[136:139], a[52:55], v[108:111]\n"
  "s_add_u32 m0, s9, 0x20800\n"
  "s_add_u32 s11, s70, s10\n"
  "buffer_load_dwordx4 v21, s[12:15], s11 offen sc1 lds\n"
  "v_mfma_f32_16x16x32_bf16 v[112:115], v[140:143], a[48:51], v[112:115]\n"
  "v_mfma_f32_16x16x32_bf16 v[116:119], v[140:143], a[52:55], v[116:119]\n"
  "buffer_load_dwordx4 v21, s[12:15], s11 offen offset:1024 sc1 lds\n"
  "v_mfma_f32_16x16x32_bf16 v[120:123], v[144:147], a[48:51], v[120:123]\n"
  "v_mfma_f32_16x16x32_bf16 v[124:127], v[144:147], a[52:55], v[124:127]\n"
  "s_waitcnt vmcnt(7)\n"
  "s_barrier\n"
  "s_waitcnt lgkmcnt(0)\n"
  "v_mfma_f32_16x16x32_bf16 v[88:91], v[148:151], a[56:59], v[88:91]\n"
  "ds_read_b128 v[128:131], v42 offset:40960\n"
  "v_mfma_f32_16x16x32_bf16 v[92:95], v[148:151], a[60:63], v[92:95]\n"
  "ds_read_b128 v[132:135], v42 offset:41984\n"
  "v_mfma_f32_16x16x32_bf16 v[96:99], v[152:155], a[56:59], v[96:99]\n"
  "ds_read_b128 v[136:139], v42 offset:43008\n"
  "v_mfma_f32_16x16x32_bf16 v[100:103], v[152:155], a[60:63], v[100:103]\n"
  "ds_read_b128 v[140:143], v42 offset:44032\n"
  "v_mfma_f32_16x16x32_bf16 v[104:107], v[156:159], a[56:59], v[104:107]\n"
  "ds_read_b128 v[144:147], v42 offset:45056\n"
  "v_mfma_f32_16x16x32_bf16 v[108:111], v[156:159], a[60:63], v[108:111]\n"
  "buffer_load_dwordx4 v21, s[12:15], s11 offen offset:2048 sc1 lds\n"
  "v_mfma_f32_16x16x32_bf16 v[112:115], v[160:163], a[56:59], v[112:115]\n"
  "buffer_load_dwordx4 v21, s[12:15], s11 offen offset:3072 sc1 lds\n"
  "v_mfma_f32_16x16x32_bf16 v[116:119], v[160:163], a[60:63], v[116:119]\n"
  "v_mfma_f32_16x16x32_bf16 v[120:123], v[164:167], a[56:59], v[120:123]\n"
  "s_add_u32 m0, m0, 0x1000\n"
  "s_add_u32 s11, s11, 0x1000\n"
  "buffer_load_dwordx4 v21, s[12:15], s11 offen sc1 lds\n"
  "v_mfma_f32_16x16x32_bf16 v[124:127], v[164:167], a[60:63], v[124:127]\n"
  "s_waitcnt lgkmcnt(0)\n"
  "v_mfma_f32_16x16x32_bf16 v[88:91], v[128:131], a[64:67], v[88:91]\n"
  "ds_read_b128 v[148:151], v42 offset:46080\n"
  "v_mfma_f32_16x16x32_bf16 v[92:95], v[128:131], a[68:71], v[92:95]\n"
  "ds_read_b128 v[152:155], v42 offset:47104\n"
  "v_mfma_f32_16x16x32_bf16 v[96:99], v[132:135], a[64:67], v[96:99]\n"
  "ds_read_b128 v[156:159], v42 offset:48128\n"
  "v_mfma_f32_16x16x32_bf16 v[100:103], v[132:135], a[68:71], v[100:103]\n"
  "ds_read_b128 v[160:163], v42 offset:49152\n"
  "v_mfma_f32_16x16x32_bf16 v[104:107], v[136:139], a[64:67], v[104:107]\n"
  "ds_read_b128 v[164:167], v42 offset:50176\n"
  "v_mfma_f32_16x16x32_bf16 v[88:91], v[168:171], v[68:71], v[88:91]\n"
  "s_add_u32 s11, s82, s10\n"
  "s_add_u32 s11, s11, s97\n"
  "s_add_u32 s11, s11, 0x19000\n"
  "s_add_u32 m0, s97, 0\n"
  "s_nop 0\n"
  "buffer_load_dwordx4 v21, s[12:15], s11 offen sc1 lds\n"
  "v_mfma_f32_16x16x32_bf16 v[92:95], v[168:171], v[72:75], v[92:95]\n"
  "v_mfma_f32_16x16x32_bf16 v[96:99], v[172:175], v[68:71], v[96:99]\n"
  "v_mfma_f32_16x16x32_bf16 v[100:103], v[172:175], v[72:75], v[100:103]\n"
  "s_add_u32 s11, s11, 0x1000\n"
  "s_add_u32 m0, m0, 0x1000\n"
  "s_nop 0\n"
  "buffer_load_dwordx4 v21, s[12:15], s11 offen sc1 lds\n"
  "v_mfma_f32_16x16x32_bf16 v[104:107], v[176:179], v[68:71], v[104:107]\n"
  "v_mfma_f32_16x16x32_bf16 v[108:111], v[136:139], a[68:71], v[108:111]\n"
  "v_mfma_f32_16x16x32_bf16 v[112:115], v[140:143], a[64:67], v[112:115]\n"
  "s_add_u32 s11, s11, 0x1000\n"
  "s_mov_b32 m0, s37\n"
  "s_add_u32 s37, s37, s38\n"
  "buffer_load_dwordx4 v28, s[12:15], s11 offen sc1 lds\n"
  "v_mfma_f32_16x16x32_bf16 v[116:119], v[140:143], a[68:71], v[116:119]\n"
  "v_mfma_f32_16x16x32_bf16 v[120:123], v[144:147], a[64:67], v[120:123]\n"
  "v_mfma_f32_16x16x32_bf16 v[124:127], v[144:147], a[68:71], v[124:127]\n"
  "s_waitcnt lgkmcnt(0)\n"
  "v_mfma_f32_16x16x32_bf16 v[88:91], v[148:151], a[72:75], v[88:91]\n"
  "ds_read_b128 v[128:131], v43\n"
  "v_mfma_f32_16x16x32_bf16 v[92:95], v[148:151], a[76:79], v[92:95]\n"
  "ds_read_b128 v[132:135], v43 offset:1024\n"
  "v_mfma_f32_16x16x32_bf16 v[96:99], v[152:155], a[72:75], v[96:99]\n"
  "ds_read_b128 v[136:139], v43 offset:2048\n"
  "v_mfma_f32_16x16x32_bf16 v[100:103], v[152:155], a[76:79], v[100:103]\n"
  "ds_read_b128 v[140:143], v43 offset:3072\n"
  "v_mfma_f32_16x16x32_bf16 v[104:107], v[156:159], a[72:75], v[104:107]\n"
  "ds_read_b128 v[144:147], v43 offset:4096\n"
  "v_mfma_f32_16x16x32_bf16 v[108:111], v[176:179], v[72:75], v[108:111]\n"
  "s_add_u32 s11, s83, s10\n"
  "s_add_u32 s11, s11, s97\n"
  "s_add_u32 s11, s11, 0x19000\n"
  "s_add_u32 m0, s97, 0x2800\n"
  "s_nop 0\n"
  "buffer_load_dwordx4 v21, s[12:15], s11 offen sc1 lds\n"
  "v_mfma_f32_16x16x32_bf16 v[112:115], v[180:183], v[68:71], v[112:115]\n"
  "v_mfma_f32_16x16x32_bf16 v[116:119], v[180:183], v[72:75], v[116:119]\n"
  "v_mfma_f32_16x16x32_bf16 v[120:123], v[184:187], v[68:71], v[120:123]\n"
  "s_add_u32 s11, s11, 0x1000\n"
  "s_add_u32 m0, m0, 0x1000\n"
  "s_nop 0\n"
  "buffer_load_dwordx4 v21, s[12:15], s11 offen sc1 lds\n"
  "v_mfma_f32_16x16x32_bf16 v[124:127], v[184:187], v[72:75], v[124:127]\n"
  "v_mfma_f32_16x16x32_bf16 v[108:111], v[156:159], a[76:79], v[108:111]\n"
  "v_mfma_f32_16x16x32_bf16 v[112:115], v[160:163], a[72:75], v[112:115]\n"
  "s_add_u32 s11, s11, 0x1000\n"
  "s_mov_b32 m0, s37\n"
  "s_add_u32 s37, s37, s38\n"
  "buffer_load_dwordx4 v28, s[12:15], s11 offen sc1 lds\n"
  "v_mfma_f32_16x16x32_bf16 v[116:119], v[160:163], a[76:79], v[116:119]\n"
  "v_mfma_f32_16x16x32_bf16 v[120:123], v[164:167], a[72:75], v[120:123]\n"
  "v_mfma_f32_16x16x32_bf16 v[124:127], v[164:167], a[76:79], v[124:127]\n"
  "s_waitcnt lgkmcnt(0)\n"
  "v_mfma_f32_16x16x32_bf16 v[88:91], v[128:131], a[80:83], v[88:91]\n"
  "ds_read_b128 v[148:151], v43 offset:5120\n"
  "v_mfma_f32_16x16x32_bf16 v[92:95], v[128:131], a[84:87], v[92:95]\n"
  "ds_read_b128 v[152:155], v43 offset:6144\n"
  "v_mfma_f32_16x16x32_bf16 v[96:99], v[132:135], a[80:83], v[96:99]\n"
  "ds_read_b128 v[156:159], v43 offset:7168\n"
  "v_mfma_f32_16x16x32_bf16 v[100:103], v[132:135], a[84:87], v[100:103]\n"
  "ds_read_b128 v[160:163], v43 offset:8192\n"
  "v_mfma_f32_16x16x32_bf16 v[104:107], v[136:139], a[80:83], v[104:107]\n"
  "ds_read_b128 v[164:167], v43 offset:9216\n"
  "v_mfma_f32_16x16x32_bf16 v[88:91], v[168:171], v[76:79], v[88:91]\n"
  "s_add_u32 s11, s84, s10\n"
  "s_add_u32 s11, s11, s97\n"
  "s_add_u32 s11, s11, 0x19000\n"
  "s_add_u32 m0, s97, 0x5000\n"
  "s_nop 0\n"
  "buffer_load_dwordx4 v21, s[12:15], s11 offen sc1 lds\n"
  "v_mfma_f32_16x16x32_bf16 v[92:95], v[168:171], v[80:83], v[92:95]\n"
  "v_mfma_f32_16x16x32_bf16 v[96:99], v[172:175], v[76:79], v[96:99]\n"
  "v_mfma_f32_16x16x32_bf16 v[100:103], v[172:175], v[80:83], v[100:103]\n"
  "s_add_u32 s11, s11, 0x1000\n"
  "s_add_u32 m0, m0, 0x1000\n"
  "s_nop 0\n"
  "buffer_load_dwordx4 v21, s[12:15], s11 offen sc1 lds\n"
  "v_mfma_f32_16x16x32_bf16 v[104:107], v[176:179], v[76:79], v[104:107]\n"
  "v_mfma_f32_16x16x32_bf16 v[108:111], v[136:139], a[84:87], v[108:111]\n"
  "v_mfma_f32_16x16x32_bf16 v[112:115], v[140:143], a[80:83], v[112:115]\n"
  "s_add_u32 s11, s11, 0x1000\n"
  "s_mov_b32 m0, s37\n"
  "s_add_u32 s37, s37, s38\n"
  "buffer_load_dwordx4 v28, s[12:15], s11 offen sc1 lds\n"
  "v_mfma_f32_16x16x32_bf16 v[116:119], v[140:143], a[84:87], v[116:119]\n"
  "v_mfma_f32_16x16x32_bf16 v[120:123], v[144:147], a[80:83], v[120:123]\n"
  "v_mfma_f32_16x16x32_bf16 v[124:127], v[144:147], a[84:87], v[124:127]\n"
  "s_waitcnt vmcnt(14)\n"
  "s_barrier\n"
  "s_waitcnt lgkmcnt(0)\n"
  "v_mfma_f32_16x16x32_bf16 v[88:91], v[148:151], a[88:91], v[88:91]\n"
  "ds_read_b128 v[128:131], v43 offset:10240\n"
  "v_mfma_f32_16x16x32_bf16 v[92:95], v[148:151], a[92:95], v[92:95]\n"
  "ds_read_b128 v[132:135], v43 offset:11264\n"
  "v_mfma_f32_16x16x32_bf16 v[96:99], v[152:155], a[88:91], v[96:99]\n"
  "ds_read_b128 v[136:139], v43 offset:12288\n"
  "v_mfma_f32_16x16x32_bf16 v[100:103], v[152:155], a[92:95], v[100:103]\n"
  "ds_read_b128 v[140:143], v43 offset:13312\n"
  "v_mfma_f32_16x16x32_bf16 v[104:107], v[156:159], a[88:91], v[104:107]\n"
  "ds_read_b128 v[144:147], v43 offset:14336\n"
  "v_mfma_f32_16x16x32_bf16 v[108:111], v[176:179], v[80:83], v[108:111]\n"
  "s_add_u32 s11, s85, s10\n"
  "s_add_u32 s11, s11, s97\n"
  "s_add_u32 s11, s11, 0x19000\n"
  "s_add_u32 m0, s97, 0x7800\n"
  "s_nop 0\n"
  "buffer_load_dwordx4 v21, s[12:15], s11 offen sc1 lds\n"
  "v_mfma_f32_16x16x32_bf16 v[112:115], v[180:183], v[76:79], v[112:115]\n"
  "v_mfma_f32_16x16x32_bf16 v[116:119], v[180:183], v[80:83], v[116:119]\n"
  "v_mfma_f32_16x16x32_bf16 v[120:123], v[184:187], v[76:79], v[120:123]\n"
  "s_add_u32 s11, s11, 0x1000\n"
  "s_add_u32 m0, m0, 0x1000\n"
  "s_nop 0\n"
  "buffer_load_dwordx4 v21, s[12:15], s11 offen sc1 lds\n"
  "v_mfma_f32_16x16x32_bf16 v[124:127], v[184:187], v[80:83], v[124:127]\n"
  "v_mfma_f32_16x16x32_bf16 v[108:111], v[156:159], a[92:95], v[108:111]\n"
  "v_mfma_f32_16x16x32_bf16 v[112:115], v[160:163], a[88:91], v[112:115]\n"
  "s_add_u32 s11, s11, 0x1000\n"
  "s_mov_b32 m0, s37\n"
  "s_add_u32 s37, s37, s38\n"
  "buffer_load_dwordx4 v28, s[12:15], s11 offen sc1 lds\n"
  "v_mfma_f32_16x16x32_bf16 v[116:119], v[160:163], a[92:95], v[116:119]\n"
  "v_mfma_f32_16x16x32_bf16 v[120:123], v[164:167], a[88:91], v[120:123]\n"
  "v_mfma_f32_16x16x32_bf16 v[124:127], v[164:167], a[92:95], v[124:127]\n"
  "s_waitcnt lgkmcnt(0)\n"
  "v_mfma_f32_16x16x32_bf16 v[88:91], v[128:131], a[96:99], v[88:91]\n"
  "ds_read_b128 v[148:151], v43 offset:15360\n"
  "v_mfma_f32_16x16x32_bf16 v[92:95], v[128:131], a[100:103], v[92:95]\n"
  "ds_read_b128 v[152:155], v43 offset:16384\n"
  "v_mfma_f32_16x16x32_bf16 v[96:99], v[132:135], a[96:99], v[96:99]\n"
  "ds_read_b128 v[156:159], v43 offset:17408\n"
  "v_mfma_f32_16x16x32_bf16 v[100:103], v[132:135], a[100:103], v[100:103]\n"
  "ds_read_b128 v[160:163], v43 offset:18432\n"
  "v_mfma_f32_16x16x32_bf16 v[104:107], v[136:139], a[96:99], v[104:107]\n"
  "ds_read_b128 v[164:167], v43 offset:19456\n"
  "v_mfma_f32_16x16x32_bf16 v[108:111], v[136:139], a[100:103], v[108:111]\n"
  "s_add_u32 s11, s86, s10\n"
  "s_add_u32 s11, s11, s97\n"
  "s_add_u32 s11, s11, 0x19000\n"
  "s_add_u32 m0, s97, 0xa000\n"
  "s_nop 0\n"
  "buffer_load_dwordx4 v21, s[12:15], s11 offen sc1 lds\n"
  "v_mfma_f32_16x16x32_bf16 v[112:115], v[140:143], a[96:99], v[112:115]\n"
  "s_add_u32 s11, s11, 0x1000\n"
  "s_add_u32 m0, m0, 0x1000\n"
  "s_nop 0\n"
  "buffer_load_dwordx4 v21, s[12:15], s11 offen sc1 lds\n"
  "v_mfma_f32_16x16x32_bf16 v[116:119], v[140:143], a[100:103], v[116:119]\n"
  "v_mfma_f32_16x16x32_bf16 v[120:123], v[144:147], a[96:99], v[120:123]\n"
  "s_add_u32 s11, s11, 0x1000\n"
  "s_mov_b32 m0, s37\n"
  "s_add_u32 s37, s37, s38\n"
  "buffer_load_dwordx4 v28, s[12:15], s11 offen sc1 lds\n"
  "v_mfma_f32_16x16x32_bf16 v[124:127], v[144:147], a[100:103], v[124:127]\n"
  "s_waitcnt lgkmcnt(0)\n"
  "v_mfma_f32_16x16x32_bf16 v[88:91], v[148:151], a[104:107], v[88:91]\n"
  "ds_read_b128 v[128:131], v43 offset:20480\n"
  "v_mfma_f32_16x16x32_bf16 v[92:95], v[148:151], a[108:111], v[92:95]\n"
  "ds_read_b128 v[132:135], v43 offset:21504\n"
  "v_mfma_f32_16x16x32_bf16 v[96:99], v[152:155], a[104:107], v[96:99]\n"
  "ds_read_b128 v[136:139], v43 offset:22528\n"
  "v_mfma_f32_16x16x32_bf16 v[100:103], v[152:155], a[108:111], v[100:103]\n"
  "ds_read_b128 v[140:143], v43 offset:23552\n"
  "v_mfma_f32_16x16x32_bf16 v[104:107], v[156:159], a[104:107], v[104:107]\n"
  "ds_read_b128 v[144:147], v43 offset:24576\n"
  "v_mfma_f32_16x16x32_bf16 v[108:111], v[156:159], a[108:111], v[108:111]\n"
  "buffer_load_dwordx4 a[160:163], v53, s[20:23], s46 offen\n"
  "v_mfma_f32_16x16x32_bf16 v[112:115], v[160:163], a[104:107], v[112:115]\n"
  "v_mfma_f32_16x16x32_bf16 v[116:119], v[160:163], a[108:111], v[116:119]\n"
  "v_mfma_f32_16x16x32_bf16 v[120:123], v[164:167], a[104:107], v[120:123]\n"
  "v_mfma_f32_16x16x32_bf16 v[124:127], v[164:167], a[108:111], v[124:127]\n"
  "s_waitcnt lgkmcnt(0)\n"
  "v_mfma_f32_16x16x32_bf16 v[88:91], v[128:131], a[112:115], v[88:91]\n"
  "ds_read_b128 v[148:151], v43 offset:25600\n"
  "v_mfma_f32_16x16x32_bf16 v[92:95], v[128:131], a[116:119], v[92:95]\n"
  "ds_read_b128 v[152:155], v43 offset:26624\n"
  "v_mfma_f32_16x16x32_bf16 v[96:99], v[132:135], a[112:115], v[96:99]\n"
  "ds_read_b128 v[156:159], v43 offset:27648\n"
  "v_mfma_f32_16x16x32_bf16 v[100:103], v[132:135], a[116:119], v[100:103]\n"
  "ds_read_b128 v[160:163], v43 offset:28672\n"
  "v_mfma_f32_16x16x32_bf16 v[104:107], v[136:139], a[112:115], v[104:107]\n"
  "ds_read_b128 v[164:167], v43 offset:29696\n"
  "v_mfma_f32_16x16x32_bf16 v[108:111], v[136:139], a[116:119], v[108:111]\n"
  "buffer_load_dwordx4 a[164:167], v54, s[20:23], s46 offen\n"
  "v_mfma_f32_16x16x32_bf16 v[112:115], v[140:143], a[112:115], v[112:115]\n"
  "v_mfma_f32_16x16x32_bf16 v[116:119], v[140:143], a[116:119], v[116:119]\n"
  "buffer_load_dwordx4 a[168:171], v55, s[20:23], s46 offen\n"
  "v_mfma_f32_16x16x32_bf16 v[120:123], v[144:147], a[112:115], v[120:123]\n"
  "v_mfma_f32_16x16x32_bf16 v[124:127], v[144:147], a[116:119], v[124:127]\n"
  "s_waitcnt vmcnt(18)\n"
  "s_barrier\n"
  "s_waitcnt lgkmcnt(0)\n"
  "v_mfma_f32_16x16x32_bf16 v[88:91], v[148:151], a[120:123], v[88:91]\n"
  "ds_read_b128 v[128:131], v43 offset:30720\n"
  "v_mfma_f32_16x16x32_bf16 v[92:95], v[148:151], a[124:127], v[92:95]\n"
  "ds_read_b128 v[132:135], v43 offset:31744\n"
  "v_mfma_f32_16x16x32_bf16 v[96:99], v[152:155], a[120:123], v[96:99]\n"
  "ds_read_b128 v[136:139], v43 offset:32768\n"
  "v_mfma_f32_16x16x32_bf16 v[100:103], v[152:155], a[124:127], v[100:103]\n"
  "ds_read_b128 v[140:143], v43 offset:33792\n"
  "v_mfma_f32_16x16x32_bf16 v[104:107], v[156:159], a[120:123], v[104:107]\n"
  "ds_read_b128 v[144:147], v43 offset:34816\n"
  "v_mfma_f32_16x16x32_bf16 v[108:111], v[156:159], a[124:127], v[108:111]\n"
  "buffer_load_dwordx4 a[172:175], v56, s[20:23], s46 offen\n"
  "v_mfma_f32_16x16x32_bf16 v[112:115], v[160:163], a[120:123], v[112:115]\n"
  "v_mfma_f32_16x16x32_bf16 v[116:119], v[160:163], a[124:127], v[116:119]\n"
  "buffer_load_dwordx4 a[176:179], v57, s[28:31], s47 offen\n"
  "v_mfma_f32_16x16x32_bf16 v[120:123], v[164:167], a[120:123], v[120:123]\n"
  "v_mfma_f32_16x16x32_bf16 v[124:127], v[164:167], a[124:127], v[124:127]\n"
  "s_waitcnt lgkmcnt(0)\n"
  "v_mfma_f32_16x16x32_bf16 v[88:91], v[128:131], a[128:131], v[88:91]\n"
  "ds_read_b128 v[148:151], v43 offset:35840\n"
  "v_mfma_f32_16x16x32_bf16 v[92:95], v[128:131], a[132:135], v[92:95]\n"
  "ds_read_b128 v[152:155], v43 offset:36864\n"
  "v_mfma_f32_16x16x32_bf16 v[96:99], v[132:135], a[128:131], v[96:99]\n"
  "ds_read_b128 v[156:159], v43 offset:37888\n"
  "v_mfma_f32_16x16x32_bf16 v[100:103], v[132:135], a[132:135], v[100:103]\n"
  "ds_read_b128 v[160:163], v43 offset:38912\n"
  "v_mfma_f32_16x16x32_bf16 v[104:107], v[136:139], a[128:131], v[104:107]\n"
  "ds_read_b128 v[164:167], v43 offset:39936\n"
  "v_mfma_f32_16x16x32_bf16 v[108:111], v[136:139], a[132:135], v[108:111]\n"
  "buffer_load_dwordx4 a[180:183], v58, s[28:31], s47 offen\n"
  "v_mfma_f32_16x16x32_bf16 v[112:115], v[140:143], a[128:131], v[112:115]\n"
  "v_mfma_f32_16x16x32_bf16 v[116:119], v[140:143], a[132:135], v[116:119]\n"
  "buffer_load_dwordx4 a[184:187], v59, s[28:31], s47 offen\n"
  "v_mfma_f32_16x16x32_bf16 v[120:123], v[144:147], a[128:131], v[120:123]\n"
  "v_mfma_f32_16x16x32_bf16 v[124:127], v[144:147], a[132:135], v[124:127]\n"
  "s_waitcnt lgkmcnt(0)\n"
  "v_mfma_f32_16x16x32_bf16 v[88:91], v[148:151], a[136:139], v[88:91]\n"
  "ds_read_b128 v[128:131], v43 offset:40960\n"
  "v_mfma_f32_16x16x32_bf16 v[92:95], v[148:151], a[140:143], v[92:95]\n"
  "ds_read_b128 v[132:135], v43 offset:41984\n"
  "v_mfma_f32_16x16x32_bf16 v[96:99], v[152:155], a[136:139], v[96:99]\n"
  "ds_read_b128 v[136:139], v43 offset:43008\n"
  "v_mfma_f32_16x16x32_bf16 v[100:103], v[152:155], a[140:143], v[100:103]\n"
  "ds_read_b128 v[140:143], v43 offset:44032\n"
  "v_mfma_f32_16x16x32_bf16 v[104:107], v[156:159], a[136:139], v[104:107]\n"
  "ds_read_b128 v[144:147], v43 offset:45056\n"
  "v_mfma_f32_16x16x32_bf16 v[108:111], v[156:159], a[140:143], v[108:111]\n"
  "v_mfma_f32_16x16x32_bf16 v[112:115], v[160:163], a[136:139], v[112:115]\n"
  "v_mfma_f32_16x16x32_bf16 v[116:119], v[160:163], a[140:143], v[116:119]\n"
  "v_mfma_f32_16x16x32_bf16 v[120:123], v[164:167], a[136:139], v[120:123]\n"
  "v_mfma_f32_16x16x32_bf16 v[124:127], v[164:167], a[140:143], v[124:127]\n"
  "s_waitcnt lgkmcnt(0)\n"
  "v_mfma_f32_16x16x32_bf16 v[88:91], v[128:131], a[144:147], v[88:91]\n"
  "ds_read_b128 v[148:151], v43 offset:46080\n"
  "v_mfma_f32_16x16x32_bf16 v[92:95], v[128:131], a[148:151], v[92:95]\n"
  "ds_read_b128 v[152:155], v43 offset:47104\n"
  "v_mfma_f32_16x16x32_bf16 v[96:99], v[132:135], a[144:147], v[96:99]\n"
  "ds_read_b128 v[156:159], v43 offset:48128\n"
  "v_mfma_f32_16x16x32_bf16 v[100:103], v[132:135], a[148:151], v[100:103]\n"
  "ds_read_b128 v[160:163], v43 offset:49152\n"
  "v_mfma_f32_16x16x32_bf16 v[104:107], v[136:139], a[144:147], v[104:107]\n"
  "ds_read_b128 v[164:167], v43 offset:50176\n"
  "v_mfma_f32_16x16x32_bf16 v[108:111], v[136:139], a[148:151], v[108:111]\n"
  "v_mfma_f32_16x16x32_bf16 v[112:115], v[140:143], a[144:147], v[112:115]\n"
  "v_mfma_f32_16x16x32_bf16 v[116:119], v[140:143], a[148:151], v[116:119]\n"
  "v_mfma_f32_16x16x32_bf16 v[120:123], v[144:147], a[144:147], v[120:123]\n"
  "v_mfma_f32_16x16x32_bf16 v[124:127], v[144:147], a[148:151], v[124:127]\n"
  "s_waitcnt lgkmcnt(0)\n"
  "v_mfma_f32_16x16x32_bf16 v[88:91], v[148:151], a[152:155], v[88:91]\n"
  "v_mfma_f32_16x16x32_bf16 v[92:95], v[148:151], a[156:159], v[92:95]\n"
  "v_mfma_f32_16x16x32_bf16 v[96:99], v[152:155], a[152:155], v[96:99]\n"
  "v_mfma_f32_16x16x32_bf16 v[100:103], v[152:155], a[156:159], v[100:103]\n"
  "v_mfma_f32_16x16x32_bf16 v[104:107], v[156:159], a[152:155], v[104:107]\n"
  "v_mfma_f32_16x16x32_bf16 v[108:111], v[156:159], a[156:159], v[108:111]\n"
  "v_mfma_f32_16x16x32_bf16 v[112:115], v[160:163], a[152:155], v[112:115]\n"
  "v_mfma_f32_16x16x32_bf16 v[116:119], v[160:163], a[156:159], v[116:119]\n"
  "v_mfma_f32_16x16x32_bf16 v[120:123], v[164:167], a[152:155], v[120:123]\n"
  "v_mfma_f32_16x16x32_bf16 v[124:127], v[164:167], a[156:159], v[124:127]\n"
  "s_barrier\n"
  "v_mov_b32 v3, 0x3f80\n"
  "v_mov_b32 v154, 0\n"
  "v_mov_b32 v155, 0\n"
  "v_mov_b32 v162, 0\n"
  "v_mov_b32 v163, 0\n"
  "v_cvt_pk_bf16_f32 v148, v88, v89\n"
  "v_cvt_pk_bf16_f32 v149, v90, v91\n"
  "v_cvt_pk_bf16_f32 v156, v92, v93\n"
  "v_cvt_pk_bf16_f32 v157, v94, v95\n"
  "v_cvt_pk_bf16_f32 v150, v96, v97\n"
  "v_cvt_pk_bf16_f32 v151, v98, v99\n"
  "v_cvt_pk_bf16_f32 v158, v100, v101\n"
  "v_cvt_pk_bf16_f32 v159, v102, v103\n"
  "v_cvt_pk_bf16_f32 v152, v104, v105\n"
  "v_cvt_pk_bf16_f32 v153, v106, v107\n"
  "v_cvt_pk_bf16_f32 v160, v108, v109\n"
  "v_cvt_pk_bf16_f32 v161, v110, v111\n"
  "v_mfma_f32_16x16x32_bf16 v[228:231], a[200:203], v[148:151], 0\n"
  "v_mfma_f32_16x16x32_bf16 v[232:235], a[208:211], v[148:151], 0\n"
  "v_mfma_f32_16x16x32_bf16 v[236:239], a[216:219], v[148:151], 0\n"
  "v_mfma_f32_16x16x32_bf16 v[240:243], a[200:203], v[156:159], 0\n"
  "v_mfma_f32_16x16x32_bf16 v[244:247], a[208:211], v[156:159], 0\n"
  "v_mfma_f32_16x16x32_bf16 v[248:251], a[216:219], v[156:159], 0\n"
  "v_mfma_f32_16x16x32_bf16 v[228:231], a[204:207], v[152:155], v[228:231]\n"
  "v_mfma_f32_16x16x32_bf16 v[232:235], a[212:215], v[152:155], v[232:235]\n"
  "v_mfma_f32_16x16x32_bf16 v[236:239], a[220:223], v[152:155], v[236:239]\n"
  "v_mfma_f32_16x16x32_bf16 v[240:243], a[204:207], v[160:163], v[240:243]\n"
  "v_mfma_f32_16x16x32_bf16 v[244:247], a[212:215], v[160:163], v[244:247]\n"
  "v_mfma_f32_16x16x32_bf16 v[248:251], a[220:223], v[160:163], v[248:251]\n"
  "v_pk_mul_f32 v[104:105], v[104:105], v[12:13] op_sel_hi:[1,0]\n"
  "v_pk_mul_f32 v[106:107], v[106:107], v[12:13] op_sel_hi:[1,0]\n"
  "v_pk_mul_f32 v[112:113], v[112:113], v[12:13] op_sel_hi:[1,0]\n"
  "v_pk_mul_f32 v[114:115], v[114:115], v[12:13] op_sel_hi:[1,0]\n"
  "v_pk_mul_f32 v[120:121], v[120:121], v[12:13] op_sel_hi:[1,0]\n"
  "v_pk_mul_f32 v[122:123], v[122:123], v[12:13] op_sel_hi:[1,0]\n"
  "v_cvt_pk_bf16_f32 v104, v104, v105\n"
  "v_cvt_pk_bf16_f32 v105, v106, v107\n"
  "v_cvt_pk_bf16_f32 v112, v112, v113\n"
  "v_cvt_pk_bf16_f32 v113, v114, v115\n"
  "v_cvt_pk_bf16_f32 v120, v120, v121\n"
  "v_cvt_pk_bf16_f32 v121, v122, v123\n"
  "v_cndmask_b32 v104, v3, v104, s[74:75]\n"
  "v_cndmask_b32 v105, 0, v105, s[74:75]\n"
  "ds_write_b64 v46, v[112:113] offset:160\n"
  "ds_write_b64 v47, v[120:121] offset:160\n"
  "ds_write_b64 v45, v[104:105] offset:160\n"
  "v_pk_mul_f32 v[108:109], v[108:109], v[24:25] op_sel_hi:[1,0]\n"
  "v_pk_mul_f32 v[110:111], v[110:111], v[24:25] op_sel_hi:[1,0]\n"
  "v_pk_mul_f32 v[116:117], v[116:117], v[24:25] op_sel_hi:[1,0]\n"
  "v_pk_mul_f32 v[118:119], v[118:119], v[24:25] op_sel_hi:[1,0]\n"
  "v_pk_mul_f32 v[124:125], v[124:125], v[24:25] op_sel_hi:[1,0]\n"
  "v_pk_mul_f32 v[126:127], v[126:127], v[24:25] op_sel_hi:[1,0]\n"
  "v_cvt_pk_bf16_f32 v108, v108, v109\n"
  "v_cvt_pk_bf16_f32 v109, v110, v111\n"
  "v_cvt_pk_bf16_f32 v116, v116, v117\n"
  "v_cvt_pk_bf16_f32 v117, v118, v119\n"
  "v_cvt_pk_bf16_f32 v124, v124, v125\n"
  "v_cvt_pk_bf16_f32 v125, v126, v127\n"
  "v_cndmask_b32 v108, v3, v108, s[74:75]\n"
  "v_cndmask_b32 v109, 0, v109, s[74:75]\n"
  "ds_write_b64 v46, v[116:117] offset:5408\n"
  "ds_write_b64 v47, v[124:125] offset:5408\n"
  "ds_write_b64 v45, v[108:109] offset:5408\n"
  "v_max3_f32 v188, v228, v229, v230\n"
  "s_add_u32 s11, s44, 0\n"
  "s_add_u32 m0, s97, 0x19000\n"
  "s_nop 0\n"
  "buffer_load_dwordx4 v48, s[16:19], s11 offen lds\n"
  "v_max3_f32 v212, v240, v241, v242\n"
  "v_max3_f32 v189, -v228, -v229, -v230\n"
  "v_max3_f32 v213, -v240, -v241, -v242\n"
  "v_cndmask_b32 v236, v236, v238, s[76:77]\n"
  "v_cndmask_b32 v248, v248, v250, s[76:77]\n"
  "v_cndmask_b32 v237, v237, v239, s[76:77]\n"
  "v_cndmask_b32 v249, v249, v251, s[76:77]\n"
  "v_max3_f32 v188, v188, v231, v232\n"
  "v_max3_f32 v212, v212, v243, v244\n"
  "v_max3_f32 v189, v189, -v231, -v232\n"
  "v_max3_f32 v213, v213, -v243, -v244\n"
  "v_max3_f32 v188, v188, v233, v234\n"
  "v_max3_f32 v212, v212, v245, v246\n"
  "v_max3_f32 v189, v189, -v233, -v234\n"
  "v_max3_f32 v213, v213, -v245, -v246\n"
  "v_max3_f32 v188, v188, v235, v236\n"
  "v_max3_f32 v212, v212, v247, v248\n"
  "v_max3_f32 v189, v189, -v235, -v236\n"
  "v_max3_f32 v213, v213, -v247, -v248\n"
  "v_max_f32 v188, v188, v237\n"
  "s_add_u32 s11, s44, 0\n"
  "s_add_u32 m0, s97, 0x1a000\n"
  "s_nop 0\n"
  "buffer_load_dwordx4 v49, s[16:19], s11 offen lds\n"
  "v_max_f32 v212, v212, v249\n"
  "v_max3_f32 v189, v189, -v237, -v237\n"
  "v_max3_f32 v213, v213, -v249, -v249\n"
  "s_nop 0\n"
  "v_permlane16_swap_b32 v188, v189\n"
  "v_permlane16_swap_b32 v212, v213\n"
  "v_max_f32 v188, v188, v189\n"
  "v_max_f32 v212, v212, v213\n"
  "v_mul_f32 v190, -v66, v188\n"
  "v_mul_f32 v214, -v84, v212\n"
  "v_mov_b32 v188, v190\n"
  "v_mov_b32 v212, v214\n"
  "s_nop 0\n"
  "v_permlane16_swap_b32 v190, v188\n"
  "v_permlane16_swap_b32 v214, v212\n"
  "v_pk_fma_f32 v[168:169], v[228:229], v[66:67], v[190:191] op_sel_hi:[1,0,0]\n"
  "v_pk_fma_f32 v[192:193], v[240:241], v[84:85], v[214:215] op_sel_hi:[1,0,0]\n"
  "v_pk_fma_f32 v[170:171], v[230:231], v[66:67], v[190:191] op_sel_hi:[1,0,0]\n"
  "v_pk_fma_f32 v[194:195], v[242:243], v[84:85], v[214:215] op_sel_hi:[1,0,0]\n"
  "v_pk_fma_f32 v[172:173], v[232:233], v[66:67], v[190:191] op_sel_hi:[1,0,0]\n"
  "v_pk_fma_f32 v[196:197], v[244:245], v[84:85], v[214:215] op_sel_hi:[1,0,0]\n"
  "v_pk_fma_f32 v[174:175], v[234:235], v[66:67], v[190:191] op_sel_hi:[1,0,0]\n"
  "s_add_u32 s11, s44, 0\n"
  "s_add_u32 m0, s97, 0x1b000\n"
  "s_nop 0\n"
  "buffer_load_dwordx4 v50, s[16:19], s11 offen lds\n"
  "v_pk_fma_f32 v[198:199], v[246:247], v[84:85], v[214:215] op_sel_hi:[1,0,0]\n"
  "v_pk_fma_f32 v[176:177], v[236:237], v[66:67], v[190:191] op_sel_hi:[1,0,0]\n"
  "v_pk_fma_f32 v[200:201], v[248:249], v[84:85], v[214:215] op_sel_hi:[1,0,0]\n"
  "v_pk_fma_f32 v[178:179], v[228:229], v[66:67], v[188:189] op_sel_hi:[1,0,0] neg_lo:[0,1,0] neg_hi:[0,1,0]\n"
  "v_pk_fma_f32 v[202:203], v[240:241], v[84:85], v[212:213] op_sel_hi:[1,0,0] neg_lo:[0,1,0] neg_hi:[0,1,0]\n"
  "v_pk_fma_f32 v[180:181], v[230:231], v[66:67], v[188:189] op_sel_hi:[1,0,0] neg_lo:[0,1,0] neg_hi:[0,1,0]\n"
  "v_pk_fma_f32 v[204:205], v[242:243], v[84:85], v[212:213] op_sel_hi:[1,0,0] neg_lo:[0,1,0] neg_hi:[0,1,0]\n"
  "v_pk_fma_f32 v[182:183], v[232:233], v[66:67], v[188:189] op_sel_hi:[1,0,0] neg_lo:[0,1,0] neg_hi:[0,1,0]\n"
  "v_pk_fma_f32 v[206:207], v[244:245], v[84:85], v[212:213] op_sel_hi:[1,0,0] neg_lo:[0,1,0] neg_hi:[0,1,0]\n"
  "v_pk_fma_f32 v[184:185], v[234:235], v[66:67], v[188:189] op_sel_hi:[1,0,0] neg_lo:[0,1,0] neg_hi:[0,1,0]\n"
  "v_pk_fma_f32 v[208:209], v[246:247], v[84:85], v[212:213] op_sel_hi:[1,0,0] neg_lo:[0,1,0] neg_hi:[0,1,0]\n"
  "v_pk_fma_f32 v[186:187], v[236:237], v[66:67], v[188:189] op_sel_hi:[1,0,0] neg_lo:[0,1,0] neg_hi:[0,1,0]\n"
  "v_pk_fma_f32 v[210:211], v[248:249], v[84:85], v[212:213] op_sel_hi:[1,0,0] neg_lo:[0,1,0] neg_hi:[0,1,0]\n"
  "v_exp_f32 v168, v168\n"
  "v_exp_f32 v192, v192\n"
  "v_exp_f32 v169, v169\n"
  "v_exp_f32 v193, v193\n"
  "v_exp_f32 v170, v170\n"
  "v_exp_f32 v194, v194\n"
  "v_exp_f32 v171, v171\n"
  "s_add_u32 s11, s44, 0\n"
  "s_add_u32 m0, s97, 0x1c000\n"
  "s_nop 0\n"
  "buffer_load_dwordx4 v51, s[16:19], s11 offen lds\n"
  "v_exp_f32 v195, v195\n"
  "v_exp_f32 v172, v172\n"
  "v_exp_f32 v196, v196\n"
  "v_exp_f32 v173, v173\n"
  "v_exp_f32 v197, v197\n"
  "v_exp_f32 v174, v174\n"
  "v_exp_f32 v198, v198\n"
  "v_exp_f32 v175, v175\n"
  "v_exp_f32 v199, v199\n"
  "v_exp_f32 v176, v176\n"
  "v_exp_f32 v200, v200\n"
  "v_exp_f32 v177, v177\n"
  "v_exp_f32 v201, v201\n"
  "v_exp_f32 v178, v178\n"
  "v_exp_f32 v202, v202\n"
  "v_exp_f32 v179, v179\n"
  "v_exp_f32 v203, v203\n"
  "v_exp_f32 v180, v180\n"
  "v_exp_f32 v204, v204\n"
  "v_exp_f32 v181, v181\n"
  "s_add_u32 s11, s44, 0\n"
  "s_add_u32 m0, s97, 0x1d000\n"
  "s_nop 0\n"
  "buffer_load_dwordx4 v52, s[16:19], s11 offen lds\n"
  "v_exp_f32 v205, v205\n"
  "v_exp_f32 v182, v182\n"
  "v_exp_f32 v206, v206\n"
  "v_exp_f32 v183, v183\n"
  "v_exp_f32 v207, v207\n"
  "v_exp_f32 v184, v184\n"
  "v_exp_f32 v208, v208\n"
  "v_exp_f32 v185, v185\n"
  "v_exp_f32 v209, v209\n"
  "v_exp_f32 v186, v186\n"
  "v_exp_f32 v210, v210\n"
  "v_exp_f32 v187, v187\n"
  "v_exp_f32 v211, v211\n"
  "v_pk_add_f32 v[188:189], v[168:169], v[170:171]\n"
  "v_pk_add_f32 v[212:213], v[192:193], v[194:195]\n"
  "v_pk_add_f32 v[190:191], v[178:179], v[180:181]\n"
  "v_pk_add_f32 v[214:215], v[202:203], v[204:205]\n"
  "v_pk_add_f32 v[188:189], v[188:189], v[172:173]\n"
  "v_pk_add_f32 v[212:213], v[212:213], v[196:197]\n"
  "v_pk_add_f32 v[190:191], v[190:191], v[182:183]\n"
  "s_add_u32 s11, s44, 0x28000\n"
  "s_add_u32 m0, s97, 0x1e000\n"
  "s_nop 0\n"
  "buffer_load_dwordx4 v48, s[16:19], s11 offen lds\n"
  "v_pk_add_f32 v[214:215], v[214:215], v[206:207]\n"
  "v_pk_add_f32 v[188:189], v[188:189], v[174:175]\n"
  "v_pk_add_f32 v[212:213], v[212:213], v[198:199]\n"
  "v_pk_add_f32 v[190:191], v[190:191], v[184:185]\n"
  "v_pk_add_f32 v[214:215], v[214:215], v[208:209]\n"
  "v_pk_add_f32 v[188:189], v[188:189], v[176:177]\n"
  "v_pk_add_f32 v[212:213], v[212:213], v[200:201]\n"
  "v_pk_add_f32 v[190:191], v[190:191], v[186:187]\n"
  "v_pk_add_f32 v[214:215], v[214:215], v[210:211]\n"
  "v_add_f32 v188, v188, v189\n"
  "v_add_f32 v212, v212, v213\n"
  "v_add_f32 v190, v190, v191\n"
  "v_add_f32 v214, v214, v215\n"
  "s_nop 0\n"
  "v_permlane16_swap_b32 v188, v190\n"
  "v_permlane16_swap_b32 v212, v214\n"
  "v_add_f32 v188, v188, v190\n"
  "v_add_f32 v212, v212, v214\n"
  "v_rcp_f32 v188, v188\n"
  "v_rcp_f32 v212, v212\n"
  "v_mov_b32 v190, v188\n"
  "v_mov_b32 v214, v212\n"
  "s_nop 0\n"
  "v_permlane16_swap_b32 v188, v190\n"
  "v_permlane16_swap_b32 v212, v214\n"
  "v_pk_mul_f32 v[168:169], v[168:169], v[188:189] op_sel_hi:[1,0]\n"
  "v_pk_mul_f32 v[192:193], v[192:193], v[212:213] op_sel_hi:[1,0]\n"
  "v_pk_mul_f32 v[178:179], v[178:179], v[190:191] op_sel_hi:[1,0]\n"
  "v_pk_mul_f32 v[202:203], v[202:203], v[214:215] op_sel_hi:[1,0]\n"
  "v_pk_mul_f32 v[170:171], v[170:171], v[188:189] op_sel_hi:[1,0]\n"
  "v_pk_mul_f32 v[194:195], v[194:195], v[212:213] op_sel_hi:[1,0]\n"
  "v_pk_mul_f32 v[180:181], v[180:181], v[190:191] op_sel_hi:[1,0]\n"
  "v_pk_mul_f32 v[204:205], v[204:205], v[214:215] op_sel_hi:[1,0]\n"
  "v_pk_mul_f32 v[172:173], v[172:173], v[188:189] op_sel_hi:[1,0]\n"
  "v_pk_mul_f32 v[196:197], v[196:197], v[212:213] op_sel_hi:[1,0]\n"
  "v_pk_mul_f32 v[182:183], v[182:183], v[190:191] op_sel_hi:[1,0]\n"
  "v_pk_mul_f32 v[206:207], v[206:207], v[214:215] op_sel_hi:[1,0]\n"
  "v_pk_mul_f32 v[174:175], v[174:175], v[188:189] op_sel_hi:[1,0]\n"
  "v_pk_mul_f32 v[198:199], v[198:199], v[212:213] op_sel_hi:[1,0]\n"
  "v_pk_mul_f32 v[184:185], v[184:185], v[190:191] op_sel_hi:[1,0]\n"
  "v_pk_mul_f32 v[208:209], v[208:209], v[214:215] op_sel_hi:[1,0]\n"
  "v_pk_mul_f32 v[176:177], v[176:177], v[188:189] op_sel_hi:[1,0]\n"
  "v_pk_mul_f32 v[200:201], v[200:201], v[212:213] op_sel_hi:[1,0]\n"
  "v_pk_mul_f32 v[186:187], v[186:187], v[190:191] op_sel_hi:[1,0]\n"
  "v_pk_mul_f32 v[210:211], v[210:211], v[214:215] op_sel_hi:[1,0]\n"
  "v_cvt_pk_bf16_f32 v228, v168, v169\n"
  "v_cvt_pk_bf16_f32 v240, v192, v193\n"
  "v_cvt_pk_bf16_f32 v229, v170, v171\n"
  "v_cvt_pk_bf16_f32 v241, v194, v195\n"
  "v_cvt_pk_bf16_f32 v230, v172, v173\n"
  "v_cvt_pk_bf16_f32 v242, v196, v197\n"
  "v_cvt_pk_bf16_f32 v231, v174, v175\n"
  "v_cvt_pk_bf16_f32 v243, v198, v199\n"
  "v_cvt_pk_bf16_f32 v232, v176, v177\n"
  "v_cvt_pk_bf16_f32 v244, v200, v201\n"
  "v_cvt_pk_bf16_f32 v233, v178, v179\n"
  "v_cvt_pk_bf16_f32 v245, v202, v203\n"
  "v_cvt_pk_bf16_f32 v234, v180, v181\n"
  "v_cvt_pk_bf16_f32 v246, v204, v205\n"
  "v_cvt_pk_bf16_f32 v235, v182, v183\n"
  "v_cvt_pk_bf16_f32 v247, v206, v207\n"
  "v_cvt_pk_bf16_f32 v236, v184, v185\n"
  "v_cvt_pk_bf16_f32 v248, v208, v209\n"
  "v_cvt_pk_bf16_f32 v237, v186, v187\n"
  "v_cvt_pk_bf16_f32 v249, v210, v211\n"
  "ds_write_b64 v36, v[228:229]\n"
  "ds_write_b64 v36, v[240:241] offset:5248\n"
  "ds_write_b64 v36, v[230:231] offset:16\n"
  "ds_write_b64 v36, v[242:243] offset:5264\n"
  "ds_write_b64 v36, v[232:233] offset:32\n"
  "ds_write_b64 v36, v[244:245] offset:5280\n"
  "ds_write_b64 v36, v[234:235] offset:48\n"
  "ds_write_b64 v36, v[246:247] offset:5296\n"
  "ds_write_b64 v36, v[236:237] offset:64\n"
  "ds_write_b64 v36, v[248:249] offset:5312\n"
  "s_waitcnt lgkmcnt(0)\n"
  "s_barrier\n"
  "ds_read_b64_tr_b16 v[128:129], v37\n"
  "ds_read_b64_tr_b16 v[148:149], v38\n"
  "ds_read_b64_tr_b16 v[130:131], v37 offset:1312\n"
  "ds_read_b64_tr_b16 v[150:151], v38 offset:1312\n"
  "ds_read_b64_tr_b16 v[132:133], v37 offset:5248\n"
  "ds_read_b64_tr_b16 v[152:153], v38 offset:5248\n"
  "ds_read_b64_tr_b16 v[134:135], v37 offset:6560\n"
  "ds_read_b64_tr_b16 v[154:155], v38 offset:6560\n"
  "ds_read_b64_tr_b16 v[136:137], v37 offset:10496\n"
  "ds_read_b64_tr_b16 v[156:157], v38 offset:10496\n"
  "ds_read_b64_tr_b16 v[138:139], v37 offset:11808\n"
  "ds_read_b64_tr_b16 v[158:159], v38 offset:11808\n"
  "s_waitcnt lgkmcnt(8)\n"
  "v_mfma_f32_32x32x16_bf16 v[168:183], v[128:131], v[148:151], 0\n"
  "ds_read_b64_tr_b16 v[140:141], v37 offset:15744\n"
  "ds_read_b64_tr_b16 v[160:161], v38 offset:15744\n"
  "ds_read_b64_tr_b16 v[142:143], v37 offset:17056\n"
  "ds_read_b64_tr_b16 v[162:163], v38 offset:17056\n"
  "s_waitcnt lgkmcnt(8)\n"
  "v_mfma_f32_32x32x16_bf16 v[168:183], v[132:135], v[152:155], v[168:183]\n"
  "ds_read_b64_tr_b16 v[128:129], v37 offset:20992\n"
  "ds_read_b64_tr_b16 v[148:149], v38 offset:20992\n"
  "ds_read_b64_tr_b16 v[130:131], v37 offset:22304\n"
  "ds_read_b64_tr_b16 v[150:151], v38 offset:22304\n"
  "s_waitcnt lgkmcnt(8)\n"
  "v_mfma_f32_32x32x16_bf16 v[168:183], v[136:139], v[156:159], v[168:183]\n"
  "ds_read_b64_tr_b16 v[132:133], v37 offset:26240\n"
  "ds_read_b64_tr_b16 v[152:153], v38 offset:26240\n"
  "ds_read_b64_tr_b16 v[134:135], v37 offset:27552\n"
  "ds_read_b64_tr_b16 v[154:155], v38 offset:27552\n"
  "s_waitcnt lgkmcnt(8)\n"
  "v_mfma_f32_32x32x16_bf16 v[168:183], v[140:143], v[160:163], v[168:183]\n"
  "ds_read_b64_tr_b16 v[136:137], v37 offset:31488\n"
  "ds_read_b64_tr_b16 v[156:157], v38 offset:31488\n"
  "ds_read_b64_tr_b16 v[138:139], v37 offset:32800\n"
  "ds_read_b64_tr_b16 v[158:159], v38 offset:32800\n"
  "s_waitcnt lgkmcnt(8)\n"
  "v_mfma_f32_32x32x16_bf16 v[168:183], v[128:131], v[148:151], v[168:183]\n"
  "ds_read_b64_tr_b16 v[140:141], v37 offset:36736\n"
  "ds_read_b64_tr_b16 v[160:161], v38 offset:36736\n"
  "ds_read_b64_tr_b16 v[142:143], v37 offset:38048\n"
  "ds_read_b64_tr_b16 v[162:163], v38 offset:38048\n"
  "s_waitcnt lgkmcnt(8)\n"
  "v_mfma_f32_32x32x16_bf16 v[168:183], v[132:135], v[152:155], v[168:183]\n"
  "s_waitcnt lgkmcnt(4)\n"
  "v_mfma_f32_32x32x16_bf16 v[168:183], v[136:139], v[156:159], v[168:183]\n"
  "s_waitcnt lgkmcnt(0)\n"
  "v_mfma_f32_32x32x16_bf16 v[168:183], v[140:143], v[160:163], v[168:183]\n"
  "s_nop 11\n"
  "v_cvt_pk_bf16_f32 v192, v168, v169\n"
  "v_cvt_pk_bf16_f32 v193, v170, v171\n"
  "v_cvt_pk_bf16_f32 v194, v172, v173\n"
  "v_cvt_pk_bf16_f32 v195, v174, v175\n"
  "v_cvt_pk_bf16_f32 v196, v176, v177\n"
  "v_cvt_pk_bf16_f32 v197, v178, v179\n"
  "v_cvt_pk_bf16_f32 v198, v180, v181\n"
  "v_cvt_pk_bf16_f32 v199, v182, v183\n"
  "s_cmp_eq_u32 s56, 1\n"
  "s_nop 3\n"
  "s_cbranch_scc1 .Lhh_ft1_5\n"
  "buffer_store_dwordx4 v[196:199], v39, s[12:15], s55 offen offset:768 sc1\n"
  "buffer_store_dwordx4 v[192:195], v39, s[12:15], s55 offen sc1\n"
  "s_branch .Lhh_ftd_6\n"
  ".Lhh_ft1_5:\n"
  "buffer_store_dwordx2 v[192:193], v40, s[12:15], s55 offen offset:1536 sc1\n"
  ".Lhh_ftd_6:\n"
  "s_nop 1\n"
  "v_lshlrev_b32 v4, 4, v0\n"
  "v_add_u32 v4, 0x1f400, v4\n"
  "s_waitcnt vmcnt(13)\n"
  "ds_write_b128 v4, a[160:163]\n"
  "s_waitcnt vmcnt(12)\n"
  "ds_write_b128 v4, a[164:167] offset:4096\n"
  "s_waitcnt vmcnt(11)\n"
  "ds_write_b128 v4, a[168:171] offset:8192\n"
  "s_waitcnt vmcnt(10)\n"
  "ds_write_b128 v60, a[172:175]\n"
  "s_waitcnt vmcnt(9)\n"
  "ds_write_b128 v4, a[176:179] offset:15360\n"
  "s_waitcnt vmcnt(8)\n"
  "ds_write_b128 v4, a[180:183] offset:19456\n"
  "s_waitcnt vmcnt(7)\n"
  "ds_write_b128 v61, a[184:187]\n"
  "s_waitcnt lgkmcnt(0)\n"
  "s_barrier\n"
  "s_cmp_eq_u32 s25, 3\n"
  "s_cbranch_scc1 .Lhh_w3_7\n"
  "ds_read_b64_tr_b16 a[160:161], v34\n"
  "ds_read_b64_tr_b16 a[162:163], v34 offset:768\n"
  "ds_read_b64_tr_b16 a[164:165], v34 offset:1536\n"
  "ds_read_b64_tr_b16 a[166:167], v34 offset:2304\n"
  "ds_read_b64_tr_b16 a[168:169], v34 offset:32\n"
  "ds_read_b64_tr_b16 a[170:171], v34 offset:800\n"
  "ds_read_b64_tr_b16 a[172:173], v34 offset:1568\n"
  "ds_read_b64_tr_b16 a[174:175], v34 offset:2336\n"
  "ds_read_b64_tr_b16 a[176:177], v35 offset:3072\n"
  "ds_read_b64_tr_b16 a[178:179], v35 offset:6912\n"
  "s_branch .Lhh_wd_8\n"
  ".Lhh_w3_7:\n"
  "ds_read_b64_tr_b16 a[160:161], v34\n"
  "ds_read_b64_tr_b16 a[162:163], v34 offset:2560\n"
  "ds_read_b64_tr_b16 a[164:165], v34 offset:8\n"
  "ds_read_b64_tr_b16 a[166:167], v34 offset:2568\n"
  "ds_read_b64_tr_b16 a[168:169], v34 offset:16\n"
  "ds_read_b64_tr_b16 a[170:171], v34 offset:2576\n"
  "ds_read_b64_tr_b16 a[172:173], v34 offset:24\n"
  "ds_read_b64_tr_b16 a[174:175], v34 offset:2584\n"
  "ds_read_b64_tr_b16 a[176:177], v34 offset:32\n"
  "ds_read_b64_tr_b16 a[178:179], v34 offset:2592\n"
  "ds_read_b64_tr_b16 a[180:181], v34 offset:5120\n"
  "ds_read_b64_tr_b16 a[182:183], v34 offset:7680\n"
  "ds_read_b64_tr_b16 a[184:185], v34 offset:5128\n"
  "ds_read_b64_tr_b16 a[186:187], v34 offset:7688\n"
  "ds_read_b64_tr_b16 a[188:189], v34 offset:5136\n"
  "ds_read_b64_tr_b16 a[190:191], v34 offset:7696\n"
  "ds_read_b64_tr_b16 a[192:193], v34 offset:5144\n"
  "ds_read_b64_tr_b16 a[194:195], v34 offset:7704\n"
  "ds_read_b64_tr_b16 a[196:197], v34 offset:5152\n"
  "ds_read_b64_tr_b16 a[198:199], v34 offset:7712\n"
  ".Lhh_wd_8:\n"
  "ds_read_b128 v[128:131], v21\n"
  "ds_read_b128 v[132:135], v21 offset:1024\n"
  "ds_read_b128 v[136:139], v44 offset:2048\n"
  "s_lshl_b32 s48, s42, 9\n"
  "s_add_u32 s48, s48, 0x1000\n"
  "s_waitcnt lgkmcnt(0)\n"
  "v_mfma_f32_16x16x32_bf16 v[88:91], v[128:131], a[0:3], 0\n"
  "ds_read_b128 v[148:151], v21 offset:2560\n"
  "v_mfma_f32_16x16x32_bf16 v[92:95], v[128:131], a[4:7], 0\n"
  "ds_read_b128 v[152:155], v21 offset:3584\n"
  "v_mfma_f32_16x16x32_bf16 v[96:99], v[132:135], a[0:3], 0\n"
  "ds_read_b128 v[156:159], v44 offset:4608\n"
  "v_mfma_f32_16x16x32_bf16 v[100:103], v[132:135], a[4:7], 0\n"
  "s_add_u32 s11, s44, 0x50000\n"
  "s_add_u32 m0, s97, 0xd000\n"
  "s_nop 0\n"
  "buffer_load_dwordx4 v51, s[16:19], s11 offen lds\n"
  "v_mfma_f32_16x16x32_bf16 v[104:107], v[136:139], a[0:3], 0\n"
  "v_mfma_f32_16x16x32_bf16 v[108:111], v[136:139], a[4:7], 0\n"
  "s_waitcnt lgkmcnt(0)\n"
  "v_mfma_f32_16x16x32_bf16 v[88:91], v[148:151], a[8:11], v[88:91]\n"
  "ds_read_b128 v[128:131], v21 offset:5120\n"
  "v_mfma_f32_16x16x32_bf16 v[92:95], v[148:151], a[12:15], v[92:95]\n"
  "ds_read_b128 v[132:135], v21 offset:6144\n"
  "v_mfma_f32_16x16x32_bf16 v[96:99], v[152:155], a[8:11], v[96:99]\n"
  "ds_read_b128 v[136:139], v44 offset:7168\n"
  "v_mfma_f32_16x16x32_bf16 v[100:103], v[152:155], a[12:15], v[100:103]\n"
  "s_add_u32 s11, s44, 0x50000\n"
  "s_add_u32 m0, s97, 0xe000\n"
  "s_nop 0\n"
  "buffer_load_dwordx4 v52, s[16:19], s11 offen lds\n"
  "v_mfma_f32_16x16x32_bf16 v[104:107], v[156:159], a[8:11], v[104:107]\n"
  "v_mfma_f32_16x16x32_bf16 v[108:111], v[156:159], a[12:15], v[108:111]\n"
  "s_waitcnt lgkmcnt(0)\n"
  "v_mfma_f32_16x16x32_bf16 v[88:91], v[128:131], a[16:19], v[88:91]\n"
  "ds_read_b128 v[148:151], v21 offset:7680\n"
  "v_mfma_f32_16x16x32_bf16 v[92:95], v[128:131], a[20:23], v[92:95]\n"
  "ds_read_b128 v[152:155], v21 offset:8704\n"
  "v_mfma_f32_16x16x32_bf16 v[96:99], v[132:135], a[16:19], v[96:99]\n"
  "ds_read_b128 v[156:159], v44 offset:9728\n"
  "v_mfma_f32_16x16x32_bf16 v[88:91], v[216:219], v[68:71], v[88:91]\n"
  "s_add_u32 s11, s44, 0x78000\n"
  "s_add_u32 m0, s97, 0xf000\n"
  "s_nop 0\n"
  "buffer_load_dwordx4 v48, s[16:19], s11 offen lds\n"
  "v_mfma_f32_16x16x32_bf16 v[92:95], v[216:219], v[72:75], v[92:95]\n"
  "v_mfma_f32_16x16x32_bf16 v[96:99], v[220:223], v[68:71], v[96:99]\n"
  "v_mfma_f32_16x16x32_bf16 v[100:103], v[132:135], a[20:23], v[100:103]\n"
  "v_mfma_f32_16x16x32_bf16 v[104:107], v[136:139], a[16:19], v[104:107]\n"
  "v_mfma_f32_16x16x32_bf16 v[108:111], v[136:139], a[20:23], v[108:111]\n"
  "s_waitcnt lgkmcnt(0)\n"
  "v_mfma_f32_16x16x32_bf16 v[88:91], v[148:151], a[24:27], v[88:91]\n"
  "ds_read_b128 v[128:131], v21 offset:10240\n"
  "v_mfma_f32_16x16x32_bf16 v[92:95], v[148:151], a[28:31], v[92:95]\n"
  "ds_read_b128 v[132:135], v21 offset:11264\n"
  "v_mfma_f32_16x16x32_bf16 v[96:99], v[152:155], a[24:27], v[96:99]\n"
  "ds_read_b128 v[136:139], v44 offset:12288\n"
  "v_mfma_f32_16x16x32_bf16 v[100:103], v[220:223], v[72:75], v[100:103]\n"
  "s_add_u32 s11, s44, 0x78000\n"
  "s_add_u32 m0, s97, 0x10000\n"
  "s_nop 0\n"
  "buffer_load_dwordx4 v49, s[16:19], s11 offen lds\n"
  "v_mfma_f32_16x16x32_bf16 v[104:107], v[224:227], v[68:71], v[104:107]\n"
  "v_mfma_f32_16x16x32_bf16 v[108:111], v[224:227], v[72:75], v[108:111]\n"
  "v_mfma_f32_16x16x32_bf16 v[100:103], v[152:155], a[28:31], v[100:103]\n"
  "v_mfma_f32_16x16x32_bf16 v[104:107], v[156:159], a[24:27], v[104:107]\n"
  "v_mfma_f32_16x16x32_bf16 v[108:111], v[156:159], a[28:31], v[108:111]\n"
  "s_waitcnt lgkmcnt(0)\n"
  "v_mfma_f32_16x16x32_bf16 v[88:91], v[128:131], a[32:35], v[88:91]\n"
  "ds_read_b128 v[148:151], v21 offset:12800\n"
  "v_mfma_f32_16x16x32_bf16 v[92:95], v[128:131], a[36:39], v[92:95]\n"
  "ds_read_b128 v[152:155], v21 offset:13824\n"
  "v_mfma_f32_16x16x32_bf16 v[96:99], v[132:135], a[32:35], v[96:99]\n"
  "ds_read_b128 v[156:159], v44 offset:14848\n"
  "v_mfma_f32_16x16x32_bf16 v[88:91], v[216:219], v[76:79], v[88:91]\n"
  "s_add_u32 s11, s44, 0x78000\n"
  "s_add_u32 m0, s97, 0x11000\n"
  "s_nop 0\n"
  "buffer_load_dwordx4 v50, s[16:19], s11 offen lds\n"
  "v_mfma_f32_16x16x32_bf16 v[92:95], v[216:219], v[80:83], v[92:95]\n"
  "v_mfma_f32_16x16x32_bf16 v[96:99], v[220:223], v[76:79], v[96:99]\n"
  "v_mfma_f32_16x16x32_bf16 v[100:103], v[132:135], a[36:39], v[100:103]\n"
  "v_mfma_f32_16x16x32_bf16 v[104:107], v[136:139], a[32:35], v[104:107]\n"
  "v_mfma_f32_16x16x32_bf16 v[108:111], v[136:139], a[36:39], v[108:111]\n"
  "s_waitcnt lgkmcnt(0)\n"
  "v_mfma_f32_16x16x32_bf16 v[88:91], v[148:151], a[40:43], v[88:91]\n"
  "ds_read_b128 v[128:131], v21 offset:15360\n"
  "v_mfma_f32_16x16x32_bf16 v[92:95], v[148:151], a[44:47], v[92:95]\n"
  "ds_read_b128 v[132:135], v21 offset:16384\n"
  "v_mfma_f32_16x16x32_bf16 v[96:99], v[152:155], a[40:43], v[96:99]\n"
  "ds_read_b128 v[136:139], v44 offset:17408\n"
  "v_mfma_f32_16x16x32_bf16 v[100:103], v[220:223], v[80:83], v[100:103]\n"
  "s_add_u32 s11, s44, 0x78000\n"
  "s_add_u32 m0, s97, 0x12000\n"
  "s_nop 0\n"
  "buffer_load_dwordx4 v51, s[16:19], s11 offen lds\n"
  "v_mfma_f32_16x16x32_bf16 v[104:107], v[224:227], v[76:79], v[104:107]\n"
  "v_mfma_f32_16x16x32_bf16 v[108:111], v[224:227], v[80:83], v[108:111]\n"
  "v_mfma_f32_16x16x32_bf16 v[100:103], v[152:155], a[44:47], v[100:103]\n"
  "v_mfma_f32_16x16x32_bf16 v[104:107], v[156:159], a[40:43], v[104:107]\n"
  "v_mfma_f32_16x16x32_bf16 v[108:111], v[156:159], a[44:47], v[108:111]\n"
  "s_waitcnt lgkmcnt(0)\n"
  "v_mfma_f32_16x16x32_bf16 v[88:91], v[128:131], a[48:51], v[88:91]\n"
  "ds_read_b128 v[148:151], v21 offset:17920\n"
  "v_mfma_f32_16x16x32_bf16 v[92:95], v[128:131], a[52:55], v[92:95]\n"
  "ds_read_b128 v[152:155], v21 offset:18944\n"
  "v_mfma_f32_16x16x32_bf16 v[96:99], v[132:135], a[48:51], v[96:99]\n"
  "ds_read_b128 v[156:159], v44 offset:19968\n"
  "v_mfma_f32_16x16x32_bf16 v[100:103], v[132:135], a[52:55], v[100:103]\n"
  "s_add_u32 s11, s44, 0x78000\n"
  "s_add_u32 m0, s97, 0x13000\n"
  "s_nop 0\n"
  "buffer_load_dwordx4 v52, s[16:19], s11 offen lds\n"
  "v_mfma_f32_16x16x32_bf16 v[104:107], v[136:139], a[48:51], v[104:107]\n"
  "v_mfma_f32_16x16x32_bf16 v[108:111], v[136:139], a[52:55], v[108:111]\n"
  "s_waitcnt lgkmcnt(0)\n"
  "v_mfma_f32_16x16x32_bf16 v[88:91], v[148:151], a[56:59], v[88:91]\n"
  "ds_read_b128 v[128:131], v21 offset:20480\n"
  "v_mfma_f32_16x16x32_bf16 v[92:95], v[148:151], a[60:63], v[92:95]\n"
  "ds_read_b128 v[132:135], v21 offset:21504\n"
  "v_mfma_f32_16x16x32_bf16 v[96:99], v[152:155], a[56:59], v[96:99]\n"
  "ds_read_b128 v[136:139], v44 offset:22528\n"
  "v_mfma_f32_16x16x32_bf16 v[100:103], v[152:155], a[60:63], v[100:103]\n"
  "s_add_u32 s64, s66, 0x1000\n"
  "s_waitcnt vmcnt(7)\n"
  "s_mov_b64 exec, 1\n"
  "buffer_store_dwordx2 v[10:11], off, s[12:15], s64 sc1\n"
  "s_mov_b64 exec, -1\n"
  "v_mfma_f32_16x16x32_bf16 v[104:107], v[156:159], a[56:59], v[104:107]\n"
  "s_add_u32 s11, s44, 0xa0000\n"
  "s_add_u32 m0, s97, 0x14000\n"
  "s_nop 0\n"
  "buffer_load_dwordx4 v48, s[16:19], s11 offen lds\n"
  "v_mfma_f32_16x16x32_bf16 v[108:111], v[156:159], a[60:63], v[108:111]\n"
  "s_waitcnt lgkmcnt(0)\n"
  "v_mfma_f32_16x16x32_bf16 v[88:91], v[128:131], a[64:67], v[88:91]\n"
  "ds_read_b128 v[148:151], v21 offset:23040\n"
  "v_mfma_f32_16x16x32_bf16 v[92:95], v[128:131], a[68:71], v[92:95]\n"
  "ds_read_b128 v[152:155], v21 offset:24064\n"
  "v_mfma_f32_16x16x32_bf16 v[96:99], v[132:135], a[64:67], v[96:99]\n"
  "ds_read_b128 v[156:159], v44 offset:25088\n"
  "v_mfma_f32_16x16x32_bf16 v[100:103], v[132:135], a[68:71], v[100:103]\n"
  "s_add_u32 s11, s44, 0xa0000\n"
  "s_add_u32 m0, s97, 0x15000\n"
  "s_nop 0\n"
  "buffer_load_dwordx4 v49, s[16:19], s11 offen lds\n"
  "v_mfma_f32_16x16x32_bf16 v[104:107], v[136:139], a[64:67], v[104:107]\n"
  "buffer_store_dwordx4 a[160:163], v29, s[12:15], s39 offen sc1\n"
  "v_mfma_f32_16x16x32_bf16 v[108:111], v[136:139], a[68:71], v[108:111]\n"
  "s_waitcnt lgkmcnt(0)\n"
  "v_mfma_f32_16x16x32_bf16 v[88:91], v[148:151], a[72:75], v[88:91]\n"
  "ds_read_b128 v[128:131], v21 offset:25600\n"
  "v_mfma_f32_16x16x32_bf16 v[92:95], v[148:151], a[76:79], v[92:95]\n"
  "ds_read_b128 v[132:135], v21 offset:26624\n"
  "v_mfma_f32_16x16x32_bf16 v[96:99], v[152:155], a[72:75], v[96:99]\n"
  "ds_read_b128 v[136:139], v44 offset:27648\n"
  "v_mfma_f32_16x16x32_bf16 v[100:103], v[152:155], a[76:79], v[100:103]\n"
  "s_add_u32 s11, s44, 0xa0000\n"
  "s_add_u32 m0, s97, 0x16000\n"
  "s_nop 0\n"
  "buffer_load_dwordx4 v50, s[16:19], s11 offen lds\n"
  "v_mfma_f32_16x16x32_bf16 v[104:107], v[156:159], a[72:75], v[104:107]\n"
  "buffer_store_dwordx4 a[164:167], v29, s[12:15], s50 offen sc1\n"
  "v_mfma_f32_16x16x32_bf16 v[108:111], v[156:159], a[76:79], v[108:111]\n"
  "s_waitcnt lgkmcnt(0)\n"
  "v_mfma_f32_16x16x32_bf16 v[88:91], v[128:131], a[80:83], v[88:91]\n"
  "ds_read_b128 v[148:151], v21 offset:28160\n"
  "v_mfma_f32_16x16x32_bf16 v[92:95], v[128:131], a[84:87], v[92:95]\n"
  "ds_read_b128 v[152:155], v21 offset:29184\n"
  "v_mfma_f32_16x16x32_bf16 v[96:99], v[132:135], a[80:83], v[96:99]\n"
  "ds_read_b128 v[156:159], v44 offset:30208\n"
  "v_mfma_f32_16x16x32_bf16 v[100:103], v[132:135], a[84:87], v[100:103]\n"
  "s_add_u32 s11, s44, 0xa0000\n"
  "s_add_u32 m0, s97, 0x17000\n"
  "s_nop 0\n"
  "buffer_load_dwordx4 v51, s[16:19], s11 offen lds\n"
  "v_mfma_f32_16x16x32_bf16 v[104:107], v[136:139], a[80:83], v[104:107]\n"
  "v_mfma_f32_16x16x32_bf16 v[108:111], v[136:139], a[84:87], v[108:111]\n"
  "s_waitcnt lgkmcnt(0)\n"
  "v_mfma_f32_16x16x32_bf16 v[88:91], v[148:151], a[88:91], v[88:91]\n"
  "ds_read_b128 v[128:131], v21 offset:30720\n"
  "v_mfma_f32_16x16x32_bf16 v[92:95], v[148:151], a[92:95], v[92:95]\n"
  "ds_read_b128 v[132:135], v21 offset:31744\n"
  "v_mfma_f32_16x16x32_bf16 v[96:99], v[152:155], a[88:91], v[96:99]\n"
  "ds_read_b128 v[136:139], v44 offset:32768\n"
  "v_mfma_f32_16x16x32_bf16 v[100:103], v[152:155], a[92:95], v[100:103]\n"
  "s_add_u32 s11, s44, 0xa0000\n"
  "s_add_u32 m0, s97, 0x18000\n"
  "s_nop 0\n"
  "buffer_load_dwordx4 v52, s[16:19], s11 offen lds\n"
  "v_mfma_f32_16x16x32_bf16 v[104:107], v[156:159], a[88:91], v[104:107]\n"
  "v_mfma_f32_16x16x32_bf16 v[108:111], v[156:159], a[92:95], v[108:111]\n"
  "s_waitcnt lgkmcnt(0)\n"
  "v_mfma_f32_16x16x32_bf16 v[88:91], v[128:131], a[96:99], v[88:91]\n"
  "ds_read_b128 v[148:151], v21 offset:33280\n"
  "v_mfma_f32_16x16x32_bf16 v[92:95], v[128:131], a[100:103], v[92:95]\n"
  "ds_read_b128 v[152:155], v21 offset:34304\n"
  "v_mfma_f32_16x16x32_bf16 v[96:99], v[132:135], a[96:99], v[96:99]\n"
  "ds_read_b128 v[156:159], v44 offset:35328\n"
  "v_mfma_f32_16x16x32_bf16 v[100:103], v[132:135], a[100:103], v[100:103]\n"
  "v_mfma_f32_16x16x32_bf16 v[104:107], v[136:139], a[96:99], v[104:107]\n"
  "v_mfma_f32_16x16x32_bf16 v[108:111], v[136:139], a[100:103], v[108:111]\n"
  "s_waitcnt lgkmcnt(0)\n"
  "v_mfma_f32_16x16x32_bf16 v[88:91], v[148:151], a[104:107], v[88:91]\n"
  "ds_read_b128 v[128:131], v21 offset:35840\n"
  "v_mfma_f32_16x16x32_bf16 v[92:95], v[148:151], a[108:111], v[92:95]\n"
  "ds_read_b128 v[132:135], v21 offset:36864\n"
  "v_mfma_f32_16x16x32_bf16 v[96:99], v[152:155], a[104:107], v[96:99]\n"
  "ds_read_b128 v[136:139], v44 offset:37888\n"
  "v_mfma_f32_16x16x32_bf16 v[100:103], v[152:155], a[108:111], v[100:103]\n"
  "v_mfma_f32_16x16x32_bf16 v[104:107], v[156:159], a[104:107], v[104:107]\n"
  "v_mfma_f32_16x16x32_bf16 v[108:111], v[156:159], a[108:111], v[108:111]\n"
  "s_waitcnt lgkmcnt(0)\n"
  "v_mfma_f32_16x16x32_bf16 v[88:91], v[128:131], a[112:115], v[88:91]\n"
  "ds_read_b128 v[148:151], v21 offset:38400\n"
  "v_mfma_f32_16x16x32_bf16 v[92:95], v[128:131], a[116:119], v[92:95]\n"
  "ds_read_b128 v[152:155], v21 offset:39424\n"
  "v_mfma_f32_16x16x32_bf16 v[96:99], v[132:135], a[112:115], v[96:99]\n"
  "ds_read_b128 v[156:159], v44 offset:40448\n"
  "v_mfma_f32_16x16x32_bf16 v[100:103], v[132:135], a[116:119], v[100:103]\n"
  "v_mfma_f32_16x16x32_bf16 v[104:107], v[136:139], a[112:115], v[104:107]\n"
  "v_mfma_f32_16x16x32_bf16 v[108:111], v[136:139], a[116:119], v[108:111]\n"
  "s_waitcnt lgkmcnt(0)\n"
  "v_mfma_f32_16x16x32_bf16 v[88:91], v[148:151], a[120:123], v[88:91]\n"
  "ds_read_b128 v[128:131], v21 offset:40960\n"
  "v_mfma_f32_16x16x32_bf16 v[92:95], v[148:151], a[124:127], v[92:95]\n"
  "ds_read_b128 v[132:135], v21 offset:41984\n"
  "v_mfma_f32_16x16x32_bf16 v[96:99], v[152:155], a[120:123], v[96:99]\n"
  "ds_read_b128 v[136:139], v44 offset:43008\n"
  "v_mfma_f32_16x16x32_bf16 v[100:103], v[152:155], a[124:127], v[100:103]\n"
  "v_mfma_f32_16x16x32_bf16 v[104:107], v[156:159], a[120:123], v[104:107]\n"
  "v_mfma_f32_16x16x32_bf16 v[108:111], v[156:159], a[124:127], v[108:111]\n"
  "s_waitcnt lgkmcnt(0)\n"
  "v_mfma_f32_16x16x32_bf16 v[88:91], v[128:131], a[128:131], v[88:91]\n"
  "ds_read_b128 v[148:151], v21 offset:43520\n"
  "v_mfma_f32_16x16x32_bf16 v[92:95], v[128:131], a[132:135], v[92:95]\n"
  "ds_read_b128 v[152:155], v21 offset:44544\n"
  "v_mfma_f32_16x16x32_bf16 v[96:99], v[132:135], a[128:131], v[96:99]\n"
  "ds_read_b128 v[156:159], v44 offset:45568\n"
  "v_mfma_f32_16x16x32_bf16 v[100:103], v[132:135], a[132:135], v[100:103]\n"
  "v_mfma_f32_16x16x32_bf16 v[104:107], v[136:139], a[128:131], v[104:107]\n"
  "v_mfma_f32_16x16x32_bf16 v[108:111], v[136:139], a[132:135], v[108:111]\n"
  "s_waitcnt lgkmcnt(0)\n"
  "v_mfma_f32_16x16x32_bf16 v[88:91], v[148:151], a[136:139], v[88:91]\n"
  "ds_read_b128 v[128:131], v21 offset:46080\n"
  "v_mfma_f32_16x16x32_bf16 v[92:95], v[148:151], a[140:143], v[92:95]\n"
  "ds_read_b128 v[132:135], v21 offset:47104\n"
  "v_mfma_f32_16x16x32_bf16 v[96:99], v[152:155], a[136:139], v[96:99]\n"
  "ds_read_b128 v[136:139], v44 offset:48128\n"
  "v_mfma_f32_16x16x32_bf16 v[100:103], v[152:155], a[140:143], v[100:103]\n"
  "v_mfma_f32_16x16x32_bf16 v[104:107], v[156:159], a[136:139], v[104:107]\n"
  "v_mfma_f32_16x16x32_bf16 v[108:111], v[156:159], a[140:143], v[108:111]\n"
  "s_waitcnt lgkmcnt(0)\n"
  "v_mfma_f32_16x16x32_bf16 v[88:91], v[128:131], a[144:147], v[88:91]\n"
  "ds_read_b128 v[148:151], v21 offset:48640\n"
  "v_mfma_f32_16x16x32_bf16 v[92:95], v[128:131], a[148:151], v[92:95]\n"
  "ds_read_b128 v[152:155], v21 offset:49664\n"
  "v_mfma_f32_16x16x32_bf16 v[96:99], v[132:135], a[144:147], v[96:99]\n"
  "ds_read_b128 v[156:159], v44 offset:50688\n"
  "v_mfma_f32_16x16x32_bf16 v[100:103], v[132:135], a[148:151], v[100:103]\n"
  "v_mfma_f32_16x16x32_bf16 v[104:107], v[136:139], a[144:147], v[104:107]\n"
  "v_mfma_f32_16x16x32_bf16 v[108:111], v[136:139], a[148:151], v[108:111]\n"
  "s_waitcnt lgkmcnt(0)\n"
  "v_mfma_f32_16x16x32_bf16 v[88:91], v[148:151], a[152:155], v[88:91]\n"
  "v_mfma_f32_16x16x32_bf16 v[92:95], v[148:151], a[156:159], v[92:95]\n"
  "v_mfma_f32_16x16x32_bf16 v[96:99], v[152:155], a[152:155], v[96:99]\n"
  "v_mfma_f32_16x16x32_bf16 v[100:103], v[152:155], a[156:159], v[100:103]\n"
  "buffer_load_dwordx2 v[64:65], v62, s[12:15], s48 offen sc1\n"
  "v_mfma_f32_16x16x32_bf16 v[104:107], v[156:159], a[152:155], v[104:107]\n"
  "v_mfma_f32_16x16x32_bf16 v[108:111], v[156:159], a[156:159], v[108:111]\n"
  "s_barrier\n"
  "v_lshlrev_b32 v9, 4, v0\n"
  "s_lshl_b32 s10, s42, 16\n"
  "s_add_u32 s10, s10, 0x20000\n"
  "s_mov_b32 s20, 0x3f80\n"
  "s_mov_b32 s21, 0x3f800000\n"
  "s_waitcnt vmcnt(0)\n"
  "v_cmp_ne_u64 vcc, s[72:73], v[64:65]\n"
  "s_nop 3\n"
  "s_cbranch_vccz .Lhh_done_10x\n"
  "s_mov_b32 s63, 0x10000\n"
  ".Lhh_poll_9:\n"
  "buffer_load_dwordx2 v[64:65], v62, s[12:15], s48 offen sc1\n"
  "s_waitcnt vmcnt(0)\n"
  "v_cmp_ne_u64 vcc, s[72:73], v[64:65]\n"
  "s_nop 4\n"
  "s_cbranch_vccz .Lhh_done_10\n"
  "s_sleep 1\n"
  "s_sub_u32 s63, s63, 1\n"
  "s_cbranch_scc0 .Lhh_poll_9\n"
  ".Lhh_done_10:\n"
  ".Lhh_done_10x:\n"
  "buffer_load_dwordx4 v[36:39], v9, s[12:15], s10 offen sc1\n"
  "s_add_u32 s10, s10, 0x1000\n"
  "buffer_load_dwordx4 v[40:43], v9, s[12:15], s10 offen sc1\n"
  "s_add_u32 s10, s10, 0x1000\n"
  "buffer_load_dwordx4 v[44:47], v9, s[12:15], s10 offen sc1\n"
  "s_add_u32 s10, s10, 0x1000\n"
  "buffer_load_dwordx4 v[56:59], v9, s[12:15], s10 offen sc1\n"
  "s_add_u32 s10, s10, 0x1000\n"
  "buffer_load_dwordx4 v[68:71], v9, s[12:15], s10 offen sc1\n"
  "s_add_u32 s10, s10, 0x1000\n"
  "buffer_load_dwordx4 v[72:75], v9, s[12:15], s10 offen sc1\n"
  "s_add_u32 s10, s10, 0x1000\n"
  "buffer_load_dwordx4 v[76:79], v9, s[12:15], s10 offen sc1\n"
  "s_add_u32 s10, s10, 0x1000\n"
  "buffer_load_dwordx4 v[80:83], v9, s[12:15], s10 offen sc1\n"
  "s_add_u32 s10, s10, 0x1000\n"
  "v_mov_b32 v216, 0\n"
  "v_mov_b32 v217, 0\n"
  "v_mov_b32 v218, 0\n"
  "v_mov_b32 v219, 0\n"
  "v_mov_b32 v220, 0\n"
  "v_mov_b32 v221, 0\n"
  "v_mov_b32 v222, 0\n"
  "v_mov_b32 v223, 0\n"
  "v_mov_b32 v154, 0\n"
  "buffer_load_dwordx4 v[112:115], v9, s[12:15], s10 offen sc1\n"
  "s_add_u32 s10, s10, 0x1000\n"
  "v_mov_b32 v155, 0\n"
  "v_mov_b32 v162, 0\n"
  "v_mov_b32 v163, 0\n"
  "v_cvt_pk_bf16_f32 v148, v88, v89\n"
  "v_cvt_pk_bf16_f32 v149, v90, v91\n"
  "v_cvt_pk_bf16_f32 v156, v92, v93\n"
  "v_cvt_pk_bf16_f32 v157, v94, v95\n"
  "buffer_load_dwordx4 v[116:119], v9, s[12:15], s10 offen sc1\n"
  "s_add_u32 s10, s10, 0x1000\n"
  "v_cvt_pk_bf16_f32 v150, v96, v97\n"
  "v_cvt_pk_bf16_f32 v151, v98, v99\n"
  "v_cvt_pk_bf16_f32 v158, v100, v101\n"
  "v_cvt_pk_bf16_f32 v159, v102, v103\n"
  "v_cvt_pk_bf16_f32 v152, v104, v105\n"
  "v_cvt_pk_bf16_f32 v153, v106, v107\n"
  "v_cvt_pk_bf16_f32 v160, v108, v109\n"
  "buffer_load_dwordx4 v[120:123], v9, s[12:15], s10 offen sc1\n"
  "s_add_u32 s10, s10, 0x1000\n"
  "v_cvt_pk_bf16_f32 v161, v110, v111\n"
  "v_mfma_f32_16x16x32_bf16 v[228:231], a[224:227], v[148:151], 0\n"
  "v_mfma_f32_16x16x32_bf16 v[232:235], a[232:235], v[148:151], 0\n"
  "v_mfma_f32_16x16x32_bf16 v[236:239], a[240:243], v[148:151], 0\n"
  "v_mfma_f32_16x16x32_bf16 v[240:243], a[224:227], v[156:159], 0\n"
  "v_mfma_f32_16x16x32_bf16 v[244:247], a[232:235], v[156:159], 0\n"
  "v_mfma_f32_16x16x32_bf16 v[248:251], a[240:243], v[156:159], 0\n"
  "buffer_load_dwordx4 v[128:131], v9, s[12:15], s10 offen sc1\n"
  "s_add_u32 s10, s10, 0x1000\n"
  "v_mfma_f32_16x16x32_bf16 v[228:231], a[228:231], v[152:155], v[228:231]\n"
  "v_mfma_f32_16x16x32_bf16 v[232:235], a[236:239], v[152:155], v[232:235]\n"
  "v_mfma_f32_16x16x32_bf16 v[236:239], a[244:247], v[152:155], v[236:239]\n"
  "v_mfma_f32_16x16x32_bf16 v[240:243], a[228:231], v[160:163], v[240:243]\n"
  "v_mfma_f32_16x16x32_bf16 v[244:247], a[236:239], v[160:163], v[244:247]\n"
  "v_mfma_f32_16x16x32_bf16 v[248:251], a[244:247], v[160:163], v[248:251]\n"
  "s_mov_b32 s22, 0xff00ff\n"
  "s_mov_b32 s23, 0xff00ff00\n"
  "v_bfe_u32 v3, v1, 2, 1\n"
  "v_lshl_add_u32 v4, v3, 3, v18\n"
  "v_add_u32 v5, 4, v4\n"
  "v_lshl_add_u32 v6, v3, 1, v18\n"
  "v_add_u32 v6, 16, v6\n"
  "v_cmp_lt_u32 vcc, 1, v18\n"
  "v_add_u32 v7, 18, v18\n"
  "v_mul_u32_u24 v8, 24, v20\n"
  "v_cndmask_b32 v6, v6, v7, vcc\n"
  "v_mul_u32_u24 v7, 0x780, v16\n"
  "v_add_u32 v7, 0x25800, v7\n"
  "buffer_load_dwordx4 v[132:135], v9, s[12:15], s10 offen sc1\n"
  "s_add_u32 s10, s10, 0x1000\n"
  "buffer_load_dwordx4 v[136:139], v9, s[12:15], s10 offen sc1\n"
  "s_add_u32 s10, s10, 0x1000\n"
  "buffer_load_dwordx4 v[140:143], v9, s[12:15], s10 offen sc1\n"
  "s_add_u32 s10, s10, 0x1000\n"
  "buffer_load_dwordx4 v[144:147], v9, s[12:15], s10 offen sc1\n"
  "v_cndmask_b32 v4, 21, v4, s[22:23]\n"
  "v_add_u32 v4, v4, v8\n"
  "v_lshl_add_u32 v14, v4, 4, v7\n"
  "v_lshl_add_u32 v23, v4, 3, v7\n"
  "v_cndmask_b32 v5, 21, v5, s[22:23]\n"
  "v_add_u32 v5, v5, v8\n"
  "v_lshl_add_u32 v26, v5, 4, v7\n"
  "v_lshl_add_u32 v27, v5, 3, v7\n"
  "v_cndmask_b32 v6, 21, v6, s[22:23]\n"
  "v_add_u32 v6, v6, v8\n"
  "v_lshl_add_u32 v28, v6, 4, v7\n"
  "v_lshl_add_u32 v34, v6, 3, v7\n"
  "v_max3_f32 v188, v228, v229, v230\n"
  "buffer_store_dwordx4 a[168:171], v29, s[12:15], s51 offen sc1\n"
  "v_max3_f32 v212, v240, v241, v242\n"
  "v_max3_f32 v189, -v228, -v229, -v230\n"
  "v_max3_f32 v213, -v240, -v241, -v242\n"
  "v_cndmask_b32 v236, v236, v238, s[76:77]\n"
  "v_cndmask_b32 v248, v248, v250, s[76:77]\n"
  "v_cndmask_b32 v237, v237, v239, s[76:77]\n"
  "v_cndmask_b32 v249, v249, v251, s[76:77]\n"
  "v_max3_f32 v188, v188, v231, v232\n"
  "v_max3_f32 v212, v212, v243, v244\n"
  "v_max3_f32 v189, v189, -v231, -v232\n"
  "v_max3_f32 v213, v213, -v243, -v244\n"
  "v_max3_f32 v188, v188, v233, v234\n"
  "v_max3_f32 v212, v212, v245, v246\n"
  "v_max3_f32 v189, v189, -v233, -v234\n"
  "v_max3_f32 v213, v213, -v245, -v246\n"
  "v_max3_f32 v188, v188, v235, v236\n"
  "buffer_store_dwordx4 a[172:175], v29, s[12:15], s52 offen sc1\n"
  "v_max3_f32 v212, v212, v247, v248\n"
  "v_max3_f32 v189, v189, -v235, -v236\n"
  "v_max3_f32 v213, v213, -v247, -v248\n"
  "v_max_f32 v188, v188, v237\n"
  "v_max_f32 v212, v212, v249\n"
  "v_max3_f32 v189, v189, -v237, -v237\n"
  "v_max3_f32 v213, v213, -v249, -v249\n"
  "s_nop 0\n"
  "v_permlane16_swap_b32 v188, v189\n"
  "v_permlane16_swap_b32 v212, v213\n"
  "v_max_f32 v188, v188, v189\n"
  "v_max_f32 v212, v212, v213\n"
  "v_mul_f32 v190, -v66, v188\n"
  "v_mul_f32 v214, -v84, v212\n"
  "v_mov_b32 v188, v190\n"
  "v_mov_b32 v212, v214\n"
  "s_nop 0\n"
  "v_permlane16_swap_b32 v190, v188\n"
  "buffer_store_dwordx4 a[176:179], v30, s[12:15], s53 offen sc1\n"
  "v_permlane16_swap_b32 v214, v212\n"
  "v_pk_fma_f32 v[168:169], v[228:229], v[66:67], v[190:191] op_sel_hi:[1,0,0]\n"
  "v_pk_fma_f32 v[192:193], v[240:241], v[84:85], v[214:215] op_sel_hi:[1,0,0]\n"
  "v_pk_fma_f32 v[170:171], v[230:231], v[66:67], v[190:191] op_sel_hi:[1,0,0]\n"
  "v_pk_fma_f32 v[194:195], v[242:243], v[84:85], v[214:215] op_sel_hi:[1,0,0]\n"
  "v_pk_fma_f32 v[172:173], v[232:233], v[66:67], v[190:191] op_sel_hi:[1,0,0]\n"
  "v_pk_fma_f32 v[196:197], v[244:245], v[84:85], v[214:215] op_sel_hi:[1,0,0]\n"
  "v_pk_fma_f32 v[174:175], v[234:235], v[66:67], v[190:191] op_sel_hi:[1,0,0]\n"
  "v_pk_fma_f32 v[198:199], v[246:247], v[84:85], v[214:215] op_sel_hi:[1,0,0]\n"
  "v_pk_fma_f32 v[176:177], v[236:237], v[66:67], v[190:191] op_sel_hi:[1,0,0]\n"
  "v_pk_fma_f32 v[200:201], v[248:249], v[84:85], v[214:215] op_sel_hi:[1,0,0]\n"
  "v_pk_fma_f32 v[178:179], v[228:229], v[66:67], v[188:189] op_sel_hi:[1,0,0] neg_lo:[0,1,0] neg_hi:[0,1,0]\n"
  "v_pk_fma_f32 v[202:203], v[240:241], v[84:85], v[212:213] op_sel_hi:[1,0,0] neg_lo:[0,1,0] neg_hi:[0,1,0]\n"
  "v_pk_fma_f32 v[180:181], v[230:231], v[66:67], v[188:189] op_sel_hi:[1,0,0] neg_lo:[0,1,0] neg_hi:[0,1,0]\n"
  "v_pk_fma_f32 v[204:205], v[242:243], v[84:85], v[212:213] op_sel_hi:[1,0,0] neg_lo:[0,1,0] neg_hi:[0,1,0]\n"
  "v_pk_fma_f32 v[182:183], v[232:233], v[66:67], v[188:189] op_sel_hi:[1,0,0] neg_lo:[0,1,0] neg_hi:[0,1,0]\n"
  "v_pk_fma_f32 v[206:207], v[244:245], v[84:85], v[212:213] op_sel_hi:[1,0,0] neg_lo:[0,1,0] neg_hi:[0,1,0]\n"
  "s_add_u32 s11, s44, 0x28000\n"
  "s_add_u32 m0, s97, 0x1f000\n"
  "s_nop 0\n"
  "buffer_load_dwordx4 v49, s[16:19], s11 offen lds\n"
  "v_pk_fma_f32 v[184:185], v[234:235], v[66:67], v[188:189] op_sel_hi:[1,0,0] neg_lo:[0,1,0] neg_hi:[0,1,0]\n"
  "v_pk_fma_f32 v[208:209], v[246:247], v[84:85], v[212:213] op_sel_hi:[1,0,0] neg_lo:[0,1,0] neg_hi:[0,1,0]\n"
  "v_pk_fma_f32 v[186:187], v[236:237], v[66:67], v[188:189] op_sel_hi:[1,0,0] neg_lo:[0,1,0] neg_hi:[0,1,0]\n"
  "v_pk_fma_f32 v[210:211], v[248:249], v[84:85], v[212:213] op_sel_hi:[1,0,0] neg_lo:[0,1,0] neg_hi:[0,1,0]\n"
  "v_exp_f32 v168, v168\n"
  "v_exp_f32 v192, v192\n"
  "v_exp_f32 v169, v169\n"
  "v_exp_f32 v193, v193\n"
  "v_exp_f32 v170, v170\n"
  "v_exp_f32 v194, v194\n"
  "v_exp_f32 v171, v171\n"
  "v_exp_f32 v195, v195\n"
  "v_exp_f32 v172, v172\n"
  "v_exp_f32 v196, v196\n"
  "v_exp_f32 v173, v173\n"
  "v_exp_f32 v197, v197\n"
  "s_add_u32 s11, s44, 0x28000\n"
  "s_add_u32 m0, s97, 0x20000\n"
  "s_nop 0\n"
  "buffer_load_dwordx4 v50, s[16:19], s11 offen lds\n"
  "v_exp_f32 v174, v174\n"
  "v_exp_f32 v198, v198\n"
  "v_exp_f32 v175, v175\n"
  "v_exp_f32 v199, v199\n"
  "v_exp_f32 v176, v176\n"
  "v_exp_f32 v200, v200\n"
  "v_exp_f32 v177, v177\n"
  "v_exp_f32 v201, v201\n"
  "v_exp_f32 v178, v178\n"
  "v_exp_f32 v202, v202\n"
  "v_exp_f32 v179, v179\n"
  "v_exp_f32 v203, v203\n"
  "v_exp_f32 v180, v180\n"
  "v_exp_f32 v204, v204\n"
  "v_exp_f32 v181, v181\n"
  "v_exp_f32 v205, v205\n"
  "v_exp_f32 v182, v182\n"
  "s_add_u32 s11, s44, 0x28000\n"
  "s_add_u32 m0, s97, 0x21000\n"
  "s_nop 0\n"
  "buffer_load_dwordx4 v51, s[16:19], s11 offen lds\n"
  "v_exp_f32 v206, v206\n"
  "v_exp_f32 v183, v183\n"
  "v_exp_f32 v207, v207\n"
  "v_exp_f32 v184, v184\n"
  "v_exp_f32 v208, v208\n"
  "v_exp_f32 v185, v185\n"
  "v_exp_f32 v209, v209\n"
  "v_exp_f32 v186, v186\n"
  "v_exp_f32 v210, v210\n"
  "v_exp_f32 v187, v187\n"
  "v_exp_f32 v211, v211\n"
  "v_pk_add_f32 v[188:189], v[168:169], v[170:171]\n"
  "v_pk_add_f32 v[212:213], v[192:193], v[194:195]\n"
  "v_pk_add_f32 v[190:191], v[178:179], v[180:181]\n"
  "v_pk_add_f32 v[214:215], v[202:203], v[204:205]\n"
  "v_pk_add_f32 v[188:189], v[188:189], v[172:173]\n"
  "s_add_u32 s11, s44, 0x28000\n"
  "s_add_u32 m0, s97, 0x22000\n"
  "s_nop 0\n"
  "buffer_load_dwordx4 v52, s[16:19], s11 offen lds\n"
  "v_pk_add_f32 v[212:213], v[212:213], v[196:197]\n"
  "v_pk_add_f32 v[190:191], v[190:191], v[182:183]\n"
  "v_pk_add_f32 v[214:215], v[214:215], v[206:207]\n"
  "v_pk_add_f32 v[188:189], v[188:189], v[174:175]\n"
  "v_pk_add_f32 v[212:213], v[212:213], v[198:199]\n"
  "v_pk_add_f32 v[190:191], v[190:191], v[184:185]\n"
  "v_pk_add_f32 v[214:215], v[214:215], v[208:209]\n"
  "v_pk_add_f32 v[188:189], v[188:189], v[176:177]\n"
  "v_pk_add_f32 v[212:213], v[212:213], v[200:201]\n"
  "v_pk_add_f32 v[190:191], v[190:191], v[186:187]\n"
  "v_pk_add_f32 v[214:215], v[214:215], v[210:211]\n"
  "v_add_f32 v188, v188, v189\n"
  "v_add_f32 v212, v212, v213\n"
  "v_add_f32 v190, v190, v191\n"
  "v_add_f32 v214, v214, v215\n"
  "s_nop 0\n"
  "v_permlane16_swap_b32 v188, v190\n"
  "s_add_u32 s11, s44, 0x50000\n"
  "s_add_u32 m0, s97, 0x23000\n"
  "s_nop 0\n"
  "buffer_load_dwordx4 v48, s[16:19], s11 offen lds\n"
  "v_permlane16_swap_b32 v212, v214\n"
  "v_add_f32 v188, v188, v190\n"
  "v_add_f32 v212, v212, v214\n"
  "v_rcp_f32 v188, v188\n"
  "v_rcp_f32 v212, v212\n"
  "v_mov_b32 v190, v188\n"
  "v_mov_b32 v214, v212\n"
  "s_nop 0\n"
  "v_permlane16_swap_b32 v188, v190\n"
  "v_permlane16_swap_b32 v212, v214\n"
  "v_pk_mul_f32 v[168:169], v[168:169], v[188:189] op_sel_hi:[1,0]\n"
  "v_pk_mul_f32 v[192:193], v[192:193], v[212:213] op_sel_hi:[1,0]\n"
  "v_pk_mul_f32 v[178:179], v[178:179], v[190:191] op_sel_hi:[1,0]\n"
  "v_pk_mul_f32 v[202:203], v[202:203], v[214:215] op_sel_hi:[1,0]\n"
  "v_pk_mul_f32 v[170:171], v[170:171], v[188:189] op_sel_hi:[1,0]\n"
  "v_pk_mul_f32 v[194:195], v[194:195], v[212:213] op_sel_hi:[1,0]\n"
  "v_pk_mul_f32 v[180:181], v[180:181], v[190:191] op_sel_hi:[1,0]\n"
  "v_pk_mul_f32 v[204:205], v[204:205], v[214:215] op_sel_hi:[1,0]\n"
  "s_add_u32 s11, s44, 0x50000\n"
  "s_add_u32 m0, s97, 0x24000\n"
  "s_nop 0\n"
  "buffer_load_dwordx4 v49, s[16:19], s11 offen lds\n"
  "v_pk_mul_f32 v[172:173], v[172:173], v[188:189] op_sel_hi:[1,0]\n"
  "v_pk_mul_f32 v[196:197], v[196:197], v[212:213] op_sel_hi:[1,0]\n"
  "v_pk_mul_f32 v[182:183], v[182:183], v[190:191] op_sel_hi:[1,0]\n"
  "v_pk_mul_f32 v[206:207], v[206:207], v[214:215] op_sel_hi:[1,0]\n"
  "v_pk_mul_f32 v[174:175], v[174:175], v[188:189] op_sel_hi:[1,0]\n"
  "v_pk_mul_f32 v[198:199], v[198:199], v[212:213] op_sel_hi:[1,0]\n"
  "v_pk_mul_f32 v[184:185], v[184:185], v[190:191] op_sel_hi:[1,0]\n"
  "v_pk_mul_f32 v[208:209], v[208:209], v[214:215] op_sel_hi:[1,0]\n"
  "v_pk_mul_f32 v[176:177], v[176:177], v[188:189] op_sel_hi:[1,0]\n"
  "v_pk_mul_f32 v[200:201], v[200:201], v[212:213] op_sel_hi:[1,0]\n"
  "v_pk_mul_f32 v[186:187], v[186:187], v[190:191] op_sel_hi:[1,0]\n"
  "v_pk_mul_f32 v[210:211], v[210:211], v[214:215] op_sel_hi:[1,0]\n"
  "v_cvt_pk_bf16_f32 v228, v168, v169\n"
  "v_cvt_pk_bf16_f32 v240, v192, v193\n"
  "v_cvt_pk_bf16_f32 v229, v170, v171\n"
  "v_cvt_pk_bf16_f32 v241, v194, v195\n"
  "s_add_u32 s11, s44, 0x50000\n"
  "s_mov_b32 m0, s45\n"
  "s_nop 0\n"
  "buffer_load_dwordx4 v50, s[16:19], s11 offen lds\n"
  "v_cvt_pk_bf16_f32 v230, v172, v173\n"
  "v_cvt_pk_bf16_f32 v242, v196, v197\n"
  "v_cvt_pk_bf16_f32 v231, v174, v175\n"
  "v_cvt_pk_bf16_f32 v243, v198, v199\n"
  "v_cvt_pk_bf16_f32 v232, v176, v177\n"
  "v_cvt_pk_bf16_f32 v244, v200, v201\n"
  "v_cvt_pk_bf16_f32 v233, v178, v179\n"
  "v_cvt_pk_bf16_f32 v245, v202, v203\n"
  "v_cvt_pk_bf16_f32 v234, v180, v181\n"
  "v_cvt_pk_bf16_f32 v246, v204, v205\n"
  "v_cvt_pk_bf16_f32 v235, v182, v183\n"
  "v_cvt_pk_bf16_f32 v247, v206, v207\n"
  "v_cvt_pk_bf16_f32 v236, v184, v185\n"
  "v_cvt_pk_bf16_f32 v248, v208, v209\n"
  "v_cvt_pk_bf16_f32 v237, v186, v187\n"
  "v_cvt_pk_bf16_f32 v249, v210, v211\n"
  "v_mov_b32 v238, 0\n"
  "v_mov_b32 v239, 0\n"
  "v_mov_b32 v250, 0\n"
  "v_mov_b32 v251, 0\n"
  "s_waitcnt vmcnt(25)\n"
  "v_dot2c_f32_bf16 v216, s20, v36\n"
  "v_dot2c_f32_bf16 v217, s21, v36\n"
  "v_dot2c_f32_bf16 v218, s20, v37\n"
  "v_dot2c_f32_bf16 v219, s21, v37\n"
  "v_dot2c_f32_bf16 v220, s20, v38\n"
  "v_dot2c_f32_bf16 v221, s21, v38\n"
  "v_dot2c_f32_bf16 v222, s20, v39\n"
  "v_dot2c_f32_bf16 v223, s21, v39\n"
  "s_waitcnt vmcnt(24)\n"
  "v_dot2c_f32_bf16 v216, s20, v40\n"
  "v_dot2c_f32_bf16 v217, s21, v40\n"
  "v_dot2c_f32_bf16 v218, s20, v41\n"
  "v_dot2c_f32_bf16 v219, s21, v41\n"
  "v_dot2c_f32_bf16 v220, s20, v42\n"
  "v_dot2c_f32_bf16 v221, s21, v42\n"
  "v_dot2c_f32_bf16 v222, s20, v43\n"
  "v_dot2c_f32_bf16 v223, s21, v43\n"
  "s_waitcnt vmcnt(23)\n"
  "v_dot2c_f32_bf16 v216, s20, v44\n"
  "v_dot2c_f32_bf16 v217, s21, v44\n"
  "v_dot2c_f32_bf16 v218, s20, v45\n"
  "v_dot2c_f32_bf16 v219, s21, v45\n"
  "v_dot2c_f32_bf16 v220, s20, v46\n"
  "v_dot2c_f32_bf16 v221, s21, v46\n"
  "v_dot2c_f32_bf16 v222, s20, v47\n"
  "v_dot2c_f32_bf16 v223, s21, v47\n"
  "s_waitcnt vmcnt(22)\n"
  "v_dot2c_f32_bf16 v216, s20, v56\n"
  "v_dot2c_f32_bf16 v217, s21, v56\n"
  "v_dot2c_f32_bf16 v218, s20, v57\n"
  "v_dot2c_f32_bf16 v219, s21, v57\n"
  "v_dot2c_f32_bf16 v220, s20, v58\n"
  "v_dot2c_f32_bf16 v221, s21, v58\n"
  "v_dot2c_f32_bf16 v222, s20, v59\n"
  "v_dot2c_f32_bf16 v223, s21, v59\n"
  "s_waitcnt vmcnt(21)\n"
  "v_dot2c_f32_bf16 v216, s20, v68\n"
  "v_dot2c_f32_bf16 v217, s21, v68\n"
  "v_dot2c_f32_bf16 v218, s20, v69\n"
  "v_dot2c_f32_bf16 v219, s21, v69\n"
  "v_dot2c_f32_bf16 v220, s20, v70\n"
  "v_dot2c_f32_bf16 v221, s21, v70\n"
  "v_dot2c_f32_bf16 v222, s20, v71\n"
  "v_dot2c_f32_bf16 v223, s21, v71\n"
  "s_waitcnt vmcnt(20)\n"
  "v_dot2c_f32_bf16 v216, s20, v72\n"
  "v_dot2c_f32_bf16 v217, s21, v72\n"
  "v_dot2c_f32_bf16 v218, s20, v73\n"
  "v_dot2c_f32_bf16 v219, s21, v73\n"
  "v_dot2c_f32_bf16 v220, s20, v74\n"
  "v_dot2c_f32_bf16 v221, s21, v74\n"
  "v_dot2c_f32_bf16 v222, s20, v75\n"
  "v_dot2c_f32_bf16 v223, s21, v75\n"
  "s_waitcnt vmcnt(19)\n"
  "v_dot2c_f32_bf16 v216, s20, v76\n"
  "v_dot2c_f32_bf16 v217, s21, v76\n"
  "v_dot2c_f32_bf16 v218, s20, v77\n"
  "v_dot2c_f32_bf16 v219, s21, v77\n"
  "v_dot2c_f32_bf16 v220, s20, v78\n"
  "v_dot2c_f32_bf16 v221, s21, v78\n"
  "v_dot2c_f32_bf16 v222, s20, v79\n"
  "v_dot2c_f32_bf16 v223, s21, v79\n"
  "s_waitcnt vmcnt(18)\n"
  "v_dot2c_f32_bf16 v216, s20, v80\n"
  "v_dot2c_f32_bf16 v217, s21, v80\n"
  "v_dot2c_f32_bf16 v218, s20, v81\n"
  "v_dot2c_f32_bf16 v219, s21, v81\n"
  "v_dot2c_f32_bf16 v220, s20, v82\n"
  "v_dot2c_f32_bf16 v221, s21, v82\n"
  "v_dot2c_f32_bf16 v222, s20, v83\n"
  "v_dot2c_f32_bf16 v223, s21, v83\n"
  "s_waitcnt vmcnt(17)\n"
  "v_dot2c_f32_bf16 v216, s20, v112\n"
  "v_dot2c_f32_bf16 v217, s21, v112\n"
  "v_dot2c_f32_bf16 v218, s20, v113\n"
  "v_dot2c_f32_bf16 v219, s21, v113\n"
  "v_dot2c_f32_bf16 v220, s20, v114\n"
  "v_dot2c_f32_bf16 v221, s21, v114\n"
  "v_dot2c_f32_bf16 v222, s20, v115\n"
  "v_dot2c_f32_bf16 v223, s21, v115\n"
  "s_waitcnt vmcnt(16)\n"
  "v_dot2c_f32_bf16 v216, s20, v116\n"
  "v_dot2c_f32_bf16 v217, s21, v116\n"
  "v_dot2c_f32_bf16 v218, s20, v117\n"
  "v_dot2c_f32_bf16 v219, s21, v117\n"
  "v_dot2c_f32_bf16 v220, s20, v118\n"
  "v_dot2c_f32_bf16 v221, s21, v118\n"
  "v_dot2c_f32_bf16 v222, s20, v119\n"
  "v_dot2c_f32_bf16 v223, s21, v119\n"
  "s_waitcnt vmcnt(15)\n"
  "v_dot2c_f32_bf16 v216, s20, v120\n"
  "v_dot2c_f32_bf16 v217, s21, v120\n"
  "v_dot2c_f32_bf16 v218, s20, v121\n"
  "v_dot2c_f32_bf16 v219, s21, v121\n"
  "v_dot2c_f32_bf16 v220, s20, v122\n"
  "v_dot2c_f32_bf16 v221, s21, v122\n"
  "v_dot2c_f32_bf16 v222, s20, v123\n"
  "v_dot2c_f32_bf16 v223, s21, v123\n"
  "s_waitcnt vmcnt(14)\n"
  "v_dot2c_f32_bf16 v216, s20, v128\n"
  "v_dot2c_f32_bf16 v217, s21, v128\n"
  "v_dot2c_f32_bf16 v218, s20, v129\n"
  "v_dot2c_f32_bf16 v219, s21, v129\n"
  "v_dot2c_f32_bf16 v220, s20, v130\n"
  "v_dot2c_f32_bf16 v221, s21, v130\n"
  "v_dot2c_f32_bf16 v222, s20, v131\n"
  "v_dot2c_f32_bf16 v223, s21, v131\n"
  "s_waitcnt vmcnt(13)\n"
  "v_dot2c_f32_bf16 v216, s20, v132\n"
  "v_dot2c_f32_bf16 v217, s21, v132\n"
  "v_dot2c_f32_bf16 v218, s20, v133\n"
  "v_dot2c_f32_bf16 v219, s21, v133\n"
  "v_dot2c_f32_bf16 v220, s20, v134\n"
  "v_dot2c_f32_bf16 v221, s21, v134\n"
  "v_dot2c_f32_bf16 v222, s20, v135\n"
  "v_dot2c_f32_bf16 v223, s21, v135\n"
  "s_waitcnt vmcnt(12)\n"
  "v_dot2c_f32_bf16 v216, s20, v136\n"
  "v_dot2c_f32_bf16 v217, s21, v136\n"
  "v_dot2c_f32_bf16 v218, s20, v137\n"
  "v_dot2c_f32_bf16 v219, s21, v137\n"
  "v_dot2c_f32_bf16 v220, s20, v138\n"
  "v_dot2c_f32_bf16 v221, s21, v138\n"
  "v_dot2c_f32_bf16 v222, s20, v139\n"
  "v_dot2c_f32_bf16 v223, s21, v139\n"
  "s_waitcnt vmcnt(11)\n"
  "v_dot2c_f32_bf16 v216, s20, v140\n"
  "v_dot2c_f32_bf16 v217, s21, v140\n"
  "v_dot2c_f32_bf16 v218, s20, v141\n"
  "v_dot2c_f32_bf16 v219, s21, v141\n"
  "v_dot2c_f32_bf16 v220, s20, v142\n"
  "v_dot2c_f32_bf16 v221, s21, v142\n"
  "v_dot2c_f32_bf16 v222, s20, v143\n"
  "v_dot2c_f32_bf16 v223, s21, v143\n"
  "s_waitcnt vmcnt(10)\n"
  "v_dot2c_f32_bf16 v216, s20, v144\n"
  "v_dot2c_f32_bf16 v217, s21, v144\n"
  "v_dot2c_f32_bf16 v218, s20, v145\n"
  "v_dot2c_f32_bf16 v219, s21, v145\n"
  "v_dot2c_f32_bf16 v220, s20, v146\n"
  "v_dot2c_f32_bf16 v221, s21, v146\n"
  "v_dot2c_f32_bf16 v222, s20, v147\n"
  "v_dot2c_f32_bf16 v223, s21, v147\n"
  "v_add_u32 v9, 0x25800, v9\n"
  "v_cvt_pk_bf16_f32 v36, v216, v217\n"
  "v_cvt_pk_bf16_f32 v37, v218, v219\n"
  "v_cvt_pk_bf16_f32 v38, v220, v221\n"
  "v_cvt_pk_bf16_f32 v39, v222, v223\n"
  "ds_write_b128 v9, v[36:39]\n"
  "v_mov_b32 v78, 0\n"
  "v_mov_b32 v79, 0\n"
  "v_mov_b32 v98, 0\n"
  "v_mov_b32 v99, 0\n"
  "v_mov_b32 v110, 0\n"
  "v_mov_b32 v111, 0\n"
  "s_lshl_b32 s67, s40, 2\n"
  "s_add_u32 s67, s67, s25\n"
  "s_mul_i32 s28, s67, 0xa000\n"
  "s_mul_i32 s67, s42, 0x500\n"
  "s_add_u32 s28, s28, s67\n"
  "s_add_u32 s28, s28, 0x120000\n"
  "s_waitcnt lgkmcnt(0)\n"
  "s_waitcnt vmcnt(0)\n"
  "s_barrier\n"
  "ds_read_b128 v[100:103], v28\n"
  "ds_read_b128 v[68:71], v14\n"
  "ds_read_b128 v[88:91], v26\n"
  "ds_read_b128 v[104:107], v28 offset:768\n"
  "ds_read_b128 v[72:75], v14 offset:768\n"
  "ds_read_b128 v[92:95], v26 offset:768\n"
  "ds_read_b64 v[108:109], v34 offset:1536\n"
  "ds_read_b64 v[76:77], v23 offset:1536\n"
  "ds_read_b64 v[96:97], v27 offset:1536\n"
  "s_add_u32 s29, s28, 0x5000\n"
  "v_lshl_add_u32 v3, v20, 1, v16\n"
  "v_lshlrev_b32 v4, 4, v15\n"
  "v_lshl_add_u32 v35, v3, 8, v4\n"
  "v_mul_u32_u24 v3, 0x5000, v20\n"
  "v_add_u32 v3, v3, v4\n"
  "v_cndmask_b32 v53, v3, v22, s[74:75]\n"
  "s_waitcnt lgkmcnt(8)\n"
  "v_mfma_f32_16x16x32_bf16 v[188:191], v[100:103], v[228:231], 0\n"
  "v_mfma_f32_16x16x32_bf16 v[212:215], v[100:103], v[240:243], 0\n"
  "s_waitcnt lgkmcnt(7)\n"
  "v_mfma_f32_16x16x32_bf16 v[180:183], v[68:71], v[228:231], 0\n"
  "v_mfma_f32_16x16x32_bf16 v[204:207], v[68:71], v[240:243], 0\n"
  "s_waitcnt lgkmcnt(6)\n"
  "v_mfma_f32_16x16x32_bf16 v[184:187], v[88:91], v[228:231], 0\n"
  "v_mfma_f32_16x16x32_bf16 v[208:211], v[88:91], v[240:243], 0\n"
  "s_waitcnt lgkmcnt(5)\n"
  "v_mfma_f32_16x16x32_bf16 v[188:191], v[104:107], v[232:235], v[188:191]\n"
  "v_mfma_f32_16x16x32_bf16 v[212:215], v[104:107], v[244:247], v[212:215]\n"
  "s_waitcnt lgkmcnt(4)\n"
  "v_mfma_f32_16x16x32_bf16 v[180:183], v[72:75], v[232:235], v[180:183]\n"
  "v_mfma_f32_16x16x32_bf16 v[204:207], v[72:75], v[244:247], v[204:207]\n"
  "s_waitcnt lgkmcnt(3)\n"
  "v_mfma_f32_16x16x32_bf16 v[184:187], v[92:95], v[232:235], v[184:187]\n"
  "v_mfma_f32_16x16x32_bf16 v[208:211], v[92:95], v[244:247], v[208:211]\n"
  "s_waitcnt lgkmcnt(2)\n"
  "v_mfma_f32_16x16x32_bf16 v[188:191], v[108:111], v[236:239], v[188:191]\n"
  "v_mfma_f32_16x16x32_bf16 v[212:215], v[108:111], v[248:251], v[212:215]\n"
  "s_waitcnt lgkmcnt(1)\n"
  "v_mfma_f32_16x16x32_bf16 v[180:183], v[76:79], v[236:239], v[180:183]\n"
  "v_mfma_f32_16x16x32_bf16 v[204:207], v[76:79], v[248:251], v[204:207]\n"
  "s_waitcnt lgkmcnt(0)\n"
  "v_mfma_f32_16x16x32_bf16 v[184:187], v[96:99], v[236:239], v[184:187]\n"
  "v_mfma_f32_16x16x32_bf16 v[208:211], v[96:99], v[248:251], v[208:211]\n"
  "s_nop 0\n"
  "v_add_f32 v168, 0x358637bd, v190\n"
  "v_add_f32 v192, 0x358637bd, v214\n"
  "v_rcp_f32 v168, v168\n"
  "v_rcp_f32 v192, v192\n"
  "v_pk_mul_f32 v[188:189], v[188:189], v[168:169] op_sel_hi:[1,0]\n"
  "v_pk_mul_f32 v[212:213], v[212:213], v[192:193] op_sel_hi:[1,0]\n"
  "v_pk_mul_f32 v[180:181], v[180:181], v[168:169] op_sel_hi:[1,0]\n"
  "v_pk_mul_f32 v[204:205], v[204:205], v[192:193] op_sel_hi:[1,0]\n"
  "v_pk_mul_f32 v[182:183], v[182:183], v[168:169] op_sel_hi:[1,0]\n"
  "v_pk_mul_f32 v[206:207], v[206:207], v[192:193] op_sel_hi:[1,0]\n"
  "v_pk_mul_f32 v[184:185], v[184:185], v[168:169] op_sel_hi:[1,0]\n"
  "v_pk_mul_f32 v[208:209], v[208:209], v[192:193] op_sel_hi:[1,0]\n"
  "v_pk_mul_f32 v[186:187], v[186:187], v[168:169] op_sel_hi:[1,0]\n"
  "v_pk_mul_f32 v[210:211], v[210:211], v[192:193] op_sel_hi:[1,0]\n"
  "v_cvt_pk_bf16_f32 v238, v188, v189\n"
  "v_cvt_pk_bf16_f32 v239, v212, v213\n"
  "v_cvt_pk_bf16_f32 v172, v180, v181\n"
  "v_cvt_pk_bf16_f32 v196, v204, v205\n"
  "v_cvt_pk_bf16_f32 v173, v182, v183\n"
  "v_cvt_pk_bf16_f32 v197, v206, v207\n"
  "v_cvt_pk_bf16_f32 v174, v184, v185\n"
  "v_cvt_pk_bf16_f32 v198, v208, v209\n"
  "v_cvt_pk_bf16_f32 v175, v186, v187\n"
  "v_cvt_pk_bf16_f32 v199, v210, v211\n"
  "buffer_store_dwordx4 v[172:175], v35, s[12:15], s28 offen sc1\n"
  "buffer_store_dwordx4 v[196:199], v35, s[12:15], s29 offen sc1\n"
  "v_permlane16_swap_b32 v238, v239\n"
  "v_mov_b32 v236, v238\n"
  "v_mov_b32 v237, v239\n"
  "s_nop 0\n"
  "v_permlane32_swap_b32 v238, v236\n"
  "v_permlane32_swap_b32 v239, v237\n"
  "buffer_store_dwordx4 v[236:239], v53, s[12:15], s28 offen offset:1024 sc1\n"
  "v_accvgpr_read_b32 v168, a160\n"
  "v_accvgpr_read_b32 v169, a161\n"
  "v_accvgpr_read_b32 v170, a162\n"
  "v_accvgpr_read_b32 v171, a163\n"
  "v_accvgpr_read_b32 v172, a164\n"
  "v_accvgpr_read_b32 v173, a165\n"
  "v_accvgpr_read_b32 v174, a166\n"
  "v_accvgpr_read_b32 v175, a167\n"
  "v_accvgpr_read_b32 v176, a168\n"
  "v_accvgpr_read_b32 v177, a169\n"
  "v_accvgpr_read_b32 v178, a170\n"
  "v_accvgpr_read_b32 v179, a171\n"
  "v_accvgpr_read_b32 v180, a172\n"
  "v_accvgpr_read_b32 v181, a173\n"
  "v_accvgpr_read_b32 v182, a174\n"
  "v_accvgpr_read_b32 v183, a175\n"
  "v_accvgpr_read_b32 v184, a176\n"
  "v_accvgpr_read_b32 v185, a177\n"
  "v_accvgpr_read_b32 v186, a178\n"
  "v_accvgpr_read_b32 v187, a179\n"
  "s_mov_b32 s67, 0x3f803f80\n"
  "v_mov_b32 v240, 0\n"
  "v_mov_b32 v241, 0\n"
  "v_mov_b32 v242, 0\n"
  "v_dot2c_f32_bf16 v240, s67, v168\n"
  "v_dot2c_f32_bf16 v241, s67, v176\n"
  "v_dot2c_f32_bf16 v242, s67, v184\n"
  "v_dot2c_f32_bf16 v240, s67, v172\n"
  "v_dot2c_f32_bf16 v241, s67, v180\n"
  "v_dot2c_f32_bf16 v240, s67, v169\n"
  "v_dot2c_f32_bf16 v241, s67, v177\n"
  "v_dot2c_f32_bf16 v242, s67, v185\n"
  "v_dot2c_f32_bf16 v240, s67, v173\n"
  "v_dot2c_f32_bf16 v241, s67, v181\n"
  "v_dot2c_f32_bf16 v240, s67, v170\n"
  "v_dot2c_f32_bf16 v241, s67, v178\n"
  "v_dot2c_f32_bf16 v242, s67, v186\n"
  "v_dot2c_f32_bf16 v240, s67, v174\n"
  "v_dot2c_f32_bf16 v241, s67, v182\n"
  "v_dot2c_f32_bf16 v240, s67, v171\n"
  "v_dot2c_f32_bf16 v241, s67, v179\n"
  "v_dot2c_f32_bf16 v242, s67, v187\n"
  "v_dot2c_f32_bf16 v240, s67, v175\n"
  "v_dot2c_f32_bf16 v241, s67, v183\n"
  "s_nop 0\n"
  "v_cndmask_b32 v243, v242, 0, s[74:75]\n"
  "v_cndmask_b32 v242, 0, v242, s[74:75]\n"
  "v_add_f32 v240, v240, v243\n"
  "v_add_f32 v241, v241, v242\n"
  "v_mov_b32 v242, v240\n"
  "v_mov_b32 v243, v241\n"
  "s_nop 0\n"
  "v_permlane16_swap_b32 v240, v242\n"
  "v_permlane16_swap_b32 v241, v243\n"
  "v_add_f32 v240, v240, v242\n"
  "v_add_f32 v241, v241, v243\n"
  "v_mov_b32 v242, v240\n"
  "v_mov_b32 v243, v241\n"
  "s_nop 0\n"
  "v_permlane32_swap_b32 v240, v242\n"
  "v_permlane32_swap_b32 v241, v243\n"
  "v_add_f32 v240, v240, v242\n"
  "v_add_f32 v241, v241, v243\n"
  "v_cndmask_b32 v240, v240, v241, s[76:77]\n"
  "buffer_store_dword v240, v33, s[12:15], s54 offen sc1\n"
  "s_lshr_b32 s8, s25, 1\n"
  "s_and_b32 s10, s25, 1\n"
  "s_lshl_b32 s67, s43, 2\n"
  "s_lshl_b32 s68, s10, 1\n"
  "s_add_u32 s67, s67, s68\n"
  "s_add_u32 s16, s67, s8\n"
  "s_xor_b32 s17, s16, 1\n"
  "v_mul_u32_u24 v9, 0x500, v15\n"
  "v_mad_u32_u24 v9, v19, 40, v9\n"
  "s_lshl_b32 s67, s40, 3\n"
  "s_add_u32 s67, s67, s16\n"
  "s_mul_i32 s18, s67, 0x5000\n"
  "s_mul_i32 s67, s62, 0xa0\n"
  "s_add_u32 s18, s18, s67\n"
  "buffer_load_dwordx4 v[36:39], v9, s[4:7], s18 offen\n"
  "buffer_load_dwordx4 v[40:43], v9, s[4:7], s18 offen offset:16\n"
  "buffer_load_dwordx2 v[44:45], v9, s[4:7], s18 offen offset:32\n"
  "s_mul_i32 s67, s8, 0xffff3800\n"
  "s_add_u32 s67, s67, 0x19000\n"
  "v_mul_u32_u24 v3, 0xa0, v17\n"
  "v_mad_u32_u24 v3, v18, 40, v3\n"
  "v_add_u32 v3, s67, v3\n"
  "v_lshlrev_b32 v4, 3, v19\n"
  "v_mov_b32 v6, 0x280\n"
  "s_mov_b32 s68, 0x7020500\n"
  "v_bfe_u32 v5, s68, v4, 8\n"
  "v_mad_u32_u24 v12, v5, v6, v3\n"
  "s_mov_b32 s68, 0x8030601\n"
  "v_bfe_u32 v5, s68, v4, 8\n"
  "v_mad_u32_u24 v13, v5, v6, v3\n"
  "s_mov_b32 s68, 0xc0f0a09\n"
  "v_bfe_u32 v5, s68, v4, 8\n"
  "v_mad_u32_u24 v14, v5, v6, v3\n"
  "s_mov_b32 s68, 0xd100b04\n"
  "v_bfe_u32 v5, s68, v4, 8\n"
  "v_mad_u32_u24 v23, v5, v6, v3\n"
  "s_mov_b32 s68, 0x19141311\n"
  "v_bfe_u32 v5, s68, v4, 8\n"
  "v_mad_u32_u24 v24, v5, v6, v3\n"
  "s_mov_b32 s68, 0x1a150e12\n"
  "v_bfe_u32 v5, s68, v4, 8\n"
  "v_mad_u32_u24 v25, v5, v6, v3\n"
  "s_mov_b32 s68, 0x1e1d1b16\n"
  "v_bfe_u32 v5, s68, v4, 8\n"
  "v_mad_u32_u24 v26, v5, v6, v3\n"
  "s_mov_b32 s68, 0x1f181c17\n"
  "v_bfe_u32 v5, s68, v4, 8\n"
  "v_mad_u32_u24 v27, v5, v6, v3\n"
  "s_mov_b32 s68, 0x27252023\n"
  "v_bfe_u32 v5, s68, v4, 8\n"
  "v_mad_u32_u24 v28, v5, v6, v3\n"
  "s_mov_b32 s68, 0x22262124\n"
  "v_bfe_u32 v5, s68, v4, 8\n"
  "v_mad_u32_u24 v29, v5, v6, v3\n"
  "v_and_b32 v30, 7, v1\n"
  "v_lshlrev_b32 v30, 5, v30\n"
  "s_lshl_b32 s4, s40, 9\n"
  "s_lshl_b32 s67, s8, 8\n"
  "s_add_u32 s4, s4, s67\n"
  "s_lshl_b32 s67, s43, 1\n"
  "s_add_u32 s67, s67, s10\n"
  "s_lshl_b32 s67, s67, 3\n"
  "s_add_u32 s4, s4, s67\n"
  "s_lshl_b32 s4, s4, 0\n"
  "s_add_u32 s48, s4, 0x3000\n"
  "s_lshl_b32 s67, s40, 3\n"
  "s_add_u32 s67, s67, s16\n"
  "s_mul_i32 s5, s67, 0x5000\n"
  "s_mul_i32 s67, s8, 0x2800\n"
  "s_add_u32 s5, s5, s67\n"
  "s_add_u32 s5, s5, 0x120000\n"
  "s_lshl_b32 s67, s40, 3\n"
  "s_add_u32 s67, s67, s17\n"
  "s_mul_i32 s6, s67, 0x5000\n"
  "s_mul_i32 s67, s8, 0x2800\n"
  "s_add_u32 s6, s6, s67\n"
  "s_add_u32 s6, s6, 0x120000\n"
  "v_lshrrev_b32 v3, 2, v1\n"
  "v_min_u32 v4, 2, v18\n"
  "v_lshlrev_b32 v4, 3, v4\n"
  "v_lshl_add_u32 v33, v3, 5, v4\n"
  "v_lshlrev_b32 v4, 11, v20\n"
  "v_lshl_add_u32 v34, v15, 5, v4\n"
  "s_lshl_b32 s7, s41, 9\n"
  "s_add_u32 s7, s7, 0x5000\n"
  "s_and_b32 s19, s41, 11\n"
  "s_lshl_b32 s19, s19, 9\n"
  "s_add_u32 s19, s19, 0x11018\n"
  "s_mul_i32 s67, s10, 0x1400\n"
  "s_add_u32 s68, s67, 0xa000\n"
  "s_add_u32 s67, s67, 0x25800\n"
  "s_cmp_eq_u32 s8, 0\n"
  "s_cselect_b32 s69, s68, s67\n"
  "s_cselect_b32 s70, s67, s68\n"
  "v_add_u32 v35, s69, v21\n"
  "v_add_u32 v66, s70, v21\n"
  "s_lshl_b32 s67, s40, 3\n"
  "s_add_u32 s67, s67, s16\n"
  "s_mul_i32 s20, s67, 0x5000\n"
  "s_lshr_b32 s67, s62, 1\n"
  "s_mul_i32 s67, s67, 0x1400\n"
  "s_add_u32 s20, s20, s67\n"
  "s_add_u32 s20, s20, 0x3a0000\n"
  "s_and_b32 s67, s62, 1\n"
  "s_lshl_b32 s68, s67, 9\n"
  "s_add_u32 s21, s20, s68\n"
  "s_add_u32 s21, s21, 0x1000\n"
  "s_lshl_b32 s67, s67, 11\n"
  "s_add_u32 s20, s20, s67\n"
  "s_lshl_b32 s67, s40, 3\n"
  "s_add_u32 s67, s67, s62\n"
  "s_lshl_b32 s67, s67, 10\n"
  "s_lshl_b32 s68, s16, 7\n"
  "s_add_u32 s67, s67, s68\n"
  "s_add_u32 s22, s67, 0x620000\n"
  "v_lshlrev_b32 v3, 4, v15\n"
  "v_lshl_add_u32 v3, v16, 8, v3\n"
  "v_cndmask_b32 v67, v3, v22, s[76:77]\n"
  "v_cmp_gt_u32 vcc, 16, v1\n"
  "v_lshlrev_b32 v3, 3, v1\n"
  "s_nop 0\n"
  "v_cndmask_b32 v92, v22, v3, vcc\n"
  "s_mul_i32 s23, s41, 0x3c000\n"
  "s_add_u32 s23, s23, s9\n"
  "s_add_u32 s23, s23, 0x658000\n"
  "s_mul_i32 s26, s62, 0x3c000\n"
  "s_add_u32 s26, s26, s9\n"
  "s_add_u32 s26, s26, 0xa18000\n"
  "s_add_u32 s64, s65, 0x3000\n"
  "s_waitcnt vmcnt(4)\n"
  "s_mov_b64 exec, 1\n"
  "buffer_store_dwordx2 v[10:11], off, s[12:15], s64 sc1\n"
  "s_mov_b64 exec, -1\n"
  "s_add_u32 s64, s66, 0x5000\n"
  "s_waitcnt vmcnt(4)\n"
  "s_mov_b64 exec, 1\n"
  "buffer_store_dwordx2 v[10:11], off, s[12:15], s64 sc1\n"
  "s_mov_b64 exec, -1\n"
  "s_add_u32 s67, s40, s41\n"
  "s_add_u32 s68, s40, s62\n"
  "s_mov_b32 s67, 0\n"
  "s_mov_b32 s68, 0\n"
  "s_add_u32 s69, s67, 0\n"
  "s_and_b32 s69, s69, 3\n"
  "s_mul_i32 s27, s69, 0x1400\n"
  "s_add_u32 s69, s68, 0\n"
  "s_and_b32 s69, s69, 3\n"
  "s_mul_i32 s31, s69, 0x800\n"
  "s_add_u32 s69, s67, 1\n"
  "s_and_b32 s69, s69, 3\n"
  "s_mul_i32 s28, s69, 0x1400\n"
  "s_add_u32 s69, s68, 1\n"
  "s_and_b32 s69, s69, 3\n"
  "s_mul_i32 s36, s69, 0x800\n"
  "s_add_u32 s69, s67, 2\n"
  "s_and_b32 s69, s69, 3\n"
  "s_mul_i32 s29, s69, 0x1400\n"
  "s_add_u32 s69, s68, 2\n"
  "s_and_b32 s69, s69, 3\n"
  "s_mul_i32 s37, s69, 0x800\n"
  "s_add_u32 s69, s67, 3\n"
  "s_and_b32 s69, s69, 3\n"
  "s_mul_i32 s30, s69, 0x1400\n"
  "s_add_u32 s69, s68, 3\n"
  "s_and_b32 s69, s69, 3\n"
  "s_mul_i32 s38, s69, 0x800\n"
  "s_add_u32 s69, s67, s8\n"
  "s_add_u32 s69, s69, 0\n"
  "s_and_b32 s69, s69, 3\n"
  "s_mul_i32 s69, s69, 0x2800\n"
  "s_mul_i32 s70, s10, 0x1400\n"
  "s_add_u32 s69, s69, s70\n"
  "s_sub_u32 s69, s69, s9\n"
  "s_add_u32 s42, s69, s23\n"
  "s_add_u32 s69, s67, s8\n"
  "s_add_u32 s69, s69, 2\n"
  "s_and_b32 s69, s69, 3\n"
  "s_mul_i32 s69, s69, 0x2800\n"
  "s_mul_i32 s70, s10, 0x1400\n"
  "s_add_u32 s69, s69, s70\n"
  "s_sub_u32 s69, s69, s9\n"
  "s_add_u32 s44, s69, s23\n"
  "ds_read_b64_tr_b16 v[128:129], v12\n"
  "ds_read_b64_tr_b16 v[130:131], v13\n"
  "ds_read_b64_tr_b16 v[132:133], v12 offset:8\n"
  "ds_read_b64_tr_b16 v[134:135], v13 offset:8\n"
  "ds_read_b64_tr_b16 v[136:137], v12 offset:16\n"
  "ds_read_b64_tr_b16 v[138:139], v13 offset:16\n"
  "ds_read_b64_tr_b16 v[140:141], v12 offset:24\n"
  "ds_read_b64_tr_b16 v[142:143], v13 offset:24\n"
  "ds_read_b64_tr_b16 v[144:145], v12 offset:32\n"
  "ds_read_b64_tr_b16 v[146:147], v13 offset:32\n"
  "s_waitcnt vmcnt(4)\n"
  "v_lshlrev_b32 v108, 16, v36\n"
  "v_and_b32 v109, 0xffff0000, v36\n"
  "v_lshlrev_b32 v110, 16, v37\n"
  "v_and_b32 v111, 0xffff0000, v37\n"
  "v_lshlrev_b32 v112, 16, v38\n"
  "v_and_b32 v113, 0xffff0000, v38\n"
  "v_lshlrev_b32 v114, 16, v39\n"
  "v_and_b32 v115, 0xffff0000, v39\n"
  "s_waitcnt vmcnt(3)\n"
  "v_lshlrev_b32 v116, 16, v40\n"
  "v_and_b32 v117, 0xffff0000, v40\n"
  "v_lshlrev_b32 v118, 16, v41\n"
  "v_and_b32 v119, 0xffff0000, v41\n"
  "v_lshlrev_b32 v120, 16, v42\n"
  "v_and_b32 v121, 0xffff0000, v42\n"
  "v_lshlrev_b32 v122, 16, v43\n"
  "v_and_b32 v123, 0xffff0000, v43\n"
  "s_waitcnt vmcnt(2)\n"
  "v_lshlrev_b32 v124, 16, v44\n"
  "v_and_b32 v125, 0xffff0000, v44\n"
  "v_lshlrev_b32 v126, 16, v45\n"
  "v_and_b32 v127, 0xffff0000, v45\n"
  "s_mov_b32 s63, 0x10000\n"
  "buffer_load_dwordx2 v[64:65], v30, s[12:15], s48 offen sc1\n"
  "s_sleep 2\n"
  ".Lhh_poll_11:\n"
  "buffer_load_dwordx2 v[36:37], v30, s[12:15], s48 offen sc1\n"
  "s_waitcnt vmcnt(1)\n"
  "v_cmp_ne_u64 vcc, s[72:73], v[64:65]\n"
  "s_nop 4\n"
  "s_cbranch_vccz .Lhh_done_12\n"
  "s_sleep 1\n"
  "buffer_load_dwordx2 v[64:65], v30, s[12:15], s48 offen sc1\n"
  "s_waitcnt vmcnt(1)\n"
  "v_cmp_ne_u64 vcc, s[72:73], v[36:37]\n"
  "s_nop 4\n"
  "s_cbranch_vccz .Lhh_done_12\n"
  "s_sleep 1\n"
  "s_sub_u32 s63, s63, 1\n"
  "s_cbranch_scc0 .Lhh_poll_11\n"
  ".Lhh_done_12:\n"
  "; polled\n"
  "; flag read in flight\n"
  "buffer_load_dwordx4 a[0:3], v21, s[12:15], s5 offen sc1\n"
  "buffer_load_dwordx4 a[4:7], v21, s[12:15], s6 offen sc1\n"
  "buffer_load_dwordx4 a[8:11], v21, s[12:15], s5 offen offset:1024 sc1\n"
  "buffer_load_dwordx4 a[12:15], v21, s[12:15], s6 offen offset:1024 sc1\n"
  "buffer_load_dwordx4 a[16:19], v21, s[12:15], s5 offen offset:2048 sc1\n"
  "buffer_load_dwordx4 a[20:23], v21, s[12:15], s6 offen offset:2048 sc1\n"
  "buffer_load_dwordx4 a[24:27], v21, s[12:15], s5 offen offset:3072 sc1\n"
  "buffer_load_dwordx4 a[28:31], v21, s[12:15], s6 offen offset:3072 sc1\n"
  "s_add_u32 s5, s5, 0x1000\n"
  "s_add_u32 s6, s6, 0x1000\n"
  "buffer_load_dwordx4 a[32:35], v21, s[12:15], s5 offen sc1\n"
  "buffer_load_dwordx4 a[36:39], v21, s[12:15], s6 offen sc1\n"
  "buffer_load_dwordx4 a[40:43], v21, s[12:15], s5 offen offset:1024 sc1\n"
  "buffer_load_dwordx4 a[44:47], v21, s[12:15], s6 offen offset:1024 sc1\n"
  "s_waitcnt vmcnt(11) lgkmcnt(8)\n"
  "v_mfma_f32_16x16x32_bf16 v[48:51], v[128:131], a[0:3], 0\n"
  "ds_read_b64_tr_b16 v[148:149], v14\n"
  "buffer_load_dwordx4 a[48:51], v21, s[12:15], s5 offen offset:2048 sc1\n"
  "s_waitcnt vmcnt(11)\n"
  "v_mfma_f32_16x16x32_bf16 v[72:75], v[128:131], a[4:7], 0\n"
  "ds_read_b64_tr_b16 v[150:151], v23\n"
  "s_waitcnt lgkmcnt(8)\n"
  "v_mfma_f32_16x16x32_bf16 v[52:55], v[132:135], a[0:3], 0\n"
  "ds_read_b64_tr_b16 v[152:153], v14 offset:8\n"
  "v_mfma_f32_16x16x32_bf16 v[76:79], v[132:135], a[4:7], 0\n"
  "ds_read_b64_tr_b16 v[154:155], v23 offset:8\n"
  "s_waitcnt lgkmcnt(8)\n"
  "v_mfma_f32_16x16x32_bf16 v[56:59], v[136:139], a[0:3], 0\n"
  "ds_read_b64_tr_b16 v[156:157], v14 offset:16\n"
  "v_mfma_f32_16x16x32_bf16 v[80:83], v[136:139], a[4:7], 0\n"
  "ds_read_b64_tr_b16 v[158:159], v23 offset:16\n"
  "buffer_load_dwordx4 a[52:55], v21, s[12:15], s6 offen offset:2048 sc1\n"
  "s_waitcnt lgkmcnt(8)\n"
  "v_mfma_f32_16x16x32_bf16 v[60:63], v[140:143], a[0:3], 0\n"
  "ds_read_b64_tr_b16 v[160:161], v14 offset:24\n"
  "v_mfma_f32_16x16x32_bf16 v[84:87], v[140:143], a[4:7], 0\n"
  "ds_read_b64_tr_b16 v[162:163], v23 offset:24\n"
  "s_waitcnt lgkmcnt(8)\n"
  "v_mfma_f32_16x16x32_bf16 v[68:71], v[144:147], a[0:3], 0\n"
  "ds_read_b64_tr_b16 v[164:165], v14 offset:32\n"
  "v_mfma_f32_16x16x32_bf16 v[88:91], v[144:147], a[4:7], 0\n"
  "ds_read_b64_tr_b16 v[166:167], v23 offset:32\n"
  "s_waitcnt vmcnt(11) lgkmcnt(8)\n"
  "v_mfma_f32_16x16x32_bf16 v[48:51], v[148:151], a[8:11], v[48:51]\n"
  "ds_read_b64_tr_b16 v[128:129], v24\n"
  "buffer_load_dwordx4 a[56:59], v21, s[12:15], s5 offen offset:3072 sc1\n"
  "s_waitcnt vmcnt(11)\n"
  "v_mfma_f32_16x16x32_bf16 v[72:75], v[148:151], a[12:15], v[72:75]\n"
  "ds_read_b64_tr_b16 v[130:131], v25\n"
  "s_waitcnt lgkmcnt(8)\n"
  "v_mfma_f32_16x16x32_bf16 v[52:55], v[152:155], a[8:11], v[52:55]\n"
  "ds_read_b64_tr_b16 v[132:133], v24 offset:8\n"
  "v_mfma_f32_16x16x32_bf16 v[76:79], v[152:155], a[12:15], v[76:79]\n"
  "ds_read_b64_tr_b16 v[134:135], v25 offset:8\n"
  "s_waitcnt lgkmcnt(8)\n"
  "v_mfma_f32_16x16x32_bf16 v[56:59], v[156:159], a[8:11], v[56:59]\n"
  "ds_read_b64_tr_b16 v[136:137], v24 offset:16\n"
  "v_mfma_f32_16x16x32_bf16 v[80:83], v[156:159], a[12:15], v[80:83]\n"
  "ds_read_b64_tr_b16 v[138:139], v25 offset:16\n"
  "buffer_load_dwordx4 a[60:63], v21, s[12:15], s6 offen offset:3072 sc1\n"
  "s_waitcnt lgkmcnt(8)\n"
  "v_mfma_f32_16x16x32_bf16 v[60:63], v[160:163], a[8:11], v[60:63]\n"
  "ds_read_b64_tr_b16 v[140:141], v24 offset:24\n"
  "v_mfma_f32_16x16x32_bf16 v[84:87], v[160:163], a[12:15], v[84:87]\n"
  "ds_read_b64_tr_b16 v[142:143], v25 offset:24\n"
  "s_waitcnt lgkmcnt(8)\n"
  "v_mfma_f32_16x16x32_bf16 v[68:71], v[164:167], a[8:11], v[68:71]\n"
  "ds_read_b64_tr_b16 v[144:145], v24 offset:32\n"
  "v_mfma_f32_16x16x32_bf16 v[88:91], v[164:167], a[12:15], v[88:91]\n"
  "ds_read_b64_tr_b16 v[146:147], v25 offset:32\n"
  "s_waitcnt vmcnt(11) lgkmcnt(8)\n"
  "v_mfma_f32_16x16x32_bf16 v[48:51], v[128:131], a[16:19], v[48:51]\n"
  "ds_read_b64_tr_b16 v[148:149], v26\n"
  "s_add_u32 s5, s5, 0x1000\n"
  "s_waitcnt vmcnt(10)\n"
  "v_mfma_f32_16x16x32_bf16 v[72:75], v[128:131], a[20:23], v[72:75]\n"
  "ds_read_b64_tr_b16 v[150:151], v27\n"
  "s_waitcnt lgkmcnt(8)\n"
  "v_mfma_f32_16x16x32_bf16 v[52:55], v[132:135], a[16:19], v[52:55]\n"
  "ds_read_b64_tr_b16 v[152:153], v26 offset:8\n"
  "s_add_u32 s6, s6, 0x1000\n"
  "v_mfma_f32_16x16x32_bf16 v[76:79], v[132:135], a[20:23], v[76:79]\n"
  "ds_read_b64_tr_b16 v[154:155], v27 offset:8\n"
  "s_waitcnt lgkmcnt(8)\n"
  "v_mfma_f32_16x16x32_bf16 v[56:59], v[136:139], a[16:19], v[56:59]\n"
  "ds_read_b64_tr_b16 v[156:157], v26 offset:16\n"
  "v_mfma_f32_16x16x32_bf16 v[80:83], v[136:139], a[20:23], v[80:83]\n"
  "ds_read_b64_tr_b16 v[158:159], v27 offset:16\n"
  "buffer_load_dwordx4 a[64:67], v21, s[12:15], s5 offen sc1\n"
  "s_waitcnt lgkmcnt(8)\n"
  "v_mfma_f32_16x16x32_bf16 v[60:63], v[140:143], a[16:19], v[60:63]\n"
  "ds_read_b64_tr_b16 v[160:161], v26 offset:24\n"
  "v_mfma_f32_16x16x32_bf16 v[84:87], v[140:143], a[20:23], v[84:87]\n"
  "ds_read_b64_tr_b16 v[162:163], v27 offset:24\n"
  "buffer_load_dwordx4 a[68:71], v21, s[12:15], s6 offen sc1\n"
  "s_waitcnt lgkmcnt(8)\n"
  "v_mfma_f32_16x16x32_bf16 v[68:71], v[144:147], a[16:19], v[68:71]\n"
  "ds_read_b64_tr_b16 v[164:165], v26 offset:32\n"
  "v_mfma_f32_16x16x32_bf16 v[88:91], v[144:147], a[20:23], v[88:91]\n"
  "ds_read_b64_tr_b16 v[166:167], v27 offset:32\n"
  "s_waitcnt vmcnt(11) lgkmcnt(8)\n"
  "v_mfma_f32_16x16x32_bf16 v[48:51], v[148:151], a[24:27], v[48:51]\n"
  "ds_read_b64_tr_b16 v[128:129], v28\n"
  "buffer_load_dwordx4 a[72:75], v21, s[12:15], s5 offen offset:1024 sc1\n"
  "s_waitcnt vmcnt(11)\n"
  "v_mfma_f32_16x16x32_bf16 v[72:75], v[148:151], a[28:31], v[72:75]\n"
  "ds_read_b64_tr_b16 v[130:131], v29\n"
  "s_waitcnt lgkmcnt(8)\n"
  "v_mfma_f32_16x16x32_bf16 v[52:55], v[152:155], a[24:27], v[52:55]\n"
  "ds_read_b64_tr_b16 v[132:133], v28 offset:8\n"
  "v_mfma_f32_16x16x32_bf16 v[76:79], v[152:155], a[28:31], v[76:79]\n"
  "ds_read_b64_tr_b16 v[134:135], v29 offset:8\n"
  "s_waitcnt lgkmcnt(8)\n"
  "v_mfma_f32_16x16x32_bf16 v[56:59], v[156:159], a[24:27], v[56:59]\n"
  "ds_read_b64_tr_b16 v[136:137], v28 offset:16\n"
  "v_mfma_f32_16x16x32_bf16 v[80:83], v[156:159], a[28:31], v[80:83]\n"
  "ds_read_b64_tr_b16 v[138:139], v29 offset:16\n"
  "buffer_load_dwordx4 a[76:79], v21, s[12:15], s6 offen offset:1024 sc1\n"
  "s_waitcnt lgkmcnt(8)\n"
  "v_mfma_f32_16x16x32_bf16 v[60:63], v[160:163], a[24:27], v[60:63]\n"
  "ds_read_b64_tr_b16 v[140:141], v28 offset:24\n"
  "v_mfma_f32_16x16x32_bf16 v[84:87], v[160:163], a[28:31], v[84:87]\n"
  "ds_read_b64_tr_b16 v[142:143], v29 offset:24\n"
  "s_waitcnt lgkmcnt(8)\n"
  "v_mfma_f32_16x16x32_bf16 v[68:71], v[164:167], a[24:27], v[68:71]\n"
  "ds_read_b64_tr_b16 v[144:145], v28 offset:32\n"
  "v_mfma_f32_16x16x32_bf16 v[88:91], v[164:167], a[28:31], v[88:91]\n"
  "ds_read_b64_tr_b16 v[146:147], v29 offset:32\n"
  "s_waitcnt vmcnt(11) lgkmcnt(8)\n"
  "v_mfma_f32_16x16x32_bf16 v[48:51], v[128:131], a[32:35], v[48:51]\n"
  "ds_read_b64_tr_b16 v[148:149], v12 offset:25600\n"
  "s_waitcnt vmcnt(10)\n"
  "v_mfma_f32_16x16x32_bf16 v[72:75], v[128:131], a[36:39], v[72:75]\n"
  "ds_read_b64_tr_b16 v[150:151], v13 offset:25600\n"
  "s_waitcnt lgkmcnt(8)\n"
  "v_mfma_f32_16x16x32_bf16 v[52:55], v[132:135], a[32:35], v[52:55]\n"
  "ds_read_b64_tr_b16 v[152:153], v12 offset:25608\n"
  "v_mfma_f32_16x16x32_bf16 v[76:79], v[132:135], a[36:39], v[76:79]\n"
  "ds_read_b64_tr_b16 v[154:155], v13 offset:25608\n"
  "s_waitcnt lgkmcnt(8)\n"
  "v_mfma_f32_16x16x32_bf16 v[56:59], v[136:139], a[32:35], v[56:59]\n"
  "ds_read_b64_tr_b16 v[156:157], v12 offset:25616\n"
  "v_mfma_f32_16x16x32_bf16 v[80:83], v[136:139], a[36:39], v[80:83]\n"
  "ds_read_b64_tr_b16 v[158:159], v13 offset:25616\n"
  "s_waitcnt lgkmcnt(8)\n"
  "v_mfma_f32_16x16x32_bf16 v[60:63], v[140:143], a[32:35], v[60:63]\n"
  "ds_read_b64_tr_b16 v[160:161], v12 offset:25624\n"
  "v_mfma_f32_16x16x32_bf16 v[84:87], v[140:143], a[36:39], v[84:87]\n"
  "ds_read_b64_tr_b16 v[162:163], v13 offset:25624\n"
  "s_waitcnt lgkmcnt(8)\n"
  "v_mfma_f32_16x16x32_bf16 v[68:71], v[144:147], a[32:35], v[68:71]\n"
  "ds_read_b64_tr_b16 v[164:165], v12 offset:25632\n"
  "v_mfma_f32_16x16x32_bf16 v[88:91], v[144:147], a[36:39], v[88:91]\n"
  "ds_read_b64_tr_b16 v[166:167], v13 offset:25632\n"
  "s_waitcnt vmcnt(9) lgkmcnt(8)\n"
  "v_mfma_f32_16x16x32_bf16 v[48:51], v[148:151], a[40:43], v[48:51]\n"
  "ds_read_b64_tr_b16 v[128:129], v14 offset:25600\n"
  "s_waitcnt vmcnt(8)\n"
  "v_mfma_f32_16x16x32_bf16 v[72:75], v[148:151], a[44:47], v[72:75]\n"
  "ds_read_b64_tr_b16 v[130:131], v23 offset:25600\n"
  "s_waitcnt lgkmcnt(8)\n"
  "v_mfma_f32_16x16x32_bf16 v[52:55], v[152:155], a[40:43], v[52:55]\n"
  "ds_read_b64_tr_b16 v[132:133], v14 offset:25608\n"
  "v_mfma_f32_16x16x32_bf16 v[76:79], v[152:155], a[44:47], v[76:79]\n"
  "ds_read_b64_tr_b16 v[134:135], v23 offset:25608\n"
  "s_waitcnt lgkmcnt(8)\n"
  "v_mfma_f32_16x16x32_bf16 v[56:59], v[156:159], a[40:43], v[56:59]\n"
  "ds_read_b64_tr_b16 v[136:137], v14 offset:25616\n"
  "v_mfma_f32_16x16x32_bf16 v[80:83], v[156:159], a[44:47], v[80:83]\n"
  "ds_read_b64_tr_b16 v[138:139], v23 offset:25616\n"
  "s_waitcnt lgkmcnt(8)\n"
  "v_mfma_f32_16x16x32_bf16 v[60:63], v[160:163], a[40:43], v[60:63]\n"
  "ds_read_b64_tr_b16 v[140:141], v14 offset:25624\n"
  "v_mfma_f32_16x16x32_bf16 v[84:87], v[160:163], a[44:47], v[84:87]\n"
  "ds_read_b64_tr_b16 v[142:143], v23 offset:25624\n"
  "s_waitcnt lgkmcnt(8)\n"
  "v_mfma_f32_16x16x32_bf16 v[68:71], v[164:167], a[40:43], v[68:71]\n"
  "ds_read_b64_tr_b16 v[144:145], v14 offset:25632\n"
  "v_mfma_f32_16x16x32_bf16 v[88:91], v[164:167], a[44:47], v[88:91]\n"
  "ds_read_b64_tr_b16 v[146:147], v23 offset:25632\n"
  "s_waitcnt vmcnt(7) lgkmcnt(8)\n"
  "v_mfma_f32_16x16x32_bf16 v[48:51], v[128:131], a[48:51], v[48:51]\n"
  "ds_read_b64_tr_b16 v[148:149], v24 offset:25600\n"
  "s_waitcnt vmcnt(6)\n"
  "v_mfma_f32_16x16x32_bf16 v[72:75], v[128:131], a[52:55], v[72:75]\n"
  "ds_read_b64_tr_b16 v[150:151], v25 offset:25600\n"
  "s_waitcnt lgkmcnt(8)\n"
  "v_mfma_f32_16x16x32_bf16 v[52:55], v[132:135], a[48:51], v[52:55]\n"
  "ds_read_b64_tr_b16 v[152:153], v24 offset:25608\n"
  "v_mfma_f32_16x16x32_bf16 v[76:79], v[132:135], a[52:55], v[76:79]\n"
  "ds_read_b64_tr_b16 v[154:155], v25 offset:25608\n"
  "s_waitcnt lgkmcnt(8)\n"
  "v_mfma_f32_16x16x32_bf16 v[56:59], v[136:139], a[48:51], v[56:59]\n"
  "ds_read_b64_tr_b16 v[156:157], v24 offset:25616\n"
  "v_mfma_f32_16x16x32_bf16 v[80:83], v[136:139], a[52:55], v[80:83]\n"
  "ds_read_b64_tr_b16 v[158:159], v25 offset:25616\n"
  "s_waitcnt lgkmcnt(8)\n"
  "v_mfma_f32_16x16x32_bf16 v[60:63], v[140:143], a[48:51], v[60:63]\n"
  "ds_read_b64_tr_b16 v[160:161], v24 offset:25624\n"
  "v_mfma_f32_16x16x32_bf16 v[84:87], v[140:143], a[52:55], v[84:87]\n"
  "ds_read_b64_tr_b16 v[162:163], v25 offset:25624\n"
  "s_waitcnt lgkmcnt(8)\n"
  "v_mfma_f32_16x16x32_bf16 v[68:71], v[144:147], a[48:51], v[68:71]\n"
  "ds_read_b64_tr_b16 v[164:165], v24 offset:25632\n"
  "v_mfma_f32_16x16x32_bf16 v[88:91], v[144:147], a[52:55], v[88:91]\n"
  "ds_read_b64_tr_b16 v[166:167], v25 offset:25632\n"
  "s_waitcnt vmcnt(5) lgkmcnt(8)\n"
  "v_mfma_f32_16x16x32_bf16 v[48:51], v[148:151], a[56:59], v[48:51]\n"
  "ds_read_b64_tr_b16 v[128:129], v26 offset:25600\n"
  "s_waitcnt vmcnt(4)\n"
  "v_mfma_f32_16x16x32_bf16 v[72:75], v[148:151], a[60:63], v[72:75]\n"
  "ds_read_b64_tr_b16 v[130:131], v27 offset:25600\n"
  "s_waitcnt lgkmcnt(8)\n"
  "v_mfma_f32_16x16x32_bf16 v[52:55], v[152:155], a[56:59], v[52:55]\n"
  "ds_read_b64_tr_b16 v[132:133], v26 offset:25608\n"
  "v_mfma_f32_16x16x32_bf16 v[76:79], v[152:155], a[60:63], v[76:79]\n"
  "ds_read_b64_tr_b16 v[134:135], v27 offset:25608\n"
  "s_waitcnt lgkmcnt(8)\n"
  "v_mfma_f32_16x16x32_bf16 v[56:59], v[156:159], a[56:59], v[56:59]\n"
  "ds_read_b64_tr_b16 v[136:137], v26 offset:25616\n"
  "v_mfma_f32_16x16x32_bf16 v[80:83], v[156:159], a[60:63], v[80:83]\n"
  "ds_read_b64_tr_b16 v[138:139], v27 offset:25616\n"
  "s_waitcnt lgkmcnt(8)\n"
  "v_mfma_f32_16x16x32_bf16 v[60:63], v[160:163], a[56:59], v[60:63]\n"
  "ds_read_b64_tr_b16 v[140:141], v26 offset:25624\n"
  "v_mfma_f32_16x16x32_bf16 v[84:87], v[160:163], a[60:63], v[84:87]\n"
  "ds_read_b64_tr_b16 v[142:143], v27 offset:25624\n"
  "s_waitcnt lgkmcnt(8)\n"
  "v_mfma_f32_16x16x32_bf16 v[68:71], v[164:167], a[56:59], v[68:71]\n"
  "ds_read_b64_tr_b16 v[144:145], v26 offset:25632\n"
  "v_mfma_f32_16x16x32_bf16 v[88:91], v[164:167], a[60:63], v[88:91]\n"
  "ds_read_b64_tr_b16 v[146:147], v27 offset:25632\n"
  "s_waitcnt vmcnt(3) lgkmcnt(8)\n"
  "v_mfma_f32_16x16x32_bf16 v[48:51], v[128:131], a[64:67], v[48:51]\n"
  "ds_read_b64_tr_b16 v[148:149], v28 offset:25600\n"
  "buffer_load_dwordx2 v[46:47], v33, s[12:15], s7 offen sc1\n"
  "s_waitcnt vmcnt(3)\n"
  "v_mfma_f32_16x16x32_bf16 v[72:75], v[128:131], a[68:71], v[72:75]\n"
  "ds_read_b64_tr_b16 v[150:151], v29 offset:25600\n"
  "s_waitcnt lgkmcnt(8)\n"
  "v_mfma_f32_16x16x32_bf16 v[52:55], v[132:135], a[64:67], v[52:55]\n"
  "ds_read_b64_tr_b16 v[152:153], v28 offset:25608\n"
  "v_mfma_f32_16x16x32_bf16 v[76:79], v[132:135], a[68:71], v[76:79]\n"
  "ds_read_b64_tr_b16 v[154:155], v29 offset:25608\n"
  "s_waitcnt lgkmcnt(8)\n"
  "v_mfma_f32_16x16x32_bf16 v[56:59], v[136:139], a[64:67], v[56:59]\n"
  "ds_read_b64_tr_b16 v[156:157], v28 offset:25616\n"
  "v_mfma_f32_16x16x32_bf16 v[80:83], v[136:139], a[68:71], v[80:83]\n"
  "ds_read_b64_tr_b16 v[158:159], v29 offset:25616\n"
  "s_waitcnt lgkmcnt(8)\n"
  "v_mfma_f32_16x16x32_bf16 v[60:63], v[140:143], a[64:67], v[60:63]\n"
  "ds_read_b64_tr_b16 v[160:161], v28 offset:25624\n"
  "v_mfma_f32_16x16x32_bf16 v[84:87], v[140:143], a[68:71], v[84:87]\n"
  "ds_read_b64_tr_b16 v[162:163], v29 offset:25624\n"
  "s_waitcnt lgkmcnt(8)\n"
  "v_mfma_f32_16x16x32_bf16 v[68:71], v[144:147], a[64:67], v[68:71]\n"
  "ds_read_b64_tr_b16 v[164:165], v28 offset:25632\n"
  "v_mfma_f32_16x16x32_bf16 v[88:91], v[144:147], a[68:71], v[88:91]\n"
  "ds_read_b64_tr_b16 v[166:167], v29 offset:25632\n"
  "s_waitcnt vmcnt(1) lgkmcnt(8)\n"
  "v_mfma_f32_16x16x32_bf16 v[72:75], v[148:151], a[76:79], v[72:75]\n"
  "s_waitcnt lgkmcnt(6)\n"
  "v_mfma_f32_16x16x32_bf16 v[76:79], v[152:155], a[76:79], v[76:79]\n"
  "s_waitcnt lgkmcnt(4)\n"
  "v_mfma_f32_16x16x32_bf16 v[80:83], v[156:159], a[76:79], v[80:83]\n"
  "s_waitcnt lgkmcnt(2)\n"
  "v_mfma_f32_16x16x32_bf16 v[84:87], v[160:163], a[76:79], v[84:87]\n"
  "s_waitcnt lgkmcnt(0)\n"
  "v_mfma_f32_16x16x32_bf16 v[88:91], v[164:167], a[76:79], v[88:91]\n"
  "v_mfma_f32_16x16x32_bf16 v[48:51], v[148:151], a[72:75], v[48:51]\n"
  "v_mfma_f32_16x16x32_bf16 v[52:55], v[152:155], a[72:75], v[52:55]\n"
  "v_mfma_f32_16x16x32_bf16 v[56:59], v[156:159], a[72:75], v[56:59]\n"
  "v_mfma_f32_16x16x32_bf16 v[60:63], v[160:163], a[72:75], v[60:63]\n"
  "v_mfma_f32_16x16x32_bf16 v[68:71], v[164:167], a[72:75], v[68:71]\n"
  "ds_write_b128 v35, v[72:75]\n"
  "ds_write_b128 v35, v[76:79] offset:1024\n"
  "ds_write_b128 v35, v[80:83] offset:2048\n"
  "ds_write_b128 v35, v[84:87] offset:3072\n"
  "ds_write_b128 v35, v[88:91] offset:4096\n"
  "v_pk_add_f32 v[108:109], v[108:109], v[48:49]\n"
  "v_pk_add_f32 v[110:111], v[110:111], v[50:51]\n"
  "v_pk_add_f32 v[112:113], v[112:113], v[52:53]\n"
  "v_pk_add_f32 v[114:115], v[114:115], v[54:55]\n"
  "v_pk_add_f32 v[116:117], v[116:117], v[56:57]\n"
  "v_pk_add_f32 v[118:119], v[118:119], v[58:59]\n"
  "v_pk_add_f32 v[120:121], v[120:121], v[60:61]\n"
  "v_pk_add_f32 v[122:123], v[122:123], v[62:63]\n"
  "v_pk_add_f32 v[124:125], v[124:125], v[68:69]\n"
  "v_pk_add_f32 v[126:127], v[126:127], v[70:71]\n"
  "s_waitcnt lgkmcnt(0)\n"
  "s_barrier\n"
  "ds_read_b128 v[128:131], v66\n"
  "ds_read_b128 v[132:135], v66 offset:1024\n"
  "ds_read_b128 v[136:139], v66 offset:2048\n"
  "ds_read_b128 v[140:143], v66 offset:3072\n"
  "ds_read_b128 v[144:147], v66 offset:4096\n"
  "s_mov_b32 s67, 0x3f803f80\n"
  "v_mov_b32 v152, 0\n"
  "v_mov_b32 v153, 0\n"
  "s_waitcnt lgkmcnt(4)\n"
  "v_pk_add_f32 v[108:109], v[108:109], v[128:129]\n"
  "v_pk_add_f32 v[110:111], v[110:111], v[130:131]\n"
  "s_waitcnt lgkmcnt(3)\n"
  "v_pk_add_f32 v[112:113], v[112:113], v[132:133]\n"
  "v_pk_add_f32 v[114:115], v[114:115], v[134:135]\n"
  "v_cvt_pk_bf16_f32 v96, v108, v109\n"
  "v_cvt_pk_bf16_f32 v97, v110, v111\n"
  "v_cvt_pk_bf16_f32 v98, v112, v113\n"
  "v_cvt_pk_bf16_f32 v99, v114, v115\n"
  "buffer_store_dwordx4 v[96:99], v21, s[12:15], s20 offen sc1\n"
  "s_waitcnt lgkmcnt(2)\n"
  "v_pk_add_f32 v[116:117], v[116:117], v[136:137]\n"
  "v_pk_add_f32 v[118:119], v[118:119], v[138:139]\n"
  "s_waitcnt lgkmcnt(1)\n"
  "v_pk_add_f32 v[120:121], v[120:121], v[140:141]\n"
  "v_pk_add_f32 v[122:123], v[122:123], v[142:143]\n"
  "v_cvt_pk_bf16_f32 v100, v116, v117\n"
  "v_cvt_pk_bf16_f32 v101, v118, v119\n"
  "v_cvt_pk_bf16_f32 v102, v120, v121\n"
  "v_cvt_pk_bf16_f32 v103, v122, v123\n"
  "buffer_store_dwordx4 v[100:103], v21, s[12:15], s20 offen offset:1024 sc1\n"
  "s_waitcnt lgkmcnt(0)\n"
  "v_pk_add_f32 v[124:125], v[124:125], v[144:145]\n"
  "v_pk_add_f32 v[126:127], v[126:127], v[146:147]\n"
  "v_cvt_pk_bf16_f32 v104, v124, v125\n"
  "v_cvt_pk_bf16_f32 v105, v126, v127\n"
  "v_mov_b32 v148, v104\n"
  "v_mov_b32 v149, v105\n"
  "v_mov_b32 v150, v104\n"
  "v_mov_b32 v151, v105\n"
  "v_dot2c_f32_bf16 v152, s67, v96\n"
  "v_dot2c_f32_bf16 v153, v96, v96\n"
  "v_dot2c_f32_bf16 v152, s67, v97\n"
  "v_dot2c_f32_bf16 v153, v97, v97\n"
  "v_permlane16_swap_b32 v148, v150\n"
  "v_permlane16_swap_b32 v149, v151\n"
  "buffer_store_dwordx4 v[148:151], v67, s[12:15], s21 offen sc1\n"
  "v_dot2c_f32_bf16 v152, s67, v98\n"
  "v_dot2c_f32_bf16 v153, v98, v98\n"
  "v_dot2c_f32_bf16 v152, s67, v99\n"
  "v_dot2c_f32_bf16 v153, v99, v99\n"
  "v_dot2c_f32_bf16 v152, s67, v100\n"
  "v_dot2c_f32_bf16 v153, v100, v100\n"
  "v_dot2c_f32_bf16 v152, s67, v101\n"
  "v_dot2c_f32_bf16 v153, v101, v101\n"
  "v_dot2c_f32_bf16 v152, s67, v102\n"
  "v_dot2c_f32_bf16 v153, v102, v102\n"
  "v_dot2c_f32_bf16 v152, s67, v103\n"
  "v_dot2c_f32_bf16 v153, v103, v103\n"
  "v_dot2c_f32_bf16 v152, s67, v104\n"
  "v_dot2c_f32_bf16 v153, v104, v104\n"
  "v_dot2c_f32_bf16 v152, s67, v105\n"
  "v_dot2c_f32_bf16 v153, v105, v105\n"
  "s_nop 1\n"
  "v_mov_b32 v154, v152\n"
  "v_mov_b32 v155, v153\n"
  "s_nop 0\n"
  "v_permlane16_swap_b32 v152, v154\n"
  "v_permlane16_swap_b32 v153, v155\n"
  "v_pk_add_f32 v[152:153], v[152:153], v[154:155]\n"
  "v_mov_b32 v154, v152\n"
  "v_mov_b32 v155, v153\n"
  "s_nop 0\n"
  "v_permlane32_swap_b32 v152, v154\n"
  "v_permlane32_swap_b32 v153, v155\n"
  "v_pk_add_f32 v[152:153], v[152:153], v[154:155]\n"
  "buffer_store_dwordx2 v[152:153], v92, s[12:15], s22 offen sc1\n"
  "s_waitcnt vmcnt(4)\n"
  "v_cmp_ne_u64 vcc, s[72:73], v[46:47]\n"
  "s_nop 3\n"
  "s_cbranch_vccz .Lhh_done_14x\n"
  "s_mov_b32 s63, 0x10000\n"
  ".Lhh_poll_13:\n"
  "buffer_load_dwordx2 v[46:47], v33, s[12:15], s7 offen sc1\n"
  "s_waitcnt vmcnt(0)\n"
  "v_cmp_ne_u64 vcc, s[72:73], v[46:47]\n"
  "s_nop 4\n"
  "s_cbranch_vccz .Lhh_done_14\n"
  "s_sleep 1\n"
  "s_sub_u32 s63, s63, 1\n"
  "s_cbranch_scc0 .Lhh_poll_13\n"
  ".Lhh_done_14:\n"
  ".Lhh_done_14x:\n"
  "s_lshl_b32 s11, s27, 2\n"
  "s_add_u32 s11, s11, s23\n"
  "s_add_u32 m0, s9, 0\n"
  "s_nop 0\n"
  "buffer_load_dwordx4 v21, s[12:15], s11 offen sc1 lds\n"
  "buffer_load_dwordx4 v21, s[12:15], s11 offen offset:1024 sc1 lds\n"
  "buffer_load_dwordx4 v21, s[12:15], s11 offen offset:2048 sc1 lds\n"
  "buffer_load_dwordx4 v21, s[12:15], s11 offen offset:3072 sc1 lds\n"
  "s_add_u32 m0, m0, 0x1000\n"
  "s_add_u32 s11, s11, 0x1000\n"
  "buffer_load_dwordx4 v21, s[12:15], s11 offen sc1 lds\n"
  "s_lshl_b32 s11, s28, 2\n"
  "s_add_u32 s11, s11, s23\n"
  "s_add_u32 m0, s9, 0x5000\n"
  "s_nop 0\n"
  "buffer_load_dwordx4 v21, s[12:15], s11 offen sc1 lds\n"
  "buffer_load_dwordx4 v21, s[12:15], s11 offen offset:1024 sc1 lds\n"
  "buffer_load_dwordx4 v21, s[12:15], s11 offen offset:2048 sc1 lds\n"
  "buffer_load_dwordx4 v21, s[12:15], s11 offen offset:3072 sc1 lds\n"
  "s_add_u32 m0, m0, 0x1000\n"
  "s_add_u32 s11, s11, 0x1000\n"
  "buffer_load_dwordx4 v21, s[12:15], s11 offen sc1 lds\n"
  "v_cmp_gt_u32 s[20:21], 3, s25\n"
  "v_lshlrev_b32 v3, 2, v0\n"
  "s_nop 0\n"
  "v_cndmask_b32 v12, v22, v3, s[20:21]\n"
  "v_lshlrev_b32 v13, 3, v0\n"
  "v_add_u32 v13, 0x23000, v13\n"
  "s_mul_i32 s7, s41, 0x1800\n"
  "s_add_u32 s7, s7, 0x640000\n"
  "s_add_u32 s22, s7, 0xc00\n"
  "v_bfe_u32 v4, v1, 2, 1\n"
  "v_lshl_add_u32 v3, v4, 3, v18\n"
  "v_lshlrev_b32 v14, 5, v3\n"
  "v_bfe_u32 v4, v1, 3, 1\n"
  "v_lshl_add_u32 v14, v4, 4, v14\n"
  "v_lshlrev_b32 v14, 0, v14\n"
  "s_lshl_b32 s48, s40, 9\n"
  "s_lshl_b32 s67, s8, 7\n"
  "s_add_u32 s48, s48, s67\n"
  "s_lshl_b32 s67, s10, 3\n"
  "s_add_u32 s48, s48, s67\n"
  "s_lshl_b32 s48, s48, 0\n"
  "s_add_u32 s48, s48, 0x7000\n"
  "s_lshl_b32 s67, s40, 2\n"
  "s_add_u32 s67, s67, s25\n"
  "s_mul_i32 s45, s67, 0xa000\n"
  "s_add_u32 s45, s45, 0x3a0000\n"
  "s_add_u32 s46, s45, 0x5000\n"
  "s_lshl_b32 s47, s40, 13\n"
  "s_lshl_b32 s67, s25, 8\n"
  "s_add_u32 s47, s47, s67\n"
  "s_add_u32 s47, s47, 0x620000\n"
  "v_lshlrev_b32 v3, 3, v15\n"
  "v_lshl_add_u32 v24, v19, 11, v3\n"
  "v_add_u32 v23, 0x23000, v3\n"
  "v_cmp_gt_u32 s[20:21], 16, v1\n"
  "v_add_u32 v25, 0x10000, v21\n"
  "v_add_u32 v26, 0x20000, v21\n"
  "s_mul_i32 s67, s8, 0x2800\n"
  "v_add_u32 v27, s67, v21\n"
  "v_add_u32 v28, s67, v25\n"
  "v_add_u32 v29, s67, v26\n"
  "v_accvgpr_write_b32 a248, 0\n"
  "v_accvgpr_write_b32 a249, 0\n"
  "s_lshl_b32 s67, s40, 2\n"
  "s_add_u32 s67, s67, s25\n"
  "s_mul_i32 s49, s67, 0x18000\n"
  "s_mul_i32 s67, s41, 0xc00\n"
  "s_add_u32 s49, s49, s67\n"
  "s_add_u32 s49, s49, 0xbf8000\n"
  "s_add_u32 s55, s49, 0xc000\n"
  "s_lshl_b32 s67, s40, 3\n"
  "s_add_u32 s67, s67, s16\n"
  "s_mul_i32 s5, s67, 0xc000\n"
  "s_mul_i32 s67, s8, 0x6000\n"
  "s_add_u32 s5, s5, s67\n"
  "s_add_u32 s5, s5, 0xbf8000\n"
  "s_lshl_b32 s67, s40, 3\n"
  "s_add_u32 s67, s67, s17\n"
  "s_mul_i32 s6, s67, 0xc000\n"
  "s_mul_i32 s67, s8, 0x6000\n"
  "s_add_u32 s6, s6, s67\n"
  "s_add_u32 s6, s6, 0xbf8000\n"
  "s_xor_b32 s70, s25, 2\n"
  "s_mul_i32 s70, s70, 0x1400\n"
  "s_add_u32 s69, s9, 0xa000\n"
  "s_add_u32 s70, s70, 0xa000\n"
  "v_add_u32 v35, s69, v21\n"
  "v_add_u32 v66, s70, v21\n"
  "v_lshlrev_b32 v108, 16, v96\n"
  "v_and_b32 v109, 0xffff0000, v96\n"
  "v_lshlrev_b32 v110, 16, v97\n"
  "v_and_b32 v111, 0xffff0000, v97\n"
  "v_lshlrev_b32 v112, 16, v98\n"
  "v_and_b32 v113, 0xffff0000, v98\n"
  "v_lshlrev_b32 v114, 16, v99\n"
  "v_and_b32 v115, 0xffff0000, v99\n"
  "v_lshlrev_b32 v116, 16, v100\n"
  "v_and_b32 v117, 0xffff0000, v100\n"
  "v_lshlrev_b32 v118, 16, v101\n"
  "v_and_b32 v119, 0xffff0000, v101\n"
  "v_lshlrev_b32 v120, 16, v102\n"
  "v_and_b32 v121, 0xffff0000, v102\n"
  "v_lshlrev_b32 v122, 16, v103\n"
  "v_and_b32 v123, 0xffff0000, v103\n"
  "v_lshlrev_b32 v124, 16, v104\n"
  "v_and_b32 v125, 0xffff0000, v104\n"
  "v_lshlrev_b32 v126, 16, v105\n"
  "v_and_b32 v127, 0xffff0000, v105\n"
  "s_add_u32 s64, s65, 0x7000\n"
  "s_waitcnt vmcnt(10)\n"
  "s_mov_b64 exec, 1\n"
  "buffer_store_dwordx2 v[10:11], off, s[12:15], s64 sc1\n"
  "s_mov_b64 exec, -1\n"
  "buffer_store_dwordx4 a[160:163], v31, s[12:15], s39 offen sc1\n"
  "buffer_store_dwordx4 a[164:167], v31, s[12:15], s39 offen offset:1024 sc1\n"
  "buffer_store_dwordx4 a[168:171], v31, s[12:15], s39 offen offset:2048 sc1\n"
  "buffer_store_dwordx4 a[172:175], v31, s[12:15], s39 offen offset:3072 sc1\n"
  "buffer_store_dwordx4 a[176:179], v31, s[12:15], s50 offen sc1\n"
  "buffer_store_dwordx4 a[180:183], v32, s[12:15], s51 offen sc1\n"
  "buffer_store_dwordx4 a[184:187], v32, s[12:15], s51 offen offset:1024 sc1\n"
  "buffer_store_dwordx4 a[188:191], v32, s[12:15], s51 offen offset:2048 sc1\n"
  "buffer_store_dwordx4 a[192:195], v32, s[12:15], s51 offen offset:3072 sc1\n"
  "buffer_store_dwordx4 a[196:199], v32, s[12:15], s52 offen sc1\n"
  "s_lshl_b32 s11, s29, 2\n"
  "s_add_u32 s11, s11, s23\n"
  "s_add_u32 m0, s9, 0xf000\n"
  "s_nop 0\n"
  "buffer_load_dwordx4 v21, s[12:15], s11 offen sc1 lds\n"
  "buffer_load_dwordx4 v21, s[12:15], s11 offen offset:1024 sc1 lds\n"
  "buffer_load_dwordx4 v21, s[12:15], s11 offen offset:2048 sc1 lds\n"
  "buffer_load_dwordx4 v21, s[12:15], s11 offen offset:3072 sc1 lds\n"
  "s_add_u32 m0, m0, 0x1000\n"
  "s_add_u32 s11, s11, 0x1000\n"
  "buffer_load_dwordx4 v21, s[12:15], s11 offen sc1 lds\n"
  "s_lshl_b32 s11, s30, 2\n"
  "s_add_u32 s11, s11, s23\n"
  "s_add_u32 m0, s9, 0x14000\n"
  "s_nop 0\n"
  "buffer_load_dwordx4 v21, s[12:15], s11 offen sc1 lds\n"
  "buffer_load_dwordx4 v21, s[12:15], s11 offen offset:1024 sc1 lds\n"
  "buffer_load_dwordx4 v21, s[12:15], s11 offen offset:2048 sc1 lds\n"
  "buffer_load_dwordx4 v21, s[12:15], s11 offen offset:3072 sc1 lds\n"
  "s_add_u32 m0, m0, 0x1000\n"
  "s_add_u32 s11, s11, 0x1000\n"
  "buffer_load_dwordx4 v21, s[12:15], s11 offen sc1 lds\n"
  "s_waitcnt vmcnt(26)\n"
  "s_barrier\n"
  "ds_read_b128 v[128:131], v21\n"
  "ds_read_b128 v[132:135], v21 offset:1024\n"
  "ds_read_b128 v[136:139], v21 offset:2048\n"
  "ds_read_b128 v[140:143], v21 offset:3072\n"
  "ds_read_b128 v[144:147], v21 offset:4096\n"
  "ds_read_b128 v[148:151], v21 offset:5120\n"
  "s_mov_b32 s63, 0x10000\n"
  "buffer_load_dwordx2 v[64:65], v14, s[12:15], s48 offen sc1\n"
  "s_sleep 2\n"
  ".Lhh_poll_15:\n"
  "buffer_load_dwordx2 v[36:37], v14, s[12:15], s48 offen sc1\n"
  "s_waitcnt vmcnt(1)\n"
  "v_cmp_ne_u64 vcc, s[72:73], v[64:65]\n"
  "s_nop 4\n"
  "s_cbranch_vccz .Lhh_done_16\n"
  "s_sleep 1\n"
  "buffer_load_dwordx2 v[64:65], v14, s[12:15], s48 offen sc1\n"
  "s_waitcnt vmcnt(1)\n"
  "v_cmp_ne_u64 vcc, s[72:73], v[36:37]\n"
  "s_nop 4\n"
  "s_cbranch_vccz .Lhh_done_16\n"
  "s_sleep 1\n"
  "s_sub_u32 s63, s63, 1\n"
  "s_cbranch_scc0 .Lhh_poll_15\n"
  ".Lhh_done_16:\n"
  "; polled\n"
  "; flag read in flight\n"
  "s_add_u32 s56, s45, s27\n"
  "buffer_load_dwordx4 a[0:3], v21, s[12:15], s56 offen sc1\n"
  "s_add_u32 s57, s46, s27\n"
  "buffer_load_dwordx4 a[4:7], v21, s[12:15], s57 offen sc1\n"
  "buffer_load_dwordx4 a[8:11], v21, s[12:15], s56 offen offset:1024 sc1\n"
  "buffer_load_dwordx4 a[12:15], v21, s[12:15], s57 offen offset:1024 sc1\n"
  "buffer_load_dwordx4 a[16:19], v21, s[12:15], s56 offen offset:2048 sc1\n"
  "buffer_load_dwordx4 a[20:23], v21, s[12:15], s57 offen offset:2048 sc1\n"
  "buffer_load_dwordx4 a[24:27], v21, s[12:15], s56 offen offset:3072 sc1\n"
  "buffer_load_dwordx4 a[28:31], v21, s[12:15], s57 offen offset:3072 sc1\n"
  "s_add_u32 s56, s56, 0x1000\n"
  "buffer_load_dwordx4 a[32:35], v21, s[12:15], s56 offen sc1\n"
  "s_add_u32 s57, s57, 0x1000\n"
  "buffer_load_dwordx4 a[36:39], v21, s[12:15], s57 offen sc1\n"
  "s_waitcnt vmcnt(9) lgkmcnt(5)\n"
  "v_mfma_f32_16x16x32_bf16 v[40:43], v[128:131], a[0:3], 0\n"
  "ds_read_b128 v[152:155], v21 offset:6144\n"
  "s_waitcnt vmcnt(8)\n"
  "v_mfma_f32_16x16x32_bf16 v[56:59], v[128:131], a[4:7], 0\n"
  "s_add_u32 s56, s45, s28\n"
  "buffer_load_dwordx4 a[40:43], v21, s[12:15], s56 offen sc1\n"
  "s_waitcnt lgkmcnt(5)\n"
  "v_mfma_f32_16x16x32_bf16 v[88:91], v[132:135], a[0:3], 0\n"
  "ds_read_b128 v[156:159], v21 offset:7168\n"
  "v_mfma_f32_16x16x32_bf16 v[92:95], v[132:135], a[4:7], 0\n"
  "s_add_u32 s57, s46, s28\n"
  "buffer_load_dwordx4 a[44:47], v21, s[12:15], s57 offen sc1\n"
  "s_waitcnt lgkmcnt(5)\n"
  "v_mfma_f32_16x16x32_bf16 v[60:63], v[136:139], a[0:3], 0\n"
  "ds_read_b128 v[128:131], v21 offset:8192\n"
  "v_mfma_f32_16x16x32_bf16 v[68:71], v[136:139], a[4:7], 0\n"
  "buffer_load_dwordx4 a[48:51], v21, s[12:15], s56 offen offset:1024 sc1\n"
  "s_waitcnt lgkmcnt(3)\n"
  "v_mfma_f32_16x16x32_bf16 v[168:171], v[140:143], a[0:3], 0\n"
  "ds_read_b128 v[132:135], v21 offset:9216\n"
  "v_mfma_f32_16x16x32_bf16 v[172:175], v[140:143], a[4:7], 0\n"
  "buffer_load_dwordx4 a[52:55], v21, s[12:15], s57 offen offset:1024 sc1\n"
  "s_waitcnt vmcnt(11)\n"
  "v_mfma_f32_16x16x32_bf16 v[40:43], v[144:147], a[8:11], v[40:43]\n"
  "ds_read_b128 v[136:139], v21 offset:10240\n"
  "s_waitcnt vmcnt(10)\n"
  "v_mfma_f32_16x16x32_bf16 v[56:59], v[144:147], a[12:15], v[56:59]\n"
  "buffer_load_dwordx4 a[56:59], v21, s[12:15], s56 offen offset:2048 sc1\n"
  "v_mfma_f32_16x16x32_bf16 v[88:91], v[148:151], a[8:11], v[88:91]\n"
  "ds_read_b128 v[140:143], v21 offset:11264\n"
  "v_mfma_f32_16x16x32_bf16 v[92:95], v[148:151], a[12:15], v[92:95]\n"
  "buffer_load_dwordx4 a[60:63], v21, s[12:15], s57 offen offset:2048 sc1\n"
  "s_waitcnt lgkmcnt(3)\n"
  "v_mfma_f32_16x16x32_bf16 v[60:63], v[152:155], a[8:11], v[60:63]\n"
  "ds_read_b128 v[144:147], v21 offset:12288\n"
  "v_mfma_f32_16x16x32_bf16 v[68:71], v[152:155], a[12:15], v[68:71]\n"
  "buffer_load_dword v48, v12, s[12:15], s7 offen sc1\n"
  "v_mfma_f32_16x16x32_bf16 v[168:171], v[156:159], a[8:11], v[168:171]\n"
  "ds_read_b128 v[148:151], v21 offset:13312\n"
  "v_mfma_f32_16x16x32_bf16 v[172:175], v[156:159], a[12:15], v[172:175]\n"
  "buffer_load_dword v49, v12, s[12:15], s7 offen offset:768 sc1\n"
  "s_waitcnt vmcnt(13)\n"
  "v_mfma_f32_16x16x32_bf16 v[40:43], v[128:131], a[16:19], v[40:43]\n"
  "ds_read_b128 v[152:155], v21 offset:14336\n"
  "s_waitcnt vmcnt(12)\n"
  "v_mfma_f32_16x16x32_bf16 v[56:59], v[128:131], a[20:23], v[56:59]\n"
  "buffer_load_dword v50, v12, s[12:15], s7 offen offset:1536 sc1\n"
  "s_waitcnt lgkmcnt(3)\n"
  "v_mfma_f32_16x16x32_bf16 v[88:91], v[132:135], a[16:19], v[88:91]\n"
  "ds_read_b128 v[156:159], v21 offset:15360\n"
  "v_mfma_f32_16x16x32_bf16 v[92:95], v[132:135], a[20:23], v[92:95]\n"
  "buffer_load_dword v51, v12, s[12:15], s7 offen offset:2304 sc1\n"
  "v_mfma_f32_16x16x32_bf16 v[60:63], v[136:139], a[16:19], v[60:63]\n"
  "ds_read_b128 v[128:131], v21 offset:16384\n"
  "v_mfma_f32_16x16x32_bf16 v[68:71], v[136:139], a[20:23], v[68:71]\n"
  "buffer_load_dword v52, v12, s[12:15], s22 offen sc1\n"
  "v_mfma_f32_16x16x32_bf16 v[168:171], v[140:143], a[16:19], v[168:171]\n"
  "ds_read_b128 v[132:135], v21 offset:17408\n"
  "v_mfma_f32_16x16x32_bf16 v[172:175], v[140:143], a[20:23], v[172:175]\n"
  "buffer_load_dword v53, v12, s[12:15], s22 offen offset:768 sc1\n"
  "s_waitcnt lgkmcnt(3)\n"
  "s_waitcnt vmcnt(15)\n"
  "v_mfma_f32_16x16x32_bf16 v[40:43], v[144:147], a[24:27], v[40:43]\n"
  "ds_read_b128 v[136:139], v21 offset:18432\n"
  "s_waitcnt vmcnt(14)\n"
  "v_mfma_f32_16x16x32_bf16 v[56:59], v[144:147], a[28:31], v[56:59]\n"
  "buffer_load_dword v54, v12, s[12:15], s22 offen offset:1536 sc1\n"
  "v_mfma_f32_16x16x32_bf16 v[88:91], v[148:151], a[24:27], v[88:91]\n"
  "ds_read_b128 v[140:143], v21 offset:19456\n"
  "v_mfma_f32_16x16x32_bf16 v[92:95], v[148:151], a[28:31], v[92:95]\n"
  "buffer_load_dword v55, v12, s[12:15], s22 offen offset:2304 sc1\n"
  "v_mfma_f32_16x16x32_bf16 v[60:63], v[152:155], a[24:27], v[60:63]\n"
  "s_barrier\n"
  "ds_read_b128 v[144:147], v21 offset:20480\n"
  "v_mfma_f32_16x16x32_bf16 v[68:71], v[152:155], a[28:31], v[68:71]\n"
  "buffer_load_dwordx4 a[64:67], v21, s[12:15], s56 offen offset:3072 sc1\n"
  "s_waitcnt lgkmcnt(3)\n"
  "v_mfma_f32_16x16x32_bf16 v[168:171], v[156:159], a[24:27], v[168:171]\n"
  "ds_read_b128 v[148:151], v21 offset:21504\n"
  "v_mfma_f32_16x16x32_bf16 v[172:175], v[156:159], a[28:31], v[172:175]\n"
  "buffer_load_dwordx4 a[68:71], v21, s[12:15], s57 offen offset:3072 sc1\n"
  "s_waitcnt vmcnt(17)\n"
  "v_mfma_f32_16x16x32_bf16 v[40:43], v[128:131], a[32:35], v[40:43]\n"
  "ds_read_b128 v[152:155], v21 offset:22528\n"
  "s_waitcnt vmcnt(16)\n"
  "v_mfma_f32_16x16x32_bf16 v[56:59], v[128:131], a[36:39], v[56:59]\n"
  "s_add_u32 s56, s56, 0x1000\n"
  "buffer_load_dwordx4 a[72:75], v21, s[12:15], s56 offen sc1\n"
  "v_mfma_f32_16x16x32_bf16 v[88:91], v[132:135], a[32:35], v[88:91]\n"
  "ds_read_b128 v[156:159], v21 offset:23552\n"
  "v_mfma_f32_16x16x32_bf16 v[92:95], v[132:135], a[36:39], v[92:95]\n"
  "s_add_u32 s57, s57, 0x1000\n"
  "buffer_load_dwordx4 a[76:79], v21, s[12:15], s57 offen sc1\n"
  "s_waitcnt lgkmcnt(3)\n"
  "v_mfma_f32_16x16x32_bf16 v[60:63], v[136:139], a[32:35], v[60:63]\n"
  "ds_read_b128 v[128:131], v21 offset:24576\n"
  "v_mfma_f32_16x16x32_bf16 v[68:71], v[136:139], a[36:39], v[68:71]\n"
  "s_add_u32 s56, s45, s29\n"
  "buffer_load_dwordx4 a[80:83], v21, s[12:15], s56 offen sc1\n"
  "v_mfma_f32_16x16x32_bf16 v[168:171], v[140:143], a[32:35], v[168:171]\n"
  "ds_read_b128 v[132:135], v21 offset:25600\n"
  "v_mfma_f32_16x16x32_bf16 v[172:175], v[140:143], a[36:39], v[172:175]\n"
  "s_add_u32 s57, s46, s29\n"
  "buffer_load_dwordx4 a[84:87], v21, s[12:15], s57 offen sc1\n"
  "s_waitcnt vmcnt(19)\n"
  "v_mfma_f32_16x16x32_bf16 v[40:43], v[144:147], a[40:43], v[40:43]\n"
  "ds_read_b128 v[136:139], v21 offset:26624\n"
  "s_waitcnt vmcnt(18)\n"
  "v_mfma_f32_16x16x32_bf16 v[56:59], v[144:147], a[44:47], v[56:59]\n"
  "buffer_load_dwordx4 a[88:91], v21, s[12:15], s56 offen offset:1024 sc1\n"
  "s_waitcnt lgkmcnt(3)\n"
  "v_mfma_f32_16x16x32_bf16 v[88:91], v[148:151], a[40:43], v[88:91]\n"
  "ds_read_b128 v[140:143], v21 offset:27648\n"
  "v_mfma_f32_16x16x32_bf16 v[92:95], v[148:151], a[44:47], v[92:95]\n"
  "buffer_load_dwordx4 a[92:95], v21, s[12:15], s57 offen offset:1024 sc1\n"
  "v_mfma_f32_16x16x32_bf16 v[60:63], v[152:155], a[40:43], v[60:63]\n"
  "ds_read_b128 v[144:147], v21 offset:28672\n"
  "v_mfma_f32_16x16x32_bf16 v[68:71], v[152:155], a[44:47], v[68:71]\n"
  "buffer_load_dwordx2 v[192:193], v24, s[12:15], s47 offen sc1\n"
  "v_mfma_f32_16x16x32_bf16 v[168:171], v[156:159], a[40:43], v[168:171]\n"
  "ds_read_b128 v[148:151], v21 offset:29696\n"
  "v_mfma_f32_16x16x32_bf16 v[172:175], v[156:159], a[44:47], v[172:175]\n"
  "buffer_load_dwordx2 v[194:195], v24, s[12:15], s47 offen offset:1024 sc1\n"
  "s_waitcnt lgkmcnt(3)\n"
  "s_waitcnt vmcnt(21)\n"
  "v_mfma_f32_16x16x32_bf16 v[40:43], v[128:131], a[48:51], v[40:43]\n"
  "ds_read_b128 v[152:155], v21 offset:30720\n"
  "s_waitcnt vmcnt(20)\n"
  "v_mfma_f32_16x16x32_bf16 v[56:59], v[128:131], a[52:55], v[56:59]\n"
  "buffer_load_dwordx2 v[196:197], v24, s[12:15], s47 offen offset:128 sc1\n"
  "v_mfma_f32_16x16x32_bf16 v[88:91], v[132:135], a[48:51], v[88:91]\n"
  "ds_read_b128 v[156:159], v21 offset:31744\n"
  "v_mfma_f32_16x16x32_bf16 v[92:95], v[132:135], a[52:55], v[92:95]\n"
  "buffer_load_dwordx2 v[198:199], v24, s[12:15], s47 offen offset:1152 sc1\n"
  "v_mfma_f32_16x16x32_bf16 v[60:63], v[136:139], a[48:51], v[60:63]\n"
  "ds_read_b128 v[128:131], v21 offset:32768\n"
  "v_mfma_f32_16x16x32_bf16 v[68:71], v[136:139], a[52:55], v[68:71]\n"
  "buffer_load_dwordx4 a[96:99], v21, s[12:15], s56 offen offset:2048 sc1\n"
  "s_waitcnt lgkmcnt(3)\n"
  "v_mfma_f32_16x16x32_bf16 v[168:171], v[140:143], a[48:51], v[168:171]\n"
  "ds_read_b128 v[132:135], v21 offset:33792\n"
  "v_mfma_f32_16x16x32_bf16 v[172:175], v[140:143], a[52:55], v[172:175]\n"
  "buffer_load_dwordx4 a[100:103], v21, s[12:15], s57 offen offset:2048 sc1\n"
  "s_waitcnt vmcnt(23)\n"
  "v_mfma_f32_16x16x32_bf16 v[40:43], v[144:147], a[56:59], v[40:43]\n"
  "ds_read_b128 v[136:139], v21 offset:34816\n"
  "s_waitcnt vmcnt(22)\n"
  "v_mfma_f32_16x16x32_bf16 v[56:59], v[144:147], a[60:63], v[56:59]\n"
  "buffer_load_dwordx4 a[104:107], v21, s[12:15], s56 offen offset:3072 sc1\n"
  "v_mfma_f32_16x16x32_bf16 v[88:91], v[148:151], a[56:59], v[88:91]\n"
  "ds_read_b128 v[140:143], v21 offset:35840\n"
  "v_mfma_f32_16x16x32_bf16 v[92:95], v[148:151], a[60:63], v[92:95]\n"
  "buffer_load_dwordx4 a[108:111], v21, s[12:15], s57 offen offset:3072 sc1\n"
  "s_waitcnt lgkmcnt(3)\n"
  "v_mfma_f32_16x16x32_bf16 v[60:63], v[152:155], a[56:59], v[60:63]\n"
  "ds_read_b128 v[144:147], v21 offset:36864\n"
  "v_mfma_f32_16x16x32_bf16 v[68:71], v[152:155], a[60:63], v[68:71]\n"
  "s_add_u32 s56, s56, 0x1000\n"
  "buffer_load_dwordx4 a[112:115], v21, s[12:15], s56 offen sc1\n"
  "v_mfma_f32_16x16x32_bf16 v[168:171], v[156:159], a[56:59], v[168:171]\n"
  "ds_read_b128 v[148:151], v21 offset:37888\n"
  "v_mfma_f32_16x16x32_bf16 v[172:175], v[156:159], a[60:63], v[172:175]\n"
  "s_add_u32 s57, s57, 0x1000\n"
  "buffer_load_dwordx4 a[116:119], v21, s[12:15], s57 offen sc1\n"
  "s_waitcnt vmcnt(17)\n"
  "v_mfma_f32_16x16x32_bf16 v[40:43], v[128:131], a[64:67], v[40:43]\n"
  "ds_read_b128 v[152:155], v21 offset:38912\n"
  "s_waitcnt vmcnt(16)\n"
  "v_mfma_f32_16x16x32_bf16 v[56:59], v[128:131], a[68:71], v[56:59]\n"
  "s_add_u32 s56, s45, s30\n"
  "buffer_load_dwordx4 a[120:123], v21, s[12:15], s56 offen sc1\n"
  "s_waitcnt lgkmcnt(3)\n"
  "v_mfma_f32_16x16x32_bf16 v[88:91], v[132:135], a[64:67], v[88:91]\n"
  "ds_read_b128 v[156:159], v21 offset:39936\n"
  "v_mfma_f32_16x16x32_bf16 v[92:95], v[132:135], a[68:71], v[92:95]\n"
  "s_add_u32 s57, s46, s30\n"
  "buffer_load_dwordx4 a[124:127], v21, s[12:15], s57 offen sc1\n"
  "v_mfma_f32_16x16x32_bf16 v[60:63], v[136:139], a[64:67], v[60:63]\n"
  "s_barrier\n"
  "ds_read_b128 v[128:131], v21 offset:61440\n"
  "v_mfma_f32_16x16x32_bf16 v[68:71], v[136:139], a[68:71], v[68:71]\n"
  "buffer_load_dwordx4 a[128:131], v21, s[12:15], s56 offen offset:1024 sc1\n"
  "v_mfma_f32_16x16x32_bf16 v[168:171], v[140:143], a[64:67], v[168:171]\n"
  "ds_read_b128 v[132:135], v21 offset:62464\n"
  "v_mfma_f32_16x16x32_bf16 v[172:175], v[140:143], a[68:71], v[172:175]\n"
  "buffer_load_dwordx4 a[132:135], v21, s[12:15], s57 offen offset:1024 sc1\n"
  "s_waitcnt lgkmcnt(3)\n"
  "s_waitcnt vmcnt(19)\n"
  "v_mfma_f32_16x16x32_bf16 v[40:43], v[144:147], a[72:75], v[40:43]\n"
  "ds_read_b128 v[136:139], v21 offset:63488\n"
  "s_waitcnt vmcnt(18)\n"
  "v_mfma_f32_16x16x32_bf16 v[56:59], v[144:147], a[76:79], v[56:59]\n"
  "buffer_load_dwordx4 a[136:139], v21, s[12:15], s56 offen offset:2048 sc1\n"
  "v_mfma_f32_16x16x32_bf16 v[88:91], v[148:151], a[72:75], v[88:91]\n"
  "ds_read_b128 v[140:143], v21 offset:64512\n"
  "v_mfma_f32_16x16x32_bf16 v[92:95], v[148:151], a[76:79], v[92:95]\n"
  "buffer_load_dwordx4 a[140:143], v21, s[12:15], s57 offen offset:2048 sc1\n"
  "v_mfma_f32_16x16x32_bf16 v[60:63], v[152:155], a[72:75], v[60:63]\n"
  "ds_read_b128 v[144:147], v25\n"
  "v_mfma_f32_16x16x32_bf16 v[68:71], v[152:155], a[76:79], v[68:71]\n"
  "buffer_load_dwordx4 a[144:147], v21, s[12:15], s56 offen offset:3072 sc1\n"
  "s_waitcnt lgkmcnt(3)\n"
  "v_mfma_f32_16x16x32_bf16 v[168:171], v[156:159], a[72:75], v[168:171]\n"
  "ds_read_b128 v[148:151], v25 offset:1024\n"
  "v_mfma_f32_16x16x32_bf16 v[172:175], v[156:159], a[76:79], v[172:175]\n"
  "buffer_load_dwordx4 a[148:151], v21, s[12:15], s57 offen offset:3072 sc1\n"
  "s_waitcnt vmcnt(21)\n"
  "v_mfma_f32_16x16x32_bf16 v[40:43], v[128:131], a[80:83], v[40:43]\n"
  "ds_read_b128 v[152:155], v25 offset:2048\n"
  "v_add_f32 v48, v48, v49\n"
  "v_add_f32 v48, v48, v50\n"
  "s_waitcnt vmcnt(20)\n"
  "v_mfma_f32_16x16x32_bf16 v[56:59], v[128:131], a[84:87], v[56:59]\n"
  "s_add_u32 m0, s9, 0x19000\n"
  "s_add_u32 s11, s42, 0x14000\n"
  "buffer_load_dwordx4 v21, s[12:15], s11 offen sc1 lds\n"
  "v_mfma_f32_16x16x32_bf16 v[88:91], v[132:135], a[80:83], v[88:91]\n"
  "ds_read_b128 v[156:159], v25 offset:3072\n"
  "v_add_f32 v48, v48, v51\n"
  "v_add_f32 v48, v48, v52\n"
  "v_mfma_f32_16x16x32_bf16 v[92:95], v[132:135], a[84:87], v[92:95]\n"
  "buffer_load_dwordx4 v21, s[12:15], s11 offen offset:1024 sc1 lds\n"
  "v_add_f32 v48, v48, v53\n"
  "s_waitcnt lgkmcnt(3)\n"
  "v_mfma_f32_16x16x32_bf16 v[60:63], v[136:139], a[80:83], v[60:63]\n"
  "ds_read_b128 v[128:131], v25 offset:4096\n"
  "v_add_f32 v48, v48, v54\n"
  "v_add_f32 v48, v48, v55\n"
  "v_mfma_f32_16x16x32_bf16 v[68:71], v[136:139], a[84:87], v[68:71]\n"
  "buffer_load_dwordx4 v21, s[12:15], s11 offen offset:2048 sc1 lds\n"
  "v_cvt_pk_bf16_f32 v50, v48, v48\n"
  "v_lshlrev_b32 v49, 16, v50\n"
  "v_mfma_f32_16x16x32_bf16 v[168:171], v[140:143], a[80:83], v[168:171]\n"
  "ds_read_b128 v[132:135], v25 offset:5120\n"
  "v_sub_f32 v49, v48, v49\n"
  "v_cvt_pk_bf16_f32 v51, v49, 0\n"
  "v_mfma_f32_16x16x32_bf16 v[172:175], v[140:143], a[84:87], v[172:175]\n"
  "buffer_load_dwordx4 v21, s[12:15], s11 offen offset:3072 sc1 lds\n"
  "ds_write_b64 v13, v[50:51]\n"
  "s_waitcnt vmcnt(23)\n"
  "v_mfma_f32_16x16x32_bf16 v[40:43], v[144:147], a[88:91], v[40:43]\n"
  "ds_read_b128 v[136:139], v25 offset:6144\n"
  "s_waitcnt vmcnt(22)\n"
  "v_mfma_f32_16x16x32_bf16 v[56:59], v[144:147], a[92:95], v[56:59]\n"
  "s_add_u32 m0, m0, 0x1000\n"
  "s_add_u32 s11, s11, 0x1000\n"
  "buffer_load_dwordx4 v21, s[12:15], s11 offen sc1 lds\n"
  "s_waitcnt lgkmcnt(4)\n"
  "v_mfma_f32_16x16x32_bf16 v[88:91], v[148:151], a[88:91], v[88:91]\n"
  "ds_read_b128 v[140:143], v25 offset:7168\n"
  "v_mfma_f32_16x16x32_bf16 v[92:95], v[148:151], a[92:95], v[92:95]\n"
  "s_add_u32 s56, s56, 0x1000\n"
  "buffer_load_dwordx4 a[152:155], v21, s[12:15], s56 offen sc1\n"
  "v_mfma_f32_16x16x32_bf16 v[60:63], v[152:155], a[88:91], v[60:63]\n"
  "ds_read_b128 v[144:147], v25 offset:8192\n"
  "v_mfma_f32_16x16x32_bf16 v[68:71], v[152:155], a[92:95], v[68:71]\n"
  "s_add_u32 s57, s57, 0x1000\n"
  "buffer_load_dwordx4 a[156:159], v21, s[12:15], s57 offen sc1\n"
  "s_add_u32 s64, s66, 0x11000\n"
  "s_mov_b64 exec, 1\n"
  "buffer_store_dwordx2 v[10:11], off, s[12:15], s64 sc1\n"
  "s_mov_b64 exec, -1\n"
  "v_mfma_f32_16x16x32_bf16 v[168:171], v[156:159], a[88:91], v[168:171]\n"
  "ds_read_b128 v[148:151], v25 offset:9216\n"
  "v_mfma_f32_16x16x32_bf16 v[172:175], v[156:159], a[92:95], v[172:175]\n"
  "s_add_u32 m0, s9, 0x1e000\n"
  "s_add_u32 s11, s44, 0x14000\n"
  "buffer_load_dwordx4 v21, s[12:15], s11 offen sc1 lds\n"
  "s_waitcnt lgkmcnt(3)\n"
  "s_waitcnt vmcnt(22)\n"
  "v_mfma_f32_16x16x32_bf16 v[40:43], v[128:131], a[96:99], v[40:43]\n"
  "ds_read_b128 v[152:155], v25 offset:10240\n"
  "s_waitcnt vmcnt(21)\n"
  "v_mfma_f32_16x16x32_bf16 v[56:59], v[128:131], a[100:103], v[56:59]\n"
  "v_mfma_f32_16x16x32_bf16 v[88:91], v[132:135], a[96:99], v[88:91]\n"
  "ds_read_b128 v[156:159], v25 offset:11264\n"
  "v_mfma_f32_16x16x32_bf16 v[92:95], v[132:135], a[100:103], v[92:95]\n"
  "buffer_load_dwordx4 v21, s[12:15], s11 offen offset:1024 sc1 lds\n"
  "v_mfma_f32_16x16x32_bf16 v[60:63], v[136:139], a[96:99], v[60:63]\n"
  "ds_read_b128 v[128:131], v25 offset:12288\n"
  "v_mfma_f32_16x16x32_bf16 v[68:71], v[136:139], a[100:103], v[68:71]\n"
  "s_waitcnt lgkmcnt(3)\n"
  "v_mfma_f32_16x16x32_bf16 v[168:171], v[140:143], a[96:99], v[168:171]\n"
  "ds_read_b128 v[132:135], v25 offset:13312\n"
  "v_mfma_f32_16x16x32_bf16 v[172:175], v[140:143], a[100:103], v[172:175]\n"
  "buffer_load_dwordx4 v21, s[12:15], s11 offen offset:2048 sc1 lds\n"
  "s_waitcnt vmcnt(22)\n"
  "v_mfma_f32_16x16x32_bf16 v[40:43], v[144:147], a[104:107], v[40:43]\n"
  "ds_read_b128 v[136:139], v25 offset:14336\n"
  "s_waitcnt vmcnt(21)\n"
  "v_mfma_f32_16x16x32_bf16 v[56:59], v[144:147], a[108:111], v[56:59]\n"
  "v_mfma_f32_16x16x32_bf16 v[88:91], v[148:151], a[104:107], v[88:91]\n"
  "ds_read_b128 v[140:143], v25 offset:15360\n"
  "v_mfma_f32_16x16x32_bf16 v[92:95], v[148:151], a[108:111], v[92:95]\n"
  "buffer_load_dwordx4 v21, s[12:15], s11 offen offset:3072 sc1 lds\n"
  "s_waitcnt lgkmcnt(3)\n"
  "v_mfma_f32_16x16x32_bf16 v[60:63], v[152:155], a[104:107], v[60:63]\n"
  "s_waitcnt lgkmcnt(0)\n"
  "s_barrier\n"
  "ds_read_b128 v[144:147], v25 offset:16384\n"
  "v_mfma_f32_16x16x32_bf16 v[68:71], v[152:155], a[108:111], v[68:71]\n"
  "v_mfma_f32_16x16x32_bf16 v[168:171], v[156:159], a[104:107], v[168:171]\n"
  "ds_read_b128 v[148:151], v25 offset:17408\n"
  "ds_read_b64 a[224:225], v23\n"
  "ds_read_b64 a[226:227], v23 offset:768\n"
  "v_mfma_f32_16x16x32_bf16 v[172:175], v[156:159], a[108:111], v[172:175]\n"
  "s_add_u32 m0, m0, 0x1000\n"
  "s_add_u32 s11, s11, 0x1000\n"
  "buffer_load_dwordx4 v21, s[12:15], s11 offen sc1 lds\n"
  "s_waitcnt vmcnt(22)\n"
  "v_mfma_f32_16x16x32_bf16 v[40:43], v[128:131], a[112:115], v[40:43]\n"
  "ds_read_b128 v[152:155], v25 offset:18432\n"
  "ds_read_b64 a[228:229], v23 offset:128\n"
  "ds_read_b64 a[230:231], v23 offset:896\n"
  "s_waitcnt vmcnt(21)\n"
  "v_mfma_f32_16x16x32_bf16 v[56:59], v[128:131], a[116:119], v[56:59]\n"
  "ds_read_b64 a[232:233], v23 offset:256\n"
  "ds_read_b64 a[234:235], v23 offset:1024\n"
  "v_mfma_f32_16x16x32_bf16 v[88:91], v[132:135], a[112:115], v[88:91]\n"
  "ds_read_b128 v[156:159], v25 offset:19456\n"
  "ds_read_b64 a[236:237], v23 offset:384\n"
  "ds_read_b64 a[238:239], v23 offset:1152\n"
  "v_mfma_f32_16x16x32_bf16 v[92:95], v[132:135], a[116:119], v[92:95]\n"
  "s_add_u32 m0, s9, 0xa000\n"
  "s_add_u32 s11, s42, 0x1e000\n"
  "buffer_load_dwordx4 v21, s[12:15], s11 offen sc1 lds\n"
  "v_mfma_f32_16x16x32_bf16 v[60:63], v[136:139], a[112:115], v[60:63]\n"
  "ds_read_b128 v[128:131], v25 offset:20480\n"
  "ds_read_b64 a[240:241], v23 offset:512\n"
  "ds_read_b64 a[242:243], v23 offset:1280\n"
  "v_mfma_f32_16x16x32_bf16 v[68:71], v[136:139], a[116:119], v[68:71]\n"
  "ds_read_b64 a[244:245], v23 offset:640\n"
  "ds_read_b64 a[246:247], v23 offset:1408\n"
  "v_add_f32 v192, v192, v194\n"
  "v_mfma_f32_16x16x32_bf16 v[168:171], v[140:143], a[112:115], v[168:171]\n"
  "ds_read_b128 v[132:135], v25 offset:21504\n"
  "v_add_f32 v196, v196, v198\n"
  "v_add_f32 v193, v193, v195\n"
  "v_mfma_f32_16x16x32_bf16 v[172:175], v[140:143], a[116:119], v[172:175]\n"
  "buffer_load_dwordx4 v21, s[12:15], s11 offen offset:1024 sc1 lds\n"
  "v_add_f32 v197, v197, v199\n"
  "s_waitcnt lgkmcnt(13)\n"
  "s_waitcnt vmcnt(22)\n"
  "v_mfma_f32_16x16x32_bf16 v[40:43], v[144:147], a[120:123], v[40:43]\n"
  "ds_read_b128 v[136:139], v25 offset:22528\n"
  "v_mov_b32 v194, v192\n"
  "v_mov_b32 v198, v196\n"
  "s_waitcnt vmcnt(21)\n"
  "v_mfma_f32_16x16x32_bf16 v[56:59], v[144:147], a[124:127], v[56:59]\n"
  "v_mov_b32 v195, v193\n"
  "v_mov_b32 v199, v197\n"
  "v_mfma_f32_16x16x32_bf16 v[88:91], v[148:151], a[120:123], v[88:91]\n"
  "ds_read_b128 v[140:143], v25 offset:23552\n"
  "v_permlane16_swap_b32 v192, v194\n"
  "v_permlane16_swap_b32 v196, v198\n"
  "v_mfma_f32_16x16x32_bf16 v[92:95], v[148:151], a[124:127], v[92:95]\n"
  "buffer_load_dwordx4 v21, s[12:15], s11 offen offset:2048 sc1 lds\n"
  "v_permlane16_swap_b32 v193, v195\n"
  "v_permlane16_swap_b32 v197, v199\n"
  "v_mfma_f32_16x16x32_bf16 v[60:63], v[152:155], a[120:123], v[60:63]\n"
  "ds_read_b128 v[144:147], v25 offset:24576\n"
  "v_add_f32 v192, v192, v194\n"
  "v_add_f32 v196, v196, v198\n"
  "v_mfma_f32_16x16x32_bf16 v[68:71], v[152:155], a[124:127], v[68:71]\n"
  "v_add_f32 v193, v193, v195\n"
  "v_add_f32 v197, v197, v199\n"
  "s_waitcnt lgkmcnt(3)\n"
  "v_mfma_f32_16x16x32_bf16 v[168:171], v[156:159], a[120:123], v[168:171]\n"
  "ds_read_b128 v[148:151], v25 offset:25600\n"
  "v_mov_b32 v194, v192\n"
  "v_mov_b32 v198, v196\n"
  "v_mfma_f32_16x16x32_bf16 v[172:175], v[156:159], a[124:127], v[172:175]\n"
  "buffer_load_dwordx4 v21, s[12:15], s11 offen offset:3072 sc1 lds\n"
  "v_mov_b32 v195, v193\n"
  "v_mov_b32 v199, v197\n"
  "s_waitcnt vmcnt(22)\n"
  "v_mfma_f32_16x16x32_bf16 v[40:43], v[128:131], a[128:131], v[40:43]\n"
  "ds_read_b128 v[152:155], v25 offset:26624\n"
  "v_permlane32_swap_b32 v192, v194\n"
  "v_permlane32_swap_b32 v196, v198\n"
  "s_waitcnt vmcnt(21)\n"
  "v_mfma_f32_16x16x32_bf16 v[56:59], v[128:131], a[132:135], v[56:59]\n"
  "v_permlane32_swap_b32 v193, v195\n"
  "v_permlane32_swap_b32 v197, v199\n"
  "v_mfma_f32_16x16x32_bf16 v[88:91], v[132:135], a[128:131], v[88:91]\n"
  "ds_read_b128 v[156:159], v25 offset:27648\n"
  "v_add_f32 v192, v192, v194\n"
  "v_add_f32 v196, v196, v198\n"
  "v_mfma_f32_16x16x32_bf16 v[92:95], v[132:135], a[132:135], v[92:95]\n"
  "s_add_u32 m0, m0, 0x1000\n"
  "s_add_u32 s11, s11, 0x1000\n"
  "buffer_load_dwordx4 v21, s[12:15], s11 offen sc1 lds\n"
  "s_waitcnt lgkmcnt(3)\n"
  "v_mfma_f32_16x16x32_bf16 v[60:63], v[136:139], a[128:131], v[60:63]\n"
  "ds_read_b128 v[128:131], v25 offset:28672\n"
  "v_add_f32 v193, v193, v195\n"
  "v_add_f32 v197, v197, v199\n"
  "v_mfma_f32_16x16x32_bf16 v[68:71], v[136:139], a[132:135], v[68:71]\n"
  "v_mul_f32 v192, 0xbacccccd, v192\n"
  "v_mul_f32 v196, 0xbacccccd, v196\n"
  "v_mfma_f32_16x16x32_bf16 v[168:171], v[140:143], a[128:131], v[168:171]\n"
  "ds_read_b128 v[132:135], v25 offset:29696\n"
  "v_mul_f32 v193, 0x3acccccd, v193\n"
  "v_mul_f32 v197, 0x3acccccd, v197\n"
  "v_mfma_f32_16x16x32_bf16 v[172:175], v[140:143], a[132:135], v[172:175]\n"
  "v_fma_f32 v193, -v192, v192, v193\n"
  "v_fma_f32 v197, -v196, v196, v197\n"
  "s_waitcnt vmcnt(21)\n"
  "v_mfma_f32_16x16x32_bf16 v[40:43], v[144:147], a[136:139], v[40:43]\n"
  "ds_read_b128 v[136:139], v25 offset:30720\n"
  "v_max_f32 v193, 0, v193\n"
  "v_max_f32 v197, 0, v197\n"
  "s_waitcnt vmcnt(20)\n"
  "v_mfma_f32_16x16x32_bf16 v[56:59], v[144:147], a[140:143], v[56:59]\n"
  "v_add_f32 v210, 0x3727c5ac, v193\n"
  "v_add_f32 v212, 0x3727c5ac, v197\n"
  "s_waitcnt lgkmcnt(3)\n"
  "v_mfma_f32_16x16x32_bf16 v[88:91], v[148:151], a[136:139], v[88:91]\n"
  "ds_read_b128 v[140:143], v25 offset:31744\n"
  "v_rsq_f32 v193, v210\n"
  "v_mfma_f32_16x16x32_bf16 v[92:95], v[148:151], a[140:143], v[92:95]\n"
  "v_rsq_f32 v197, v212\n"
  "v_mfma_f32_16x16x32_bf16 v[60:63], v[152:155], a[136:139], v[60:63]\n"
  "ds_read_b128 v[144:147], v25 offset:32768\n"
  "v_cvt_pk_bf16_f32 v201, v192, 0\n"
  "v_cvt_pk_bf16_f32 v205, v196, 0\n"
  "v_mfma_f32_16x16x32_bf16 v[68:71], v[152:155], a[140:143], v[68:71]\n"
  "v_lshlrev_b32 v194, 16, v201\n"
  "v_lshlrev_b32 v198, 16, v205\n"
  "v_mfma_f32_16x16x32_bf16 v[168:171], v[156:159], a[136:139], v[168:171]\n"
  "ds_read_b128 v[148:151], v25 offset:33792\n"
  "v_sub_f32 v194, v192, v194\n"
  "v_sub_f32 v198, v196, v198\n"
  "v_mfma_f32_16x16x32_bf16 v[172:175], v[156:159], a[140:143], v[172:175]\n"
  "v_cvt_pk_bf16_f32 v200, v192, v194\n"
  "v_cvt_pk_bf16_f32 v204, v196, v198\n"
  "s_waitcnt lgkmcnt(3)\n"
  "s_waitcnt vmcnt(19)\n"
  "v_mfma_f32_16x16x32_bf16 v[40:43], v[128:131], a[144:147], v[40:43]\n"
  "ds_read_b128 v[152:155], v25 offset:34816\n"
  "v_cndmask_b32 v201, 0, v201, s[20:21]\n"
  "v_cndmask_b32 v205, 0, v205, s[20:21]\n"
  "s_waitcnt vmcnt(18)\n"
  "v_mfma_f32_16x16x32_bf16 v[56:59], v[128:131], a[148:151], v[56:59]\n"
  "v_cndmask_b32 v200, 0, v200, s[20:21]\n"
  "v_cndmask_b32 v204, 0, v204, s[20:21]\n"
  "v_mfma_f32_16x16x32_bf16 v[88:91], v[132:135], a[144:147], v[88:91]\n"
  "ds_read_b128 v[156:159], v25 offset:35840\n"
  "v_mov_b32 v202, 0\n"
  "v_mov_b32 v206, 0\n"
  "v_mfma_f32_16x16x32_bf16 v[92:95], v[132:135], a[148:151], v[92:95]\n"
  "v_mov_b32 v203, 0\n"
  "v_mov_b32 v207, 0\n"
  "v_mfma_f32_16x16x32_bf16 v[60:63], v[136:139], a[144:147], v[60:63]\n"
  "s_waitcnt vmcnt(13)\n"
  "s_barrier\n"
  "ds_read_b128 v[128:131], v25 offset:36864\n"
  "v_mfma_f32_16x16x32_bf16 v[68:71], v[136:139], a[148:151], v[68:71]\n"
  "v_mul_f32 v38, 0xc0135761, v193\n"
  "v_mul_f32 v44, 0xc0135761, v197\n"
  "s_waitcnt lgkmcnt(3)\n"
  "v_mfma_f32_16x16x32_bf16 v[168:171], v[140:143], a[144:147], v[168:171]\n"
  "ds_read_b128 v[132:135], v25 offset:37888\n"
  "v_mul_f32 v194, v193, v193\n"
  "v_mul_f32 v198, v197, v197\n"
  "v_mfma_f32_16x16x32_bf16 v[172:175], v[140:143], a[148:151], v[172:175]\n"
  "s_add_u32 m0, s9, 0x23000\n"
  "s_add_u32 s11, s44, 0x1e000\n"
  "buffer_load_dwordx4 v21, s[12:15], s11 offen sc1 lds\n"
  "s_waitcnt vmcnt(13)\n"
  "v_mfma_f32_16x16x32_bf16 v[40:43], v[144:147], a[152:155], v[40:43]\n"
  "ds_read_b128 v[136:139], v25 offset:38912\n"
  "v_mul_f32 v106, 0x3d372713, v38\n"
  "v_mul_f32 v208, 0x3d372713, v44\n"
  "s_waitcnt vmcnt(12)\n"
  "v_mfma_f32_16x16x32_bf16 v[56:59], v[144:147], a[156:159], v[56:59]\n"
  "v_mul_f32 v106, v106, v194\n"
  "v_mul_f32 v208, v208, v198\n"
  "v_mfma_f32_16x16x32_bf16 v[88:91], v[148:151], a[152:155], v[88:91]\n"
  "ds_read_b128 v[140:143], v25 offset:39936\n"
  "v_mfma_f32_16x16x32_bf16 v[92:95], v[148:151], a[156:159], v[92:95]\n"
  "buffer_load_dwordx4 v21, s[12:15], s11 offen offset:1024 sc1 lds\n"
  "s_waitcnt lgkmcnt(3)\n"
  "v_mfma_f32_16x16x32_bf16 v[60:63], v[152:155], a[152:155], v[60:63]\n"
  "ds_read_b128 v[144:147], v25 offset:40960\n"
  "v_mfma_f32_16x16x32_bf16 v[68:71], v[152:155], a[156:159], v[68:71]\n"
  "v_mfma_f32_16x16x32_bf16 v[168:171], v[156:159], a[152:155], v[168:171]\n"
  "ds_read_b128 v[148:151], v25 offset:41984\n"
  "v_mfma_f32_16x16x32_bf16 v[172:175], v[156:159], a[156:159], v[172:175]\n"
  "buffer_load_dwordx4 v21, s[12:15], s11 offen offset:2048 sc1 lds\n"
  "v_mfma_f32_16x16x32_bf16 v[40:43], a[224:227], v[200:203], v[40:43]\n"
  "v_mfma_f32_16x16x32_bf16 v[56:59], a[224:227], v[204:207], v[56:59]\n"
  "v_mfma_f32_16x16x32_bf16 v[88:91], a[226:229], v[200:203], v[88:91]\n"
  "v_mfma_f32_16x16x32_bf16 v[92:95], a[226:229], v[204:207], v[92:95]\n"
  "v_mfma_f32_16x16x32_bf16 v[60:63], a[228:231], v[200:203], v[60:63]\n"
  "v_mfma_f32_16x16x32_bf16 v[68:71], a[228:231], v[204:207], v[68:71]\n"
  "v_mfma_f32_16x16x32_bf16 v[168:171], a[230:233], v[200:203], v[168:171]\n"
  "v_mfma_f32_16x16x32_bf16 v[172:175], a[230:233], v[204:207], v[172:175]\n"
  "v_mfma_f32_16x16x32_bf16 v[72:75], v[128:131], a[0:3], 0\n"
  "ds_read_b128 v[152:155], v25 offset:43008\n"
  "v_mfma_f32_16x16x32_bf16 v[76:79], v[128:131], a[4:7], 0\n"
  "s_waitcnt lgkmcnt(3)\n"
  "v_mfma_f32_16x16x32_bf16 v[176:179], v[132:135], a[0:3], 0\n"
  "ds_read_b128 v[156:159], v25 offset:44032\n"
  "v_mul_f32 v216, v40, v40\n"
  "v_mul_f32 v217, v41, v41\n"
  "v_mfma_f32_16x16x32_bf16 v[180:183], v[132:135], a[4:7], 0\n"
  "buffer_load_dwordx4 v21, s[12:15], s11 offen offset:3072 sc1 lds\n"
  "v_mul_f32 v218, v42, v42\n"
  "v_mul_f32 v219, v43, v43\n"
  "v_mfma_f32_16x16x32_bf16 v[72:75], v[136:139], a[8:11], v[72:75]\n"
  "ds_read_b128 v[128:131], v25 offset:45056\n"
  "v_fma_f32 v216, v216, v106, v38\n"
  "v_fma_f32 v217, v217, v106, v38\n"
  "v_mfma_f32_16x16x32_bf16 v[76:79], v[136:139], a[12:15], v[76:79]\n"
  "v_fma_f32 v218, v218, v106, v38\n"
  "v_fma_f32 v219, v219, v106, v38\n"
  "v_mfma_f32_16x16x32_bf16 v[176:179], v[140:143], a[8:11], v[176:179]\n"
  "ds_read_b128 v[132:135], v25 offset:46080\n"
  "v_mul_f32 v216, v216, v40\n"
  "v_mul_f32 v217, v217, v41\n"
  "v_mfma_f32_16x16x32_bf16 v[180:183], v[140:143], a[12:15], v[180:183]\n"
  "s_add_u32 m0, m0, 0x1000\n"
  "s_add_u32 s11, s11, 0x1000\n"
  "buffer_load_dwordx4 v21, s[12:15], s11 offen sc1 lds\n"
  "s_waitcnt lgkmcnt(3)\n"
  "v_mfma_f32_16x16x32_bf16 v[72:75], v[144:147], a[16:19], v[72:75]\n"
  "ds_read_b128 v[136:139], v25 offset:47104\n"
  "v_mul_f32 v218, v218, v42\n"
  "v_mul_f32 v219, v219, v43\n"
  "v_mfma_f32_16x16x32_bf16 v[76:79], v[144:147], a[20:23], v[76:79]\n"
  "v_exp_f32 v216, v216\n"
  "v_mfma_f32_16x16x32_bf16 v[176:179], v[148:151], a[16:19], v[176:179]\n"
  "ds_read_b128 v[140:143], v25 offset:48128\n"
  "v_mul_f32 v88, v88, v40\n"
  "v_mfma_f32_16x16x32_bf16 v[180:183], v[148:151], a[20:23], v[180:183]\n"
  "s_add_u32 m0, s9, 0\n"
  "s_add_u32 s11, s42, 0x28000\n"
  "buffer_load_dwordx4 v21, s[12:15], s11 offen sc1 lds\n"
  "v_mfma_f32_16x16x32_bf16 v[72:75], v[152:155], a[24:27], v[72:75]\n"
  "ds_read_b128 v[144:147], v25 offset:49152\n"
  "v_exp_f32 v217, v217\n"
  "v_mfma_f32_16x16x32_bf16 v[76:79], v[152:155], a[28:31], v[76:79]\n"
  "v_mul_f32 v89, v89, v41\n"
  "s_waitcnt lgkmcnt(3)\n"
  "v_mfma_f32_16x16x32_bf16 v[176:179], v[156:159], a[24:27], v[176:179]\n"
  "ds_read_b128 v[148:151], v25 offset:50176\n"
  "v_exp_f32 v218, v218\n"
  "v_mfma_f32_16x16x32_bf16 v[180:183], v[156:159], a[28:31], v[180:183]\n"
  "buffer_load_dwordx4 v21, s[12:15], s11 offen offset:1024 sc1 lds\n"
  "v_mul_f32 v90, v90, v42\n"
  "v_mfma_f32_16x16x32_bf16 v[72:75], v[128:131], a[32:35], v[72:75]\n"
  "ds_read_b128 v[152:155], v25 offset:51200\n"
  "v_exp_f32 v219, v219\n"
  "v_mfma_f32_16x16x32_bf16 v[76:79], v[128:131], a[36:39], v[76:79]\n"
  "v_mul_f32 v91, v91, v43\n"
  "v_fma_f32 v216, v216, v210, v210\n"
  "v_mfma_f32_16x16x32_bf16 v[176:179], v[132:135], a[32:35], v[176:179]\n"
  "ds_read_b128 v[156:159], v25 offset:52224\n"
  "v_fma_f32 v217, v217, v210, v210\n"
  "v_fma_f32 v218, v218, v210, v210\n"
  "v_mfma_f32_16x16x32_bf16 v[180:183], v[132:135], a[36:39], v[180:183]\n"
  "buffer_load_dwordx4 v21, s[12:15], s11 offen offset:2048 sc1 lds\n"
  "v_fma_f32 v219, v219, v210, v210\n"
  "s_waitcnt lgkmcnt(3)\n"
  "v_mfma_f32_16x16x32_bf16 v[72:75], v[136:139], a[40:43], v[72:75]\n"
  "ds_read_b128 v[128:131], v25 offset:53248\n"
  "v_rcp_f32 v216, v216\n"
  "v_mfma_f32_16x16x32_bf16 v[76:79], v[136:139], a[44:47], v[76:79]\n"
  "v_rcp_f32 v217, v217\n"
  "v_mfma_f32_16x16x32_bf16 v[176:179], v[140:143], a[40:43], v[176:179]\n"
  "ds_read_b128 v[132:135], v25 offset:54272\n"
  "v_rcp_f32 v218, v218\n"
  "v_mfma_f32_16x16x32_bf16 v[180:183], v[140:143], a[44:47], v[180:183]\n"
  "buffer_load_dwordx4 v21, s[12:15], s11 offen offset:3072 sc1 lds\n"
  "v_rcp_f32 v219, v219\n"
  "v_mfma_f32_16x16x32_bf16 v[72:75], v[144:147], a[48:51], v[72:75]\n"
  "ds_read_b128 v[136:139], v25 offset:55296\n"
  "v_mul_f32 v88, v88, v216\n"
  "v_mul_f32 v89, v89, v217\n"
  "v_mfma_f32_16x16x32_bf16 v[76:79], v[144:147], a[52:55], v[76:79]\n"
  "v_mul_f32 v90, v90, v218\n"
  "v_mul_f32 v91, v91, v219\n"
  "s_waitcnt lgkmcnt(3)\n"
  "v_mfma_f32_16x16x32_bf16 v[176:179], v[148:151], a[48:51], v[176:179]\n"
  "ds_read_b128 v[140:143], v25 offset:56320\n"
  "v_cvt_pk_bf16_f32 v224, v88, v89\n"
  "v_cvt_pk_bf16_f32 v225, v90, v91\n"
  "v_mfma_f32_16x16x32_bf16 v[180:183], v[148:151], a[52:55], v[180:183]\n"
  "s_add_u32 m0, m0, 0x1000\n"
  "s_add_u32 s11, s11, 0x1000\n"
  "buffer_load_dwordx4 v21, s[12:15], s11 offen sc1 lds\n"
  "v_mfma_f32_16x16x32_bf16 v[72:75], v[152:155], a[56:59], v[72:75]\n"
  "s_waitcnt vmcnt(15)\n"
  "s_barrier\n"
  "ds_read_b128 v[144:147], v25 offset:57344\n"
  "v_mfma_f32_16x16x32_bf16 v[76:79], v[152:155], a[60:63], v[76:79]\n"
  "buffer_load_dwordx2 v[46:47], v34, s[12:15], s19 offen sc1\n"
  "v_mul_f32 v220, v56, v56\n"
  "v_mul_f32 v221, v57, v57\n"
  "v_mfma_f32_16x16x32_bf16 v[176:179], v[156:159], a[56:59], v[176:179]\n"
  "ds_read_b128 v[148:151], v25 offset:58368\n"
  "v_mul_f32 v222, v58, v58\n"
  "v_mul_f32 v223, v59, v59\n"
  "v_mfma_f32_16x16x32_bf16 v[180:183], v[156:159], a[60:63], v[180:183]\n"
  "s_add_u32 m0, s9, 0x5000\n"
  "s_add_u32 s11, s44, 0x28000\n"
  "buffer_load_dwordx4 v21, s[12:15], s11 offen sc1 lds\n"
  "s_waitcnt lgkmcnt(3)\n"
  "v_mfma_f32_16x16x32_bf16 v[72:75], v[128:131], a[64:67], v[72:75]\n"
  "ds_read_b128 v[152:155], v25 offset:59392\n"
  "v_fma_f32 v220, v220, v208, v44\n"
  "v_fma_f32 v221, v221, v208, v44\n"
  "v_mfma_f32_16x16x32_bf16 v[76:79], v[128:131], a[68:71], v[76:79]\n"
  "v_fma_f32 v222, v222, v208, v44\n"
  "v_fma_f32 v223, v223, v208, v44\n"
  "v_mfma_f32_16x16x32_bf16 v[176:179], v[132:135], a[64:67], v[176:179]\n"
  "ds_read_b128 v[156:159], v25 offset:60416\n"
  "v_mul_f32 v220, v220, v56\n"
  "v_mul_f32 v221, v221, v57\n"
  "v_mfma_f32_16x16x32_bf16 v[180:183], v[132:135], a[68:71], v[180:183]\n"
  "buffer_load_dwordx4 v21, s[12:15], s11 offen offset:1024 sc1 lds\n"
  "v_mul_f32 v222, v222, v58\n"
  "v_mul_f32 v223, v223, v59\n"
  "v_mfma_f32_16x16x32_bf16 v[72:75], v[136:139], a[72:75], v[72:75]\n"
  "ds_read_b128 v[128:131], v25 offset:61440\n"
  "v_exp_f32 v220, v220\n"
  "v_mfma_f32_16x16x32_bf16 v[76:79], v[136:139], a[76:79], v[76:79]\n"
  "v_mul_f32 v92, v92, v56\n"
  "s_waitcnt lgkmcnt(3)\n"
  "v_mfma_f32_16x16x32_bf16 v[176:179], v[140:143], a[72:75], v[176:179]\n"
  "ds_read_b128 v[132:135], v25 offset:62464\n"
  "v_exp_f32 v221, v221\n"
  "v_mfma_f32_16x16x32_bf16 v[180:183], v[140:143], a[76:79], v[180:183]\n"
  "buffer_load_dwordx4 v21, s[12:15], s11 offen offset:2048 sc1 lds\n"
  "v_mul_f32 v93, v93, v57\n"
  "v_mfma_f32_16x16x32_bf16 v[72:75], v[144:147], a[80:83], v[72:75]\n"
  "ds_read_b128 v[136:139], v25 offset:63488\n"
  "v_exp_f32 v222, v222\n"
  "v_mfma_f32_16x16x32_bf16 v[76:79], v[144:147], a[84:87], v[76:79]\n"
  "v_mul_f32 v94, v94, v58\n"
  "v_mfma_f32_16x16x32_bf16 v[176:179], v[148:151], a[80:83], v[176:179]\n"
  "ds_read_b128 v[140:143], v25 offset:64512\n"
  "v_exp_f32 v223, v223\n"
  "v_mfma_f32_16x16x32_bf16 v[180:183], v[148:151], a[84:87], v[180:183]\n"
  "buffer_load_dwordx4 v21, s[12:15], s11 offen offset:3072 sc1 lds\n"
  "v_mul_f32 v95, v95, v59\n"
  "s_waitcnt lgkmcnt(3)\n"
  "v_mfma_f32_16x16x32_bf16 v[72:75], v[152:155], a[88:91], v[72:75]\n"
  "ds_read_b128 v[144:147], v26\n"
  "v_fma_f32 v220, v220, v212, v212\n"
  "v_fma_f32 v221, v221, v212, v212\n"
  "v_mfma_f32_16x16x32_bf16 v[76:79], v[152:155], a[92:95], v[76:79]\n"
  "v_fma_f32 v222, v222, v212, v212\n"
  "v_fma_f32 v223, v223, v212, v212\n"
  "v_mfma_f32_16x16x32_bf16 v[176:179], v[156:159], a[88:91], v[176:179]\n"
  "ds_read_b128 v[148:151], v26 offset:1024\n"
  "v_rcp_f32 v220, v220\n"
  "v_mfma_f32_16x16x32_bf16 v[180:183], v[156:159], a[92:95], v[180:183]\n"
  "s_add_u32 m0, m0, 0x1000\n"
  "s_add_u32 s11, s11, 0x1000\n"
  "buffer_load_dwordx4 v21, s[12:15], s11 offen sc1 lds\n"
  "v_mfma_f32_16x16x32_bf16 v[72:75], v[128:131], a[96:99], v[72:75]\n"
  "ds_read_b128 v[152:155], v26 offset:2048\n"
  "v_rcp_f32 v221, v221\n"
  "v_mfma_f32_16x16x32_bf16 v[76:79], v[128:131], a[100:103], v[76:79]\n"
  "v_rcp_f32 v222, v222\n"
  "s_waitcnt lgkmcnt(3)\n"
  "v_mfma_f32_16x16x32_bf16 v[176:179], v[132:135], a[96:99], v[176:179]\n"
  "ds_read_b128 v[156:159], v26 offset:3072\n"
  "v_rcp_f32 v223, v223\n"
  "v_mfma_f32_16x16x32_bf16 v[180:183], v[132:135], a[100:103], v[180:183]\n"
  "s_add_u32 m0, s9, 0xf000\n"
  "s_add_u32 s11, s42, 0x32000\n"
  "buffer_load_dwordx4 v21, s[12:15], s11 offen sc1 lds\n"
  "v_mfma_f32_16x16x32_bf16 v[72:75], v[136:139], a[104:107], v[72:75]\n"
  "ds_read_b128 v[128:131], v26 offset:4096\n"
  "v_mul_f32 v92, v92, v220\n"
  "v_mul_f32 v93, v93, v221\n"
  "v_mfma_f32_16x16x32_bf16 v[76:79], v[136:139], a[108:111], v[76:79]\n"
  "s_waitcnt vmcnt(6)\n"
  "v_cmp_ne_u64 vcc, s[72:73], v[46:47]\n"
  "s_nop 3\n"
  "s_cbranch_vccz .Lhh_done_20x\n"
  "s_mov_b32 s63, 0x10000\n"
  ".Lhh_poll_19:\n"
  "buffer_load_dwordx2 v[46:47], v34, s[12:15], s19 offen sc1\n"
  "s_waitcnt vmcnt(0)\n"
  "v_cmp_ne_u64 vcc, s[72:73], v[46:47]\n"
  "s_nop 4\n"
  "s_cbranch_vccz .Lhh_done_20\n"
  "s_sleep 1\n"
  "s_sub_u32 s63, s63, 1\n"
  "s_cbranch_scc0 .Lhh_poll_19\n"
  ".Lhh_done_20:\n"
  ".Lhh_done_20x:\n"
  "v_mfma_f32_16x16x32_bf16 v[176:179], v[140:143], a[104:107], v[176:179]\n"
  "ds_read_b128 v[132:135], v26 offset:5120\n"
  "v_mul_f32 v94, v94, v222\n"
  "v_mul_f32 v95, v95, v223\n"
  "v_mfma_f32_16x16x32_bf16 v[180:183], v[140:143], a[108:111], v[180:183]\n"
  "buffer_load_dwordx4 v21, s[12:15], s11 offen offset:1024 sc1 lds\n"
  "v_cvt_pk_bf16_f32 v228, v92, v93\n"
  "s_waitcnt lgkmcnt(3)\n"
  "v_mfma_f32_16x16x32_bf16 v[72:75], v[144:147], a[112:115], v[72:75]\n"
  "ds_read_b128 v[136:139], v26 offset:6144\n"
  "v_cvt_pk_bf16_f32 v229, v94, v95\n"
  "v_mul_f32 v216, v60, v60\n"
  "v_mfma_f32_16x16x32_bf16 v[76:79], v[144:147], a[116:119], v[76:79]\n"
  "v_mul_f32 v217, v61, v61\n"
  "v_mul_f32 v218, v62, v62\n"
  "v_mfma_f32_16x16x32_bf16 v[176:179], v[148:151], a[112:115], v[176:179]\n"
  "ds_read_b128 v[140:143], v26 offset:7168\n"
  "v_mul_f32 v219, v63, v63\n"
  "v_fma_f32 v216, v216, v106, v38\n"
  "v_mfma_f32_16x16x32_bf16 v[180:183], v[148:151], a[116:119], v[180:183]\n"
  "buffer_load_dwordx4 v21, s[12:15], s11 offen offset:2048 sc1 lds\n"
  "v_fma_f32 v217, v217, v106, v38\n"
  "v_fma_f32 v218, v218, v106, v38\n"
  "v_mfma_f32_16x16x32_bf16 v[72:75], v[152:155], a[120:123], v[72:75]\n"
  "ds_read_b128 v[144:147], v26 offset:8192\n"
  "v_fma_f32 v219, v219, v106, v38\n"
  "v_mul_f32 v216, v216, v60\n"
  "v_mfma_f32_16x16x32_bf16 v[76:79], v[152:155], a[124:127], v[76:79]\n"
  "v_mul_f32 v217, v217, v61\n"
  "v_mul_f32 v218, v218, v62\n"
  "s_waitcnt lgkmcnt(3)\n"
  "v_mfma_f32_16x16x32_bf16 v[176:179], v[156:159], a[120:123], v[176:179]\n"
  "ds_read_b128 v[148:151], v26 offset:9216\n"
  "v_mul_f32 v219, v219, v63\n"
  "v_mfma_f32_16x16x32_bf16 v[180:183], v[156:159], a[124:127], v[180:183]\n"
  "buffer_load_dwordx4 v21, s[12:15], s11 offen offset:3072 sc1 lds\n"
  "v_exp_f32 v216, v216\n"
  "v_mfma_f32_16x16x32_bf16 v[72:75], v[128:131], a[128:131], v[72:75]\n"
  "ds_read_b128 v[152:155], v26 offset:10240\n"
  "v_mul_f32 v168, v168, v60\n"
  "v_mfma_f32_16x16x32_bf16 v[76:79], v[128:131], a[132:135], v[76:79]\n"
  "v_exp_f32 v217, v217\n"
  "v_mfma_f32_16x16x32_bf16 v[176:179], v[132:135], a[128:131], v[176:179]\n"
  "ds_read_b128 v[156:159], v26 offset:11264\n"
  "v_mul_f32 v169, v169, v61\n"
  "v_mfma_f32_16x16x32_bf16 v[180:183], v[132:135], a[132:135], v[180:183]\n"
  "s_add_u32 m0, m0, 0x1000\n"
  "s_add_u32 s11, s11, 0x1000\n"
  "buffer_load_dwordx4 v21, s[12:15], s11 offen sc1 lds\n"
  "s_waitcnt lgkmcnt(3)\n"
  "v_mfma_f32_16x16x32_bf16 v[72:75], v[136:139], a[136:139], v[72:75]\n"
  "s_barrier\n"
  "ds_read_b128 v[128:131], v21 offset:40960\n"
  "v_mfma_f32_16x16x32_bf16 v[76:79], v[136:139], a[140:143], v[76:79]\n"
  "v_exp_f32 v218, v218\n"
  "v_mfma_f32_16x16x32_bf16 v[176:179], v[140:143], a[136:139], v[176:179]\n"
  "ds_read_b128 v[132:135], v21 offset:41984\n"
  "v_mul_f32 v170, v170, v62\n"
  "v_mfma_f32_16x16x32_bf16 v[180:183], v[140:143], a[140:143], v[180:183]\n"
  "s_add_u32 m0, s9, 0x14000\n"
  "s_add_u32 s11, s44, 0x32000\n"
  "buffer_load_dwordx4 v21, s[12:15], s11 offen sc1 lds\n"
  "v_mfma_f32_16x16x32_bf16 v[72:75], v[144:147], a[144:147], v[72:75]\n"
  "ds_read_b128 v[136:139], v21 offset:43008\n"
  "v_exp_f32 v219, v219\n"
  "v_mfma_f32_16x16x32_bf16 v[76:79], v[144:147], a[148:151], v[76:79]\n"
  "v_mul_f32 v171, v171, v63\n"
  "v_fma_f32 v216, v216, v210, v210\n"
  "s_waitcnt lgkmcnt(3)\n"
  "v_mfma_f32_16x16x32_bf16 v[176:179], v[148:151], a[144:147], v[176:179]\n"
  "ds_read_b128 v[140:143], v21 offset:44032\n"
  "v_fma_f32 v217, v217, v210, v210\n"
  "v_fma_f32 v218, v218, v210, v210\n"
  "v_mfma_f32_16x16x32_bf16 v[180:183], v[148:151], a[148:151], v[180:183]\n"
  "buffer_load_dwordx4 v21, s[12:15], s11 offen offset:1024 sc1 lds\n"
  "v_fma_f32 v219, v219, v210, v210\n"
  "v_mfma_f32_16x16x32_bf16 v[72:75], v[152:155], a[152:155], v[72:75]\n"
  "ds_read_b128 v[144:147], v21 offset:45056\n"
  "v_rcp_f32 v216, v216\n"
  "v_mfma_f32_16x16x32_bf16 v[76:79], v[152:155], a[156:159], v[76:79]\n"
  "v_rcp_f32 v217, v217\n"
  "v_mfma_f32_16x16x32_bf16 v[176:179], v[156:159], a[152:155], v[176:179]\n"
  "ds_read_b128 v[148:151], v21 offset:46080\n"
  "v_rcp_f32 v218, v218\n"
  "v_mfma_f32_16x16x32_bf16 v[180:183], v[156:159], a[156:159], v[180:183]\n"
  "buffer_load_dwordx4 v21, s[12:15], s11 offen offset:2048 sc1 lds\n"
  "v_rcp_f32 v219, v219\n"
  "s_waitcnt lgkmcnt(3)\n"
  "v_mfma_f32_16x16x32_bf16 v[72:75], a[232:235], v[200:203], v[72:75]\n"
  "v_mfma_f32_16x16x32_bf16 v[76:79], a[232:235], v[204:207], v[76:79]\n"
  "v_mfma_f32_16x16x32_bf16 v[176:179], a[234:237], v[200:203], v[176:179]\n"
  "v_mfma_f32_16x16x32_bf16 v[180:183], a[234:237], v[204:207], v[180:183]\n"
  "v_mfma_f32_16x16x32_bf16 v[80:83], v[128:131], a[0:3], 0\n"
  "ds_read_b128 v[152:155], v21 offset:47104\n"
  "v_mul_f32 v168, v168, v216\n"
  "v_mul_f32 v169, v169, v217\n"
  "v_mfma_f32_16x16x32_bf16 v[84:87], v[128:131], a[4:7], 0\n"
  "v_mul_f32 v170, v170, v218\n"
  "v_mul_f32 v171, v171, v219\n"
  "v_mfma_f32_16x16x32_bf16 v[184:187], v[132:135], a[0:3], 0\n"
  "ds_read_b128 v[156:159], v21 offset:48128\n"
  "v_cvt_pk_bf16_f32 v226, v168, v169\n"
  "v_cvt_pk_bf16_f32 v227, v170, v171\n"
  "v_mfma_f32_16x16x32_bf16 v[188:191], v[132:135], a[4:7], 0\n"
  "buffer_load_dwordx4 v21, s[12:15], s11 offen offset:3072 sc1 lds\n"
  "buffer_store_dwordx4 v[224:227], v21, s[12:15], s49 offen sc1\n"
  "v_mul_f32 v220, v68, v68\n"
  "v_mfma_f32_16x16x32_bf16 v[80:83], v[136:139], a[8:11], v[80:83]\n"
  "ds_read_b128 v[128:131], v21 offset:49152\n"
  "v_mul_f32 v221, v69, v69\n"
  "v_mul_f32 v222, v70, v70\n"
  "v_mfma_f32_16x16x32_bf16 v[84:87], v[136:139], a[12:15], v[84:87]\n"
  "v_mul_f32 v223, v71, v71\n"
  "v_fma_f32 v220, v220, v208, v44\n"
  "s_waitcnt lgkmcnt(3)\n"
  "v_mfma_f32_16x16x32_bf16 v[184:187], v[140:143], a[8:11], v[184:187]\n"
  "ds_read_b128 v[132:135], v21 offset:50176\n"
  "v_fma_f32 v221, v221, v208, v44\n"
  "v_fma_f32 v222, v222, v208, v44\n"
  "v_mfma_f32_16x16x32_bf16 v[188:191], v[140:143], a[12:15], v[188:191]\n"
  "s_add_u32 m0, m0, 0x1000\n"
  "s_add_u32 s11, s11, 0x1000\n"
  "buffer_load_dwordx4 v21, s[12:15], s11 offen sc1 lds\n"
  "v_mfma_f32_16x16x32_bf16 v[80:83], v[144:147], a[16:19], v[80:83]\n"
  "ds_read_b128 v[136:139], v21 offset:51200\n"
  "v_fma_f32 v223, v223, v208, v44\n"
  "v_mul_f32 v220, v220, v68\n"
  "v_mfma_f32_16x16x32_bf16 v[84:87], v[144:147], a[20:23], v[84:87]\n"
  "v_mul_f32 v221, v221, v69\n"
  "v_mul_f32 v222, v222, v70\n"
  "v_mfma_f32_16x16x32_bf16 v[184:187], v[148:151], a[16:19], v[184:187]\n"
  "ds_read_b128 v[140:143], v21 offset:52224\n"
  "v_mul_f32 v223, v223, v71\n"
  "v_mfma_f32_16x16x32_bf16 v[188:191], v[148:151], a[20:23], v[188:191]\n"
  "s_mul_i32 s11, s31, 10\n"
  "s_add_u32 s11, s11, s26\n"
  "s_add_u32 m0, s9, 0x19000\n"
  "s_nop 0\n"
  "buffer_load_dwordx4 v21, s[12:15], s11 offen sc1 lds\n"
  "s_waitcnt lgkmcnt(3)\n"
  "v_mfma_f32_16x16x32_bf16 v[80:83], v[152:155], a[24:27], v[80:83]\n"
  "ds_read_b128 v[144:147], v21 offset:53248\n"
  "v_exp_f32 v220, v220\n"
  "v_mfma_f32_16x16x32_bf16 v[84:87], v[152:155], a[28:31], v[84:87]\n"
  "v_mul_f32 v172, v172, v68\n"
  "v_mfma_f32_16x16x32_bf16 v[184:187], v[156:159], a[24:27], v[184:187]\n"
  "ds_read_b128 v[148:151], v21 offset:54272\n"
  "v_exp_f32 v221, v221\n"
  "v_mfma_f32_16x16x32_bf16 v[188:191], v[156:159], a[28:31], v[188:191]\n"
  "buffer_load_dwordx4 v21, s[12:15], s11 offen offset:1024 sc1 lds\n"
  "v_mul_f32 v173, v173, v69\n"
  "v_mfma_f32_16x16x32_bf16 v[80:83], v[128:131], a[32:35], v[80:83]\n"
  "ds_read_b128 v[152:155], v21 offset:55296\n"
  "v_exp_f32 v222, v222\n"
  "v_mfma_f32_16x16x32_bf16 v[84:87], v[128:131], a[36:39], v[84:87]\n"
  "v_mul_f32 v174, v174, v70\n"
  "s_waitcnt lgkmcnt(3)\n"
  "v_mfma_f32_16x16x32_bf16 v[184:187], v[132:135], a[32:35], v[184:187]\n"
  "ds_read_b128 v[156:159], v21 offset:56320\n"
  "v_exp_f32 v223, v223\n"
  "v_mfma_f32_16x16x32_bf16 v[188:191], v[132:135], a[36:39], v[188:191]\n"
  "buffer_load_dwordx4 v21, s[12:15], s11 offen offset:2048 sc1 lds\n"
  "v_mul_f32 v175, v175, v71\n"
  "v_fma_f32 v220, v220, v212, v212\n"
  "v_mfma_f32_16x16x32_bf16 v[80:83], v[136:139], a[40:43], v[80:83]\n"
  "ds_read_b128 v[128:131], v21 offset:57344\n"
  "v_fma_f32 v221, v221, v212, v212\n"
  "v_fma_f32 v222, v222, v212, v212\n"
  "v_mfma_f32_16x16x32_bf16 v[84:87], v[136:139], a[44:47], v[84:87]\n"
  "v_fma_f32 v223, v223, v212, v212\n"
  "v_mfma_f32_16x16x32_bf16 v[184:187], v[140:143], a[40:43], v[184:187]\n"
  "ds_read_b128 v[132:135], v21 offset:58368\n"
  "v_rcp_f32 v220, v220\n"
  "v_mfma_f32_16x16x32_bf16 v[188:191], v[140:143], a[44:47], v[188:191]\n"
  "buffer_load_dwordx4 v21, s[12:15], s11 offen offset:3072 sc1 lds\n"
  "v_rcp_f32 v221, v221\n"
  "s_waitcnt lgkmcnt(3)\n"
  "v_mfma_f32_16x16x32_bf16 v[80:83], v[144:147], a[48:51], v[80:83]\n"
  "ds_read_b128 v[136:139], v21 offset:59392\n"
  "v_rcp_f32 v222, v222\n"
  "v_mfma_f32_16x16x32_bf16 v[84:87], v[144:147], a[52:55], v[84:87]\n"
  "v_rcp_f32 v223, v223\n"
  "v_mfma_f32_16x16x32_bf16 v[184:187], v[148:151], a[48:51], v[184:187]\n"
  "ds_read_b128 v[140:143], v21 offset:60416\n"
  "v_mul_f32 v172, v172, v220\n"
  "v_mul_f32 v173, v173, v221\n"
  "v_mfma_f32_16x16x32_bf16 v[188:191], v[148:151], a[52:55], v[188:191]\n"
  "s_add_u32 m0, m0, 0x1000\n"
  "s_add_u32 s11, s11, 0x1000\n"
  "buffer_load_dwordx4 v21, s[12:15], s11 offen sc1 lds\n"
  "v_mfma_f32_16x16x32_bf16 v[80:83], v[152:155], a[56:59], v[80:83]\n"
  "s_barrier\n"
  "ds_read_b128 v[144:147], v26 offset:12288\n"
  "v_mfma_f32_16x16x32_bf16 v[84:87], v[152:155], a[60:63], v[84:87]\n"
  "v_mul_f32 v174, v174, v222\n"
  "v_mul_f32 v175, v175, v223\n"
  "s_waitcnt lgkmcnt(3)\n"
  "v_mfma_f32_16x16x32_bf16 v[184:187], v[156:159], a[56:59], v[184:187]\n"
  "ds_read_b128 v[148:151], v26 offset:13312\n"
  "v_cvt_pk_bf16_f32 v230, v172, v173\n"
  "v_cvt_pk_bf16_f32 v231, v174, v175\n"
  "v_mfma_f32_16x16x32_bf16 v[188:191], v[156:159], a[60:63], v[188:191]\n"
  "s_mul_i32 s11, s36, 10\n"
  "s_add_u32 s11, s11, s26\n"
  "s_add_u32 m0, s9, 0x1e000\n"
  "s_nop 0\n"
  "buffer_load_dwordx4 v21, s[12:15], s11 offen sc1 lds\n"
  "v_mfma_f32_16x16x32_bf16 v[80:83], v[128:131], a[64:67], v[80:83]\n"
  "ds_read_b128 v[152:155], v26 offset:14336\n"
  "buffer_store_dwordx4 v[228:231], v21, s[12:15], s55 offen sc1\n"
  "v_mul_f32 v216, v72, v72\n"
  "v_mfma_f32_16x16x32_bf16 v[84:87], v[128:131], a[68:71], v[84:87]\n"
  "v_mul_f32 v217, v73, v73\n"
  "v_mul_f32 v218, v74, v74\n"
  "v_mfma_f32_16x16x32_bf16 v[184:187], v[132:135], a[64:67], v[184:187]\n"
  "ds_read_b128 v[156:159], v26 offset:15360\n"
  "v_mul_f32 v219, v75, v75\n"
  "v_fma_f32 v216, v216, v106, v38\n"
  "v_mfma_f32_16x16x32_bf16 v[188:191], v[132:135], a[68:71], v[188:191]\n"
  "buffer_load_dwordx4 v21, s[12:15], s11 offen offset:1024 sc1 lds\n"
  "v_fma_f32 v217, v217, v106, v38\n"
  "s_waitcnt lgkmcnt(3)\n"
  "v_mfma_f32_16x16x32_bf16 v[80:83], v[136:139], a[72:75], v[80:83]\n"
  "ds_read_b128 v[128:131], v26 offset:16384\n"
  "v_fma_f32 v218, v218, v106, v38\n"
  "v_fma_f32 v219, v219, v106, v38\n"
  "v_mfma_f32_16x16x32_bf16 v[84:87], v[136:139], a[76:79], v[84:87]\n"
  "v_mul_f32 v216, v216, v72\n"
  "v_mul_f32 v217, v217, v73\n"
  "v_mfma_f32_16x16x32_bf16 v[184:187], v[140:143], a[72:75], v[184:187]\n"
  "ds_read_b128 v[132:135], v26 offset:17408\n"
  "v_mul_f32 v218, v218, v74\n"
  "v_mul_f32 v219, v219, v75\n"
  "v_mfma_f32_16x16x32_bf16 v[188:191], v[140:143], a[76:79], v[188:191]\n"
  "buffer_load_dwordx4 v21, s[12:15], s11 offen offset:2048 sc1 lds\n"
  "v_exp_f32 v216, v216\n"
  "v_mfma_f32_16x16x32_bf16 v[80:83], v[144:147], a[80:83], v[80:83]\n"
  "ds_read_b128 v[136:139], v26 offset:18432\n"
  "v_mul_f32 v176, v176, v72\n"
  "v_mfma_f32_16x16x32_bf16 v[84:87], v[144:147], a[84:87], v[84:87]\n"
  "v_exp_f32 v217, v217\n"
  "s_waitcnt lgkmcnt(3)\n"
  "v_mfma_f32_16x16x32_bf16 v[184:187], v[148:151], a[80:83], v[184:187]\n"
  "ds_read_b128 v[140:143], v26 offset:19456\n"
  "v_mul_f32 v177, v177, v73\n"
  "v_mfma_f32_16x16x32_bf16 v[188:191], v[148:151], a[84:87], v[188:191]\n"
  "buffer_load_dwordx4 v21, s[12:15], s11 offen offset:3072 sc1 lds\n"
  "v_exp_f32 v218, v218\n"
  "v_mfma_f32_16x16x32_bf16 v[80:83], v[152:155], a[88:91], v[80:83]\n"
  "ds_read_b128 v[144:147], v26 offset:20480\n"
  "v_mul_f32 v178, v178, v74\n"
  "v_mfma_f32_16x16x32_bf16 v[84:87], v[152:155], a[92:95], v[84:87]\n"
  "v_exp_f32 v219, v219\n"
  "v_mfma_f32_16x16x32_bf16 v[184:187], v[156:159], a[88:91], v[184:187]\n"
  "ds_read_b128 v[148:151], v26 offset:21504\n"
  "v_mul_f32 v179, v179, v75\n"
  "v_fma_f32 v216, v216, v210, v210\n"
  "v_mfma_f32_16x16x32_bf16 v[188:191], v[156:159], a[92:95], v[188:191]\n"
  "s_add_u32 m0, m0, 0x1000\n"
  "s_add_u32 s11, s11, 0x1000\n"
  "buffer_load_dwordx4 v21, s[12:15], s11 offen sc1 lds\n"
  "s_waitcnt lgkmcnt(3)\n"
  "v_mfma_f32_16x16x32_bf16 v[80:83], v[128:131], a[96:99], v[80:83]\n"
  "ds_read_b128 v[152:155], v26 offset:22528\n"
  "v_fma_f32 v217, v217, v210, v210\n"
  "v_fma_f32 v218, v218, v210, v210\n"
  "v_mfma_f32_16x16x32_bf16 v[84:87], v[128:131], a[100:103], v[84:87]\n"
  "v_fma_f32 v219, v219, v210, v210\n"
  "v_mfma_f32_16x16x32_bf16 v[184:187], v[132:135], a[96:99], v[184:187]\n"
  "ds_read_b128 v[156:159], v26 offset:23552\n"
  "v_rcp_f32 v216, v216\n"
  "v_mfma_f32_16x16x32_bf16 v[188:191], v[132:135], a[100:103], v[188:191]\n"
  "v_rcp_f32 v217, v217\n"
  "v_mfma_f32_16x16x32_bf16 v[80:83], v[136:139], a[104:107], v[80:83]\n"
  "ds_read_b128 v[128:131], v26 offset:24576\n"
  "v_rcp_f32 v218, v218\n"
  "v_mfma_f32_16x16x32_bf16 v[84:87], v[136:139], a[108:111], v[84:87]\n"
  "v_rcp_f32 v219, v219\n"
  "s_waitcnt lgkmcnt(3)\n"
  "v_mfma_f32_16x16x32_bf16 v[184:187], v[140:143], a[104:107], v[184:187]\n"
  "ds_read_b128 v[132:135], v26 offset:25600\n"
  "v_mul_f32 v176, v176, v216\n"
  "v_mul_f32 v177, v177, v217\n"
  "v_mfma_f32_16x16x32_bf16 v[188:191], v[140:143], a[108:111], v[188:191]\n"
  "v_mul_f32 v178, v178, v218\n"
  "v_mul_f32 v179, v179, v219\n"
  "v_mfma_f32_16x16x32_bf16 v[80:83], v[144:147], a[112:115], v[80:83]\n"
  "ds_read_b128 v[136:139], v26 offset:26624\n"
  "v_cvt_pk_bf16_f32 v224, v176, v177\n"
  "v_cvt_pk_bf16_f32 v225, v178, v179\n"
  "v_mfma_f32_16x16x32_bf16 v[84:87], v[144:147], a[116:119], v[84:87]\n"
  "v_mul_f32 v220, v76, v76\n"
  "v_mul_f32 v221, v77, v77\n"
  "v_mfma_f32_16x16x32_bf16 v[184:187], v[148:151], a[112:115], v[184:187]\n"
  "ds_read_b128 v[140:143], v26 offset:27648\n"
  "v_mul_f32 v222, v78, v78\n"
  "v_mul_f32 v223, v79, v79\n"
  "v_mfma_f32_16x16x32_bf16 v[188:191], v[148:151], a[116:119], v[188:191]\n"
  "v_fma_f32 v220, v220, v208, v44\n"
  "v_fma_f32 v221, v221, v208, v44\n"
  "s_waitcnt lgkmcnt(3)\n"
  "v_mfma_f32_16x16x32_bf16 v[80:83], v[152:155], a[120:123], v[80:83]\n"
  "ds_read_b128 v[144:147], v26 offset:28672\n"
  "v_fma_f32 v222, v222, v208, v44\n"
  "v_fma_f32 v223, v223, v208, v44\n"
  "v_mfma_f32_16x16x32_bf16 v[84:87], v[152:155], a[124:127], v[84:87]\n"
  "s_add_u32 s64, s65, 0x9000\n"
  "s_waitcnt vmcnt(4)\n"
  "s_mov_b64 exec, 1\n"
  "buffer_store_dwordx2 v[10:11], off, s[12:15], s64 sc1\n"
  "s_mov_b64 exec, -1\n"
  "v_mfma_f32_16x16x32_bf16 v[184:187], v[156:159], a[120:123], v[184:187]\n"
  "ds_read_b128 v[148:151], v26 offset:29696\n"
  "v_mul_f32 v220, v220, v76\n"
  "v_mul_f32 v221, v221, v77\n"
  "v_mfma_f32_16x16x32_bf16 v[188:191], v[156:159], a[124:127], v[188:191]\n"
  "v_mul_f32 v222, v222, v78\n"
  "v_mul_f32 v223, v223, v79\n"
  "v_mfma_f32_16x16x32_bf16 v[80:83], v[128:131], a[128:131], v[80:83]\n"
  "ds_read_b128 v[152:155], v26 offset:30720\n"
  "v_exp_f32 v220, v220\n"
  "v_mfma_f32_16x16x32_bf16 v[84:87], v[128:131], a[132:135], v[84:87]\n"
  "v_mul_f32 v180, v180, v76\n"
  "s_waitcnt lgkmcnt(3)\n"
  "v_mfma_f32_16x16x32_bf16 v[184:187], v[132:135], a[128:131], v[184:187]\n"
  "ds_read_b128 v[156:159], v26 offset:31744\n"
  "v_exp_f32 v221, v221\n"
  "v_mfma_f32_16x16x32_bf16 v[188:191], v[132:135], a[132:135], v[188:191]\n"
  "v_mul_f32 v181, v181, v77\n"
  "v_mfma_f32_16x16x32_bf16 v[80:83], v[136:139], a[136:139], v[80:83]\n"
  "s_barrier\n"
  "ds_read_b128 v[128:131], v21\n"
  "v_mfma_f32_16x16x32_bf16 v[84:87], v[136:139], a[140:143], v[84:87]\n"
  "v_exp_f32 v222, v222\n"
  "v_mfma_f32_16x16x32_bf16 v[184:187], v[140:143], a[136:139], v[184:187]\n"
  "ds_read_b128 v[132:135], v21 offset:1024\n"
  "v_mul_f32 v182, v182, v78\n"
  "v_mfma_f32_16x16x32_bf16 v[188:191], v[140:143], a[140:143], v[188:191]\n"
  "s_mul_i32 s11, s37, 10\n"
  "s_add_u32 s11, s11, s26\n"
  "s_add_u32 m0, s9, 0xa000\n"
  "s_nop 0\n"
  "buffer_load_dwordx4 v21, s[12:15], s11 offen sc1 lds\n"
  "s_waitcnt lgkmcnt(3)\n"
  "v_mfma_f32_16x16x32_bf16 v[80:83], v[144:147], a[144:147], v[80:83]\n"
  "ds_read_b128 v[136:139], v21 offset:2048\n"
  "v_exp_f32 v223, v223\n"
  "v_mfma_f32_16x16x32_bf16 v[84:87], v[144:147], a[148:151], v[84:87]\n"
  "v_mul_f32 v183, v183, v79\n"
  "v_fma_f32 v220, v220, v212, v212\n"
  "v_mfma_f32_16x16x32_bf16 v[184:187], v[148:151], a[144:147], v[184:187]\n"
  "ds_read_b128 v[140:143], v21 offset:3072\n"
  "v_fma_f32 v221, v221, v212, v212\n"
  "v_fma_f32 v222, v222, v212, v212\n"
  "v_mfma_f32_16x16x32_bf16 v[188:191], v[148:151], a[148:151], v[188:191]\n"
  "buffer_load_dwordx4 v21, s[12:15], s11 offen offset:1024 sc1 lds\n"
  "v_fma_f32 v223, v223, v212, v212\n"
  "v_mfma_f32_16x16x32_bf16 v[80:83], v[152:155], a[152:155], v[80:83]\n"
  "ds_read_b128 v[144:147], v21 offset:4096\n"
  "v_rcp_f32 v220, v220\n"
  "v_mfma_f32_16x16x32_bf16 v[84:87], v[152:155], a[156:159], v[84:87]\n"
  "v_rcp_f32 v221, v221\n"
  "s_waitcnt lgkmcnt(3)\n"
  "v_mfma_f32_16x16x32_bf16 v[184:187], v[156:159], a[152:155], v[184:187]\n"
  "ds_read_b128 v[148:151], v21 offset:5120\n"
  "v_rcp_f32 v222, v222\n"
  "v_mfma_f32_16x16x32_bf16 v[188:191], v[156:159], a[156:159], v[188:191]\n"
  "buffer_load_dwordx4 v21, s[12:15], s11 offen offset:2048 sc1 lds\n"
  "v_rcp_f32 v223, v223\n"
  "v_mfma_f32_16x16x32_bf16 v[80:83], a[236:239], v[200:203], v[80:83]\n"
  "v_mfma_f32_16x16x32_bf16 v[84:87], a[236:239], v[204:207], v[84:87]\n"
  "v_mfma_f32_16x16x32_bf16 v[184:187], a[238:241], v[200:203], v[184:187]\n"
  "v_mfma_f32_16x16x32_bf16 v[188:191], a[238:241], v[204:207], v[188:191]\n"
  "v_mfma_f32_16x16x32_bf16 v[40:43], v[128:131], a[0:3], 0\n"
  "ds_read_b128 v[152:155], v21 offset:6144\n"
  "v_mul_f32 v180, v180, v220\n"
  "v_mul_f32 v181, v181, v221\n"
  "v_mfma_f32_16x16x32_bf16 v[56:59], v[128:131], a[4:7], 0\n"
  "v_mul_f32 v182, v182, v222\n"
  "v_mul_f32 v183, v183, v223\n"
  "v_mfma_f32_16x16x32_bf16 v[88:91], v[132:135], a[0:3], 0\n"
  "ds_read_b128 v[156:159], v21 offset:7168\n"
  "v_cvt_pk_bf16_f32 v228, v180, v181\n"
  "v_cvt_pk_bf16_f32 v229, v182, v183\n"
  "v_mfma_f32_16x16x32_bf16 v[92:95], v[132:135], a[4:7], 0\n"
  "buffer_load_dwordx4 v21, s[12:15], s11 offen offset:3072 sc1 lds\n"
  "v_mul_f32 v216, v80, v80\n"
  "s_waitcnt lgkmcnt(3)\n"
  "v_mfma_f32_16x16x32_bf16 v[40:43], v[136:139], a[8:11], v[40:43]\n"
  "ds_read_b128 v[128:131], v21 offset:8192\n"
  "v_mul_f32 v217, v81, v81\n"
  "v_mul_f32 v218, v82, v82\n"
  "v_mfma_f32_16x16x32_bf16 v[56:59], v[136:139], a[12:15], v[56:59]\n"
  "v_mul_f32 v219, v83, v83\n"
  "v_fma_f32 v216, v216, v106, v38\n"
  "v_mfma_f32_16x16x32_bf16 v[88:91], v[140:143], a[8:11], v[88:91]\n"
  "ds_read_b128 v[132:135], v21 offset:9216\n"
  "v_fma_f32 v217, v217, v106, v38\n"
  "v_fma_f32 v218, v218, v106, v38\n"
  "v_mfma_f32_16x16x32_bf16 v[92:95], v[140:143], a[12:15], v[92:95]\n"
  "s_add_u32 m0, m0, 0x1000\n"
  "s_add_u32 s11, s11, 0x1000\n"
  "buffer_load_dwordx4 v21, s[12:15], s11 offen sc1 lds\n"
  "v_mfma_f32_16x16x32_bf16 v[40:43], v[144:147], a[16:19], v[40:43]\n"
  "ds_read_b128 v[136:139], v21 offset:10240\n"
  "v_fma_f32 v219, v219, v106, v38\n"
  "v_mul_f32 v216, v216, v80\n"
  "v_mfma_f32_16x16x32_bf16 v[56:59], v[144:147], a[20:23], v[56:59]\n"
  "v_mul_f32 v217, v217, v81\n"
  "v_mul_f32 v218, v218, v82\n"
  "s_waitcnt lgkmcnt(3)\n"
  "v_mfma_f32_16x16x32_bf16 v[88:91], v[148:151], a[16:19], v[88:91]\n"
  "ds_read_b128 v[140:143], v21 offset:11264\n"
  "v_mul_f32 v219, v219, v83\n"
  "v_mfma_f32_16x16x32_bf16 v[92:95], v[148:151], a[20:23], v[92:95]\n"
  "v_exp_f32 v216, v216\n"
  "v_mfma_f32_16x16x32_bf16 v[40:43], v[152:155], a[24:27], v[40:43]\n"
  "ds_read_b128 v[144:147], v21 offset:12288\n"
  "v_mul_f32 v184, v184, v80\n"
  "v_mfma_f32_16x16x32_bf16 v[56:59], v[152:155], a[28:31], v[56:59]\n"
  "v_exp_f32 v217, v217\n"
  "v_mfma_f32_16x16x32_bf16 v[88:91], v[156:159], a[24:27], v[88:91]\n"
  "ds_read_b128 v[148:151], v21 offset:13312\n"
  "v_mul_f32 v185, v185, v81\n"
  "v_mfma_f32_16x16x32_bf16 v[92:95], v[156:159], a[28:31], v[92:95]\n"
  "v_exp_f32 v218, v218\n"
  "s_waitcnt lgkmcnt(3)\n"
  "v_mfma_f32_16x16x32_bf16 v[40:43], v[128:131], a[32:35], v[40:43]\n"
  "ds_read_b128 v[152:155], v21 offset:14336\n"
  "v_mul_f32 v186, v186, v82\n"
  "v_mfma_f32_16x16x32_bf16 v[56:59], v[128:131], a[36:39], v[56:59]\n"
  "s_add_u32 s48, s4, 0x9000\n"
  "buffer_load_dwordx2 v[64:65], v30, s[12:15], s48 offen sc1\n"
  "v_exp_f32 v219, v219\n"
  "v_mfma_f32_16x16x32_bf16 v[88:91], v[132:135], a[32:35], v[88:91]\n"
  "ds_read_b128 v[156:159], v21 offset:15360\n"
  "v_mul_f32 v187, v187, v83\n"
  "v_fma_f32 v216, v216, v210, v210\n"
  "v_mfma_f32_16x16x32_bf16 v[92:95], v[132:135], a[36:39], v[92:95]\n"
  "v_fma_f32 v217, v217, v210, v210\n"
  "v_fma_f32 v218, v218, v210, v210\n"
  "v_mfma_f32_16x16x32_bf16 v[40:43], v[136:139], a[40:43], v[40:43]\n"
  "ds_read_b128 v[128:131], v21 offset:16384\n"
  "v_fma_f32 v219, v219, v210, v210\n"
  "v_mfma_f32_16x16x32_bf16 v[56:59], v[136:139], a[44:47], v[56:59]\n"
  "v_rcp_f32 v216, v216\n"
  "s_waitcnt lgkmcnt(3)\n"
  "v_mfma_f32_16x16x32_bf16 v[88:91], v[140:143], a[40:43], v[88:91]\n"
  "ds_read_b128 v[132:135], v21 offset:17408\n"
  "v_rcp_f32 v217, v217\n"
  "v_mfma_f32_16x16x32_bf16 v[92:95], v[140:143], a[44:47], v[92:95]\n"
  "v_rcp_f32 v218, v218\n"
  "v_mfma_f32_16x16x32_bf16 v[40:43], v[144:147], a[48:51], v[40:43]\n"
  "ds_read_b128 v[136:139], v21 offset:18432\n"
  "v_rcp_f32 v219, v219\n"
  "v_mfma_f32_16x16x32_bf16 v[56:59], v[144:147], a[52:55], v[56:59]\n"
  "v_mul_f32 v184, v184, v216\n"
  "v_mul_f32 v185, v185, v217\n"
  "v_mfma_f32_16x16x32_bf16 v[88:91], v[148:151], a[48:51], v[88:91]\n"
  "ds_read_b128 v[140:143], v21 offset:19456\n"
  "v_mul_f32 v186, v186, v218\n"
  "v_mul_f32 v187, v187, v219\n"
  "v_mfma_f32_16x16x32_bf16 v[92:95], v[148:151], a[52:55], v[92:95]\n"
  "v_cvt_pk_bf16_f32 v226, v184, v185\n"
  "v_cvt_pk_bf16_f32 v227, v186, v187\n"
  "s_waitcnt lgkmcnt(3)\n"
  "v_mfma_f32_16x16x32_bf16 v[40:43], v[152:155], a[56:59], v[40:43]\n"
  "s_barrier\n"
  "ds_read_b128 v[144:147], v21 offset:20480\n"
  "v_mfma_f32_16x16x32_bf16 v[56:59], v[152:155], a[60:63], v[56:59]\n"
  "buffer_store_dwordx4 v[224:227], v21, s[12:15], s49 offen offset:1024 sc1\n"
  "v_mul_f32 v220, v84, v84\n"
  "v_mul_f32 v221, v85, v85\n"
  "v_mfma_f32_16x16x32_bf16 v[88:91], v[156:159], a[56:59], v[88:91]\n"
  "ds_read_b128 v[148:151], v21 offset:21504\n"
  "v_mul_f32 v222, v86, v86\n"
  "v_mul_f32 v223, v87, v87\n"
  "v_mfma_f32_16x16x32_bf16 v[92:95], v[156:159], a[60:63], v[92:95]\n"
  "s_mul_i32 s11, s38, 10\n"
  "s_add_u32 s11, s11, s26\n"
  "s_add_u32 m0, s9, 0x23000\n"
  "s_nop 0\n"
  "buffer_load_dwordx4 v21, s[12:15], s11 offen sc1 lds\n"
  "v_mfma_f32_16x16x32_bf16 v[40:43], v[128:131], a[64:67], v[40:43]\n"
  "ds_read_b128 v[152:155], v21 offset:22528\n"
  "v_fma_f32 v220, v220, v208, v44\n"
  "v_fma_f32 v221, v221, v208, v44\n"
  "v_mfma_f32_16x16x32_bf16 v[56:59], v[128:131], a[68:71], v[56:59]\n"
  "v_fma_f32 v222, v222, v208, v44\n"
  "v_fma_f32 v223, v223, v208, v44\n"
  "s_waitcnt lgkmcnt(3)\n"
  "v_mfma_f32_16x16x32_bf16 v[88:91], v[132:135], a[64:67], v[88:91]\n"
  "ds_read_b128 v[156:159], v21 offset:23552\n"
  "v_mul_f32 v220, v220, v84\n"
  "v_mul_f32 v221, v221, v85\n"
  "v_mfma_f32_16x16x32_bf16 v[92:95], v[132:135], a[68:71], v[92:95]\n"
  "buffer_load_dwordx4 v21, s[12:15], s11 offen offset:1024 sc1 lds\n"
  "v_mul_f32 v222, v222, v86\n"
  "v_mul_f32 v223, v223, v87\n"
  "v_mfma_f32_16x16x32_bf16 v[40:43], v[136:139], a[72:75], v[40:43]\n"
  "ds_read_b128 v[128:131], v21 offset:24576\n"
  "v_exp_f32 v220, v220\n"
  "v_mfma_f32_16x16x32_bf16 v[56:59], v[136:139], a[76:79], v[56:59]\n"
  "v_mul_f32 v188, v188, v84\n"
  "v_mfma_f32_16x16x32_bf16 v[88:91], v[140:143], a[72:75], v[88:91]\n"
  "ds_read_b128 v[132:135], v21 offset:25600\n"
  "v_exp_f32 v221, v221\n"
  "v_mfma_f32_16x16x32_bf16 v[92:95], v[140:143], a[76:79], v[92:95]\n"
  "buffer_load_dwordx4 v21, s[12:15], s11 offen offset:2048 sc1 lds\n"
  "v_mul_f32 v189, v189, v85\n"
  "s_waitcnt lgkmcnt(3)\n"
  "v_mfma_f32_16x16x32_bf16 v[40:43], v[144:147], a[80:83], v[40:43]\n"
  "ds_read_b128 v[136:139], v21 offset:26624\n"
  "v_exp_f32 v222, v222\n"
  "v_mfma_f32_16x16x32_bf16 v[56:59], v[144:147], a[84:87], v[56:59]\n"
  "v_mul_f32 v190, v190, v86\n"
  "v_mfma_f32_16x16x32_bf16 v[88:91], v[148:151], a[80:83], v[88:91]\n"
  "ds_read_b128 v[140:143], v21 offset:27648\n"
  "v_exp_f32 v223, v223\n"
  "v_mfma_f32_16x16x32_bf16 v[92:95], v[148:151], a[84:87], v[92:95]\n"
  "buffer_load_dwordx4 v21, s[12:15], s11 offen offset:3072 sc1 lds\n"
  "v_mul_f32 v191, v191, v87\n"
  "v_fma_f32 v220, v220, v212, v212\n"
  "v_mfma_f32_16x16x32_bf16 v[40:43], v[152:155], a[88:91], v[40:43]\n"
  "ds_read_b128 v[144:147], v21 offset:28672\n"
  "v_fma_f32 v221, v221, v212, v212\n"
  "v_fma_f32 v222, v222, v212, v212\n"
  "v_mfma_f32_16x16x32_bf16 v[56:59], v[152:155], a[92:95], v[56:59]\n"
  "v_fma_f32 v223, v223, v212, v212\n"
  "s_waitcnt lgkmcnt(3)\n"
  "v_mfma_f32_16x16x32_bf16 v[88:91], v[156:159], a[88:91], v[88:91]\n"
  "ds_read_b128 v[148:151], v21 offset:29696\n"
  "v_rcp_f32 v220, v220\n"
  "v_mfma_f32_16x16x32_bf16 v[92:95], v[156:159], a[92:95], v[92:95]\n"
  "s_add_u32 m0, m0, 0x1000\n"
  "s_add_u32 s11, s11, 0x1000\n"
  "buffer_load_dwordx4 v21, s[12:15], s11 offen sc1 lds\n"
  "v_mfma_f32_16x16x32_bf16 v[40:43], v[128:131], a[96:99], v[40:43]\n"
  "ds_read_b128 v[152:155], v21 offset:30720\n"
  "v_rcp_f32 v221, v221\n"
  "v_mfma_f32_16x16x32_bf16 v[56:59], v[128:131], a[100:103], v[56:59]\n"
  "s_waitcnt vmcnt(6)\n"
  "v_cmp_ne_u64 vcc, s[72:73], v[64:65]\n"
  "s_nop 3\n"
  "s_cbranch_vccz .Lhh_done_18x\n"
  "s_mov_b32 s63, 0x10000\n"
  ".Lhh_poll_17:\n"
  "buffer_load_dwordx2 v[64:65], v30, s[12:15], s48 offen sc1\n"
  "s_waitcnt vmcnt(0)\n"
  "v_cmp_ne_u64 vcc, s[72:73], v[64:65]\n"
  "s_nop 4\n"
  "s_cbranch_vccz .Lhh_done_18\n"
  "s_sleep 1\n"
  "s_sub_u32 s63, s63, 1\n"
  "s_cbranch_scc0 .Lhh_poll_17\n"
  ".Lhh_done_18:\n"
  ".Lhh_done_18x:\n"
  "v_mfma_f32_16x16x32_bf16 v[88:91], v[132:135], a[96:99], v[88:91]\n"
  "ds_read_b128 v[156:159], v21 offset:31744\n"
  "v_rcp_f32 v222, v222\n"
  "v_mfma_f32_16x16x32_bf16 v[92:95], v[132:135], a[100:103], v[92:95]\n"
  "s_mul_i32 s58, s31, 3\n"
  "s_add_u32 s58, s58, s5\n"
  "buffer_load_dwordx4 a[160:163], v21, s[12:15], s58 offen sc1\n"
  "s_waitcnt lgkmcnt(3)\n"
  "v_mfma_f32_16x16x32_bf16 v[40:43], v[136:139], a[104:107], v[40:43]\n"
  "ds_read_b128 v[128:131], v21 offset:32768\n"
  "v_rcp_f32 v223, v223\n"
  "v_mfma_f32_16x16x32_bf16 v[56:59], v[136:139], a[108:111], v[56:59]\n"
  "v_mul_f32 v188, v188, v220\n"
  "v_mul_f32 v189, v189, v221\n"
  "v_mfma_f32_16x16x32_bf16 v[88:91], v[140:143], a[104:107], v[88:91]\n"
  "ds_read_b128 v[132:135], v21 offset:33792\n"
  "v_mul_f32 v190, v190, v222\n"
  "v_mul_f32 v191, v191, v223\n"
  "v_mfma_f32_16x16x32_bf16 v[92:95], v[140:143], a[108:111], v[92:95]\n"
  "s_mul_i32 s59, s31, 3\n"
  "s_add_u32 s59, s59, s6\n"
  "buffer_load_dwordx4 a[164:167], v21, s[12:15], s59 offen sc1\n"
  "v_mfma_f32_16x16x32_bf16 v[40:43], v[144:147], a[112:115], v[40:43]\n"
  "ds_read_b128 v[136:139], v21 offset:34816\n"
  "v_cvt_pk_bf16_f32 v230, v188, v189\n"
  "v_cvt_pk_bf16_f32 v231, v190, v191\n"
  "v_mfma_f32_16x16x32_bf16 v[56:59], v[144:147], a[116:119], v[56:59]\n"
  "buffer_store_dwordx4 v[228:231], v21, s[12:15], s55 offen offset:1024 sc1\n"
  "s_waitcnt lgkmcnt(3)\n"
  "v_mfma_f32_16x16x32_bf16 v[88:91], v[148:151], a[112:115], v[88:91]\n"
  "ds_read_b128 v[140:143], v21 offset:35840\n"
  "v_mfma_f32_16x16x32_bf16 v[92:95], v[148:151], a[116:119], v[92:95]\n"
  "s_add_u32 s58, s58, 0xc00\n"
  "buffer_load_dwordx4 a[168:171], v21, s[12:15], s58 offen sc1\n"
  "v_mfma_f32_16x16x32_bf16 v[40:43], v[152:155], a[120:123], v[40:43]\n"
  "ds_read_b128 v[144:147], v21 offset:36864\n"
  "v_mfma_f32_16x16x32_bf16 v[56:59], v[152:155], a[124:127], v[56:59]\n"
  "v_mfma_f32_16x16x32_bf16 v[88:91], v[156:159], a[120:123], v[88:91]\n"
  "ds_read_b128 v[148:151], v21 offset:37888\n"
  "v_mfma_f32_16x16x32_bf16 v[92:95], v[156:159], a[124:127], v[92:95]\n"
  "s_add_u32 s59, s59, 0xc00\n"
  "buffer_load_dwordx4 a[172:175], v21, s[12:15], s59 offen sc1\n"
  "s_waitcnt lgkmcnt(3)\n"
  "v_mfma_f32_16x16x32_bf16 v[40:43], v[128:131], a[128:131], v[40:43]\n"
  "ds_read_b128 v[152:155], v21 offset:38912\n"
  "v_mfma_f32_16x16x32_bf16 v[56:59], v[128:131], a[132:135], v[56:59]\n"
  "v_mfma_f32_16x16x32_bf16 v[88:91], v[132:135], a[128:131], v[88:91]\n"
  "ds_read_b128 v[156:159], v21 offset:39936\n"
  "v_mfma_f32_16x16x32_bf16 v[92:95], v[132:135], a[132:135], v[92:95]\n"
  "s_mul_i32 s58, s36, 3\n"
  "s_add_u32 s58, s58, s5\n"
  "buffer_load_dwordx4 a[176:179], v21, s[12:15], s58 offen sc1\n"
  "v_mfma_f32_16x16x32_bf16 v[40:43], v[136:139], a[136:139], v[40:43]\n"
  "s_barrier\n"
  "ds_read_b128 v[128:131], v21 offset:61440\n"
  "v_mfma_f32_16x16x32_bf16 v[56:59], v[136:139], a[140:143], v[56:59]\n"
  "s_waitcnt lgkmcnt(3)\n"
  "v_mfma_f32_16x16x32_bf16 v[88:91], v[140:143], a[136:139], v[88:91]\n"
  "ds_read_b128 v[132:135], v21 offset:62464\n"
  "v_mfma_f32_16x16x32_bf16 v[92:95], v[140:143], a[140:143], v[92:95]\n"
  "s_mul_i32 s59, s36, 3\n"
  "s_add_u32 s59, s59, s6\n"
  "buffer_load_dwordx4 a[180:183], v21, s[12:15], s59 offen sc1\n"
  "v_mfma_f32_16x16x32_bf16 v[40:43], v[144:147], a[144:147], v[40:43]\n"
  "ds_read_b128 v[136:139], v21 offset:63488\n"
  "v_mfma_f32_16x16x32_bf16 v[56:59], v[144:147], a[148:151], v[56:59]\n"
  "v_mfma_f32_16x16x32_bf16 v[88:91], v[148:151], a[144:147], v[88:91]\n"
  "ds_read_b128 v[140:143], v21 offset:64512\n"
  "v_mfma_f32_16x16x32_bf16 v[92:95], v[148:151], a[148:151], v[92:95]\n"
  "s_add_u32 s58, s58, 0xc00\n"
  "buffer_load_dwordx4 a[184:187], v21, s[12:15], s58 offen sc1\n"
  "s_waitcnt lgkmcnt(3)\n"
  "v_mfma_f32_16x16x32_bf16 v[40:43], v[152:155], a[152:155], v[40:43]\n"
  "ds_read_b128 v[144:147], v25\n"
  "v_mfma_f32_16x16x32_bf16 v[56:59], v[152:155], a[156:159], v[56:59]\n"
  "v_mfma_f32_16x16x32_bf16 v[88:91], v[156:159], a[152:155], v[88:91]\n"
  "ds_read_b128 v[148:151], v25 offset:1024\n"
  "v_mfma_f32_16x16x32_bf16 v[92:95], v[156:159], a[156:159], v[92:95]\n"
  "s_add_u32 s59, s59, 0xc00\n"
  "buffer_load_dwordx4 a[188:191], v21, s[12:15], s59 offen sc1\n"
  "v_mfma_f32_16x16x32_bf16 v[40:43], a[240:243], v[200:203], v[40:43]\n"
  "v_mfma_f32_16x16x32_bf16 v[56:59], a[240:243], v[204:207], v[56:59]\n"
  "v_mfma_f32_16x16x32_bf16 v[88:91], a[242:245], v[200:203], v[88:91]\n"
  "v_mfma_f32_16x16x32_bf16 v[92:95], a[242:245], v[204:207], v[92:95]\n"
  "v_mfma_f32_16x16x32_bf16 v[60:63], v[128:131], a[0:3], 0\n"
  "ds_read_b128 v[152:155], v25 offset:2048\n"
  "v_mfma_f32_16x16x32_bf16 v[68:71], v[128:131], a[4:7], 0\n"
  "s_waitcnt lgkmcnt(3)\n"
  "v_mfma_f32_16x16x32_bf16 v[168:171], v[132:135], a[0:3], 0\n"
  "ds_read_b128 v[156:159], v25 offset:3072\n"
  "v_mul_f32 v216, v40, v40\n"
  "v_mul_f32 v217, v41, v41\n"
  "v_mfma_f32_16x16x32_bf16 v[172:175], v[132:135], a[4:7], 0\n"
  "s_mul_i32 s58, s37, 3\n"
  "s_add_u32 s58, s58, s5\n"
  "buffer_load_dwordx4 a[192:195], v21, s[12:15], s58 offen sc1\n"
  "v_mfma_f32_16x16x32_bf16 v[60:63], v[136:139], a[8:11], v[60:63]\n"
  "ds_read_b128 v[128:131], v25 offset:4096\n"
  "v_mul_f32 v218, v42, v42\n"
  "v_mul_f32 v219, v43, v43\n"
  "v_mfma_f32_16x16x32_bf16 v[68:71], v[136:139], a[12:15], v[68:71]\n"
  "v_fma_f32 v216, v216, v106, v38\n"
  "v_fma_f32 v217, v217, v106, v38\n"
  "v_mfma_f32_16x16x32_bf16 v[168:171], v[140:143], a[8:11], v[168:171]\n"
  "ds_read_b128 v[132:135], v25 offset:5120\n"
  "v_fma_f32 v218, v218, v106, v38\n"
  "v_fma_f32 v219, v219, v106, v38\n"
  "v_mfma_f32_16x16x32_bf16 v[172:175], v[140:143], a[12:15], v[172:175]\n"
  "s_mul_i32 s59, s37, 3\n"
  "s_add_u32 s59, s59, s6\n"
  "buffer_load_dwordx4 a[196:199], v21, s[12:15], s59 offen sc1\n"
  "s_waitcnt lgkmcnt(3)\n"
  "v_mfma_f32_16x16x32_bf16 v[60:63], v[144:147], a[16:19], v[60:63]\n"
  "ds_read_b128 v[136:139], v25 offset:6144\n"
  "v_mul_f32 v216, v216, v40\n"
  "v_mul_f32 v217, v217, v41\n"
  "v_mfma_f32_16x16x32_bf16 v[68:71], v[144:147], a[20:23], v[68:71]\n"
  "v_mul_f32 v218, v218, v42\n"
  "v_mul_f32 v219, v219, v43\n"
  "v_mfma_f32_16x16x32_bf16 v[168:171], v[148:151], a[16:19], v[168:171]\n"
  "ds_read_b128 v[140:143], v25 offset:7168\n"
  "v_exp_f32 v216, v216\n"
  "v_mfma_f32_16x16x32_bf16 v[172:175], v[148:151], a[20:23], v[172:175]\n"
  "s_add_u32 s58, s58, 0xc00\n"
  "buffer_load_dwordx4 a[200:203], v21, s[12:15], s58 offen sc1\n"
  "v_mul_f32 v88, v88, v40\n"
  "v_mfma_f32_16x16x32_bf16 v[60:63], v[152:155], a[24:27], v[60:63]\n"
  "ds_read_b128 v[144:147], v25 offset:8192\n"
  "v_exp_f32 v217, v217\n"
  "v_mfma_f32_16x16x32_bf16 v[68:71], v[152:155], a[28:31], v[68:71]\n"
  "v_mul_f32 v89, v89, v41\n"
  "s_waitcnt lgkmcnt(3)\n"
  "v_mfma_f32_16x16x32_bf16 v[168:171], v[156:159], a[24:27], v[168:171]\n"
  "ds_read_b128 v[148:151], v25 offset:9216\n"
  "v_exp_f32 v218, v218\n"
  "v_mfma_f32_16x16x32_bf16 v[172:175], v[156:159], a[28:31], v[172:175]\n"
  "s_add_u32 s59, s59, 0xc00\n"
  "buffer_load_dwordx4 a[204:207], v21, s[12:15], s59 offen sc1\n"
  "v_mul_f32 v90, v90, v42\n"
  "v_mfma_f32_16x16x32_bf16 v[60:63], v[128:131], a[32:35], v[60:63]\n"
  "ds_read_b128 v[152:155], v25 offset:10240\n"
  "v_exp_f32 v219, v219\n"
  "v_mfma_f32_16x16x32_bf16 v[68:71], v[128:131], a[36:39], v[68:71]\n"
  "v_mul_f32 v91, v91, v43\n"
  "v_fma_f32 v216, v216, v210, v210\n"
  "v_mfma_f32_16x16x32_bf16 v[168:171], v[132:135], a[32:35], v[168:171]\n"
  "ds_read_b128 v[156:159], v25 offset:11264\n"
  "v_fma_f32 v217, v217, v210, v210\n"
  "v_fma_f32 v218, v218, v210, v210\n"
  "v_mfma_f32_16x16x32_bf16 v[172:175], v[132:135], a[36:39], v[172:175]\n"
  "s_mul_i32 s58, s38, 3\n"
  "s_add_u32 s58, s58, s5\n"
  "buffer_load_dwordx4 a[208:211], v21, s[12:15], s58 offen sc1\n"
  "s_waitcnt lgkmcnt(3)\n"
  "v_mfma_f32_16x16x32_bf16 v[60:63], v[136:139], a[40:43], v[60:63]\n"
  "ds_read_b128 v[128:131], v25 offset:12288\n"
  "v_fma_f32 v219, v219, v210, v210\n"
  "v_mfma_f32_16x16x32_bf16 v[68:71], v[136:139], a[44:47], v[68:71]\n"
  "v_rcp_f32 v216, v216\n"
  "v_mfma_f32_16x16x32_bf16 v[168:171], v[140:143], a[40:43], v[168:171]\n"
  "ds_read_b128 v[132:135], v25 offset:13312\n"
  "v_rcp_f32 v217, v217\n"
  "v_mfma_f32_16x16x32_bf16 v[172:175], v[140:143], a[44:47], v[172:175]\n"
  "s_mul_i32 s59, s38, 3\n"
  "s_add_u32 s59, s59, s6\n"
  "buffer_load_dwordx4 a[212:215], v21, s[12:15], s59 offen sc1\n"
  "v_mfma_f32_16x16x32_bf16 v[60:63], v[144:147], a[48:51], v[60:63]\n"
  "ds_read_b128 v[136:139], v25 offset:14336\n"
  "v_rcp_f32 v218, v218\n"
  "v_mfma_f32_16x16x32_bf16 v[68:71], v[144:147], a[52:55], v[68:71]\n"
  "v_rcp_f32 v219, v219\n"
  "s_waitcnt lgkmcnt(3)\n"
  "v_mfma_f32_16x16x32_bf16 v[168:171], v[148:151], a[48:51], v[168:171]\n"
  "ds_read_b128 v[140:143], v25 offset:15360\n"
  "v_mul_f32 v88, v88, v216\n"
  "v_mul_f32 v89, v89, v217\n"
  "v_mfma_f32_16x16x32_bf16 v[172:175], v[148:151], a[52:55], v[172:175]\n"
  "s_mul_i32 s11, s31, 10\n"
  "s_add_u32 s11, s11, s26\n"
  "s_add_u32 m0, s9, 0\n"
  "s_add_u32 s11, s11, 0x14000\n"
  "buffer_load_dwordx4 v21, s[12:15], s11 offen sc1 lds\n"
  "v_mfma_f32_16x16x32_bf16 v[60:63], v[152:155], a[56:59], v[60:63]\n"
  "s_barrier\n"
  "ds_read_b128 v[144:147], v25 offset:16384\n"
  "v_mfma_f32_16x16x32_bf16 v[68:71], v[152:155], a[60:63], v[68:71]\n"
  "v_mul_f32 v90, v90, v218\n"
  "v_mul_f32 v91, v91, v219\n"
  "v_mfma_f32_16x16x32_bf16 v[168:171], v[156:159], a[56:59], v[168:171]\n"
  "ds_read_b128 v[148:151], v25 offset:17408\n"
  "v_cvt_pk_bf16_f32 v224, v88, v89\n"
  "v_cvt_pk_bf16_f32 v225, v90, v91\n"
  "v_mfma_f32_16x16x32_bf16 v[172:175], v[156:159], a[60:63], v[172:175]\n"
  "buffer_load_dwordx4 v21, s[12:15], s11 offen offset:1024 sc1 lds\n"
  "v_mul_f32 v220, v56, v56\n"
  "s_waitcnt lgkmcnt(3)\n"
  "v_mfma_f32_16x16x32_bf16 v[60:63], v[128:131], a[64:67], v[60:63]\n"
  "ds_read_b128 v[152:155], v25 offset:18432\n"
  "v_mul_f32 v221, v57, v57\n"
  "v_mul_f32 v222, v58, v58\n"
  "v_mfma_f32_16x16x32_bf16 v[68:71], v[128:131], a[68:71], v[68:71]\n"
  "v_mul_f32 v223, v59, v59\n"
  "v_fma_f32 v220, v220, v208, v44\n"
  "v_mfma_f32_16x16x32_bf16 v[168:171], v[132:135], a[64:67], v[168:171]\n"
  "ds_read_b128 v[156:159], v25 offset:19456\n"
  "v_fma_f32 v221, v221, v208, v44\n"
  "v_fma_f32 v222, v222, v208, v44\n"
  "v_mfma_f32_16x16x32_bf16 v[172:175], v[132:135], a[68:71], v[172:175]\n"
  "buffer_load_dwordx4 v21, s[12:15], s11 offen offset:2048 sc1 lds\n"
  "v_fma_f32 v223, v223, v208, v44\n"
  "v_mul_f32 v220, v220, v56\n"
  "v_mfma_f32_16x16x32_bf16 v[60:63], v[136:139], a[72:75], v[60:63]\n"
  "ds_read_b128 v[128:131], v25 offset:20480\n"
  "v_mul_f32 v221, v221, v57\n"
  "v_mul_f32 v222, v222, v58\n"
  "v_mfma_f32_16x16x32_bf16 v[68:71], v[136:139], a[76:79], v[68:71]\n"
  "v_mul_f32 v223, v223, v59\n"
  "s_waitcnt lgkmcnt(3)\n"
  "v_mfma_f32_16x16x32_bf16 v[168:171], v[140:143], a[72:75], v[168:171]\n"
  "ds_read_b128 v[132:135], v25 offset:21504\n"
  "v_exp_f32 v220, v220\n"
  "v_mfma_f32_16x16x32_bf16 v[172:175], v[140:143], a[76:79], v[172:175]\n"
  "buffer_load_dwordx4 v21, s[12:15], s11 offen offset:3072 sc1 lds\n"
  "v_mul_f32 v92, v92, v56\n"
  "v_mfma_f32_16x16x32_bf16 v[60:63], v[144:147], a[80:83], v[60:63]\n"
  "ds_read_b128 v[136:139], v25 offset:22528\n"
  "v_exp_f32 v221, v221\n"
  "v_mfma_f32_16x16x32_bf16 v[68:71], v[144:147], a[84:87], v[68:71]\n"
  "v_mul_f32 v93, v93, v57\n"
  "v_mfma_f32_16x16x32_bf16 v[168:171], v[148:151], a[80:83], v[168:171]\n"
  "ds_read_b128 v[140:143], v25 offset:23552\n"
  "v_exp_f32 v222, v222\n"
  "v_mfma_f32_16x16x32_bf16 v[172:175], v[148:151], a[84:87], v[172:175]\n"
  "s_add_u32 m0, m0, 0x1000\n"
  "s_add_u32 s11, s11, 0x1000\n"
  "buffer_load_dwordx4 v21, s[12:15], s11 offen sc1 lds\n"
  "s_waitcnt lgkmcnt(3)\n"
  "v_mfma_f32_16x16x32_bf16 v[60:63], v[152:155], a[88:91], v[60:63]\n"
  "ds_read_b128 v[144:147], v25 offset:24576\n"
  "v_mul_f32 v94, v94, v58\n"
  "v_mfma_f32_16x16x32_bf16 v[68:71], v[152:155], a[92:95], v[68:71]\n"
  "v_exp_f32 v223, v223\n"
  "v_mfma_f32_16x16x32_bf16 v[168:171], v[156:159], a[88:91], v[168:171]\n"
  "ds_read_b128 v[148:151], v25 offset:25600\n"
  "v_mul_f32 v95, v95, v59\n"
  "v_fma_f32 v220, v220, v212, v212\n"
  "v_mfma_f32_16x16x32_bf16 v[172:175], v[156:159], a[92:95], v[172:175]\n"
  "s_add_u32 s58, s58, 0xc00\n"
  "buffer_load_dwordx4 a[216:219], v21, s[12:15], s58 offen sc1\n"
  "v_fma_f32 v221, v221, v212, v212\n"
  "v_mfma_f32_16x16x32_bf16 v[60:63], v[128:131], a[96:99], v[60:63]\n"
  "ds_read_b128 v[152:155], v25 offset:26624\n"
  "v_fma_f32 v222, v222, v212, v212\n"
  "v_fma_f32 v223, v223, v212, v212\n"
  "v_mfma_f32_16x16x32_bf16 v[68:71], v[128:131], a[100:103], v[68:71]\n"
  "v_rcp_f32 v220, v220\n"
  "s_waitcnt lgkmcnt(3)\n"
  "v_mfma_f32_16x16x32_bf16 v[168:171], v[132:135], a[96:99], v[168:171]\n"
  "ds_read_b128 v[156:159], v25 offset:27648\n"
  "v_rcp_f32 v221, v221\n"
  "v_mfma_f32_16x16x32_bf16 v[172:175], v[132:135], a[100:103], v[172:175]\n"
  "s_add_u32 s59, s59, 0xc00\n"
  "buffer_load_dwordx4 a[220:223], v21, s[12:15], s59 offen sc1\n"
  "v_rcp_f32 v222, v222\n"
  "v_mfma_f32_16x16x32_bf16 v[60:63], v[136:139], a[104:107], v[60:63]\n"
  "ds_read_b128 v[128:131], v25 offset:28672\n"
  "v_rcp_f32 v223, v223\n"
  "v_mfma_f32_16x16x32_bf16 v[68:71], v[136:139], a[108:111], v[68:71]\n"
  "v_mul_f32 v92, v92, v220\n"
  "v_mul_f32 v93, v93, v221\n"
  "v_mfma_f32_16x16x32_bf16 v[168:171], v[140:143], a[104:107], v[168:171]\n"
  "ds_read_b128 v[132:135], v25 offset:29696\n"
  "v_mul_f32 v94, v94, v222\n"
  "v_mul_f32 v95, v95, v223\n"
  "v_mfma_f32_16x16x32_bf16 v[172:175], v[140:143], a[108:111], v[172:175]\n"
  "s_mul_i32 s11, s36, 10\n"
  "s_add_u32 s11, s11, s26\n"
  "s_add_u32 m0, s9, 0x5000\n"
  "s_add_u32 s11, s11, 0x14000\n"
  "buffer_load_dwordx4 v21, s[12:15], s11 offen sc1 lds\n"
  "s_waitcnt lgkmcnt(3)\n"
  "v_mfma_f32_16x16x32_bf16 v[60:63], v[144:147], a[112:115], v[60:63]\n"
  "ds_read_b128 v[136:139], v25 offset:30720\n"
  "v_cvt_pk_bf16_f32 v228, v92, v93\n"
  "v_cvt_pk_bf16_f32 v229, v94, v95\n"
  "v_mfma_f32_16x16x32_bf16 v[68:71], v[144:147], a[116:119], v[68:71]\n"
  "s_add_u32 s64, s65, 0xb000\n"
  "s_waitcnt vmcnt(20)\n"
  "s_mov_b64 exec, 1\n"
  "buffer_store_dwordx2 v[10:11], off, s[12:15], s64 sc1\n"
  "s_mov_b64 exec, -1\n"
  "v_mfma_f32_16x16x32_bf16 v[168:171], v[148:151], a[112:115], v[168:171]\n"
  "ds_read_b128 v[140:143], v25 offset:31744\n"
  "v_mfma_f32_16x16x32_bf16 v[172:175], v[148:151], a[116:119], v[172:175]\n"
  "buffer_load_dwordx4 v21, s[12:15], s11 offen offset:1024 sc1 lds\n"
  "v_mfma_f32_16x16x32_bf16 v[60:63], v[152:155], a[120:123], v[60:63]\n"
  "ds_read_b128 v[144:147], v25 offset:32768\n"
  "v_mfma_f32_16x16x32_bf16 v[68:71], v[152:155], a[124:127], v[68:71]\n"
  "s_waitcnt lgkmcnt(3)\n"
  "v_mfma_f32_16x16x32_bf16 v[168:171], v[156:159], a[120:123], v[168:171]\n"
  "ds_read_b128 v[148:151], v25 offset:33792\n"
  "v_mfma_f32_16x16x32_bf16 v[172:175], v[156:159], a[124:127], v[172:175]\n"
  "buffer_load_dwordx4 v21, s[12:15], s11 offen offset:2048 sc1 lds\n"
  "v_mfma_f32_16x16x32_bf16 v[60:63], v[128:131], a[128:131], v[60:63]\n"
  "ds_read_b128 v[152:155], v25 offset:34816\n"
  "v_mfma_f32_16x16x32_bf16 v[68:71], v[128:131], a[132:135], v[68:71]\n"
  "v_mfma_f32_16x16x32_bf16 v[168:171], v[132:135], a[128:131], v[168:171]\n"
  "ds_read_b128 v[156:159], v25 offset:35840\n"
  "v_mfma_f32_16x16x32_bf16 v[172:175], v[132:135], a[132:135], v[172:175]\n"
  "buffer_load_dwordx4 v21, s[12:15], s11 offen offset:3072 sc1 lds\n"
  "s_waitcnt lgkmcnt(3)\n"
  "v_mfma_f32_16x16x32_bf16 v[60:63], v[136:139], a[136:139], v[60:63]\n"
  "v_mfma_f32_16x16x32_bf16 v[68:71], v[136:139], a[140:143], v[68:71]\n"
  "v_mfma_f32_16x16x32_bf16 v[168:171], v[140:143], a[136:139], v[168:171]\n"
  "v_mfma_f32_16x16x32_bf16 v[172:175], v[140:143], a[140:143], v[172:175]\n"
  "s_add_u32 m0, m0, 0x1000\n"
  "s_add_u32 s11, s11, 0x1000\n"
  "buffer_load_dwordx4 v21, s[12:15], s11 offen sc1 lds\n"
  "v_mfma_f32_16x16x32_bf16 v[60:63], v[144:147], a[144:147], v[60:63]\n"
  "v_mfma_f32_16x16x32_bf16 v[68:71], v[144:147], a[148:151], v[68:71]\n"
  "s_waitcnt lgkmcnt(0)\n"
  "v_mfma_f32_16x16x32_bf16 v[168:171], v[148:151], a[144:147], v[168:171]\n"
  "v_mfma_f32_16x16x32_bf16 v[172:175], v[148:151], a[148:151], v[172:175]\n"
  "v_mfma_f32_16x16x32_bf16 v[60:63], v[152:155], a[152:155], v[60:63]\n"
  "v_mfma_f32_16x16x32_bf16 v[68:71], v[152:155], a[156:159], v[68:71]\n"
  "v_mfma_f32_16x16x32_bf16 v[168:171], v[156:159], a[152:155], v[168:171]\n"
  "v_mfma_f32_16x16x32_bf16 v[172:175], v[156:159], a[156:159], v[172:175]\n"
  "v_mfma_f32_16x16x32_bf16 v[60:63], a[244:247], v[200:203], v[60:63]\n"
  "v_mfma_f32_16x16x32_bf16 v[68:71], a[244:247], v[204:207], v[68:71]\n"
  "v_mfma_f32_16x16x32_bf16 v[168:171], a[246:249], v[200:203], v[168:171]\n"
  "v_mfma_f32_16x16x32_bf16 v[172:175], a[246:249], v[204:207], v[172:175]\n"
  "s_nop 4\n"
  "v_pk_mul_f32 v[216:217], v[60:61], v[60:61]\n"
  "v_pk_mul_f32 v[220:221], v[68:69], v[68:69]\n"
  "v_pk_mul_f32 v[218:219], v[62:63], v[62:63]\n"
  "v_pk_mul_f32 v[222:223], v[70:71], v[70:71]\n"
  "v_pk_fma_f32 v[216:217], v[216:217], v[106:107], v[38:39] op_sel_hi:[1,0,0]\n"
  "v_pk_fma_f32 v[220:221], v[220:221], v[208:209], v[44:45] op_sel_hi:[1,0,0]\n"
  "v_pk_fma_f32 v[218:219], v[218:219], v[106:107], v[38:39] op_sel_hi:[1,0,0]\n"
  "v_pk_fma_f32 v[222:223], v[222:223], v[208:209], v[44:45] op_sel_hi:[1,0,0]\n"
  "v_pk_mul_f32 v[216:217], v[216:217], v[60:61]\n"
  "v_pk_mul_f32 v[220:221], v[220:221], v[68:69]\n"
  "v_pk_mul_f32 v[218:219], v[218:219], v[62:63]\n"
  "v_pk_mul_f32 v[222:223], v[222:223], v[70:71]\n"
  "v_pk_mul_f32 v[168:169], v[168:169], v[60:61]\n"
  "v_pk_mul_f32 v[172:173], v[172:173], v[68:69]\n"
  "v_pk_mul_f32 v[170:171], v[170:171], v[62:63]\n"
  "v_pk_mul_f32 v[174:175], v[174:175], v[70:71]\n"
  "v_exp_f32 v216, v216\n"
  "v_exp_f32 v220, v220\n"
  "v_exp_f32 v217, v217\n"
  "v_exp_f32 v221, v221\n"
  "v_exp_f32 v218, v218\n"
  "v_exp_f32 v222, v222\n"
  "v_exp_f32 v219, v219\n"
  "v_exp_f32 v223, v223\n"
  "v_pk_fma_f32 v[216:217], v[216:217], v[210:211], v[210:211] op_sel_hi:[1,0,0]\n"
  "v_pk_fma_f32 v[220:221], v[220:221], v[212:213], v[212:213] op_sel_hi:[1,0,0]\n"
  "v_pk_fma_f32 v[218:219], v[218:219], v[210:211], v[210:211] op_sel_hi:[1,0,0]\n"
  "v_pk_fma_f32 v[222:223], v[222:223], v[212:213], v[212:213] op_sel_hi:[1,0,0]\n"
  "v_rcp_f32 v216, v216\n"
  "v_rcp_f32 v220, v220\n"
  "v_rcp_f32 v217, v217\n"
  "v_rcp_f32 v221, v221\n"
  "v_rcp_f32 v218, v218\n"
  "v_rcp_f32 v222, v222\n"
  "v_rcp_f32 v219, v219\n"
  "v_rcp_f32 v223, v223\n"
  "v_pk_mul_f32 v[168:169], v[168:169], v[216:217]\n"
  "v_pk_mul_f32 v[172:173], v[172:173], v[220:221]\n"
  "v_pk_mul_f32 v[170:171], v[170:171], v[218:219]\n"
  "v_pk_mul_f32 v[174:175], v[174:175], v[222:223]\n"
  "v_cvt_pk_bf16_f32 v226, v168, v169\n"
  "v_cvt_pk_bf16_f32 v230, v172, v173\n"
  "v_cvt_pk_bf16_f32 v227, v170, v171\n"
  "v_cvt_pk_bf16_f32 v231, v174, v175\n"
  "buffer_store_dwordx4 v[224:227], v21, s[12:15], s49 offen offset:2048 sc1\n"
  "buffer_store_dwordx4 v[228:231], v21, s[12:15], s55 offen offset:2048 sc1\n"
  "s_add_u32 s48, s4, 0xb000\n"
  "buffer_load_dwordx2 v[64:65], v30, s[12:15], s48 offen sc1\n"
  "s_barrier\n"
  "ds_read_b128 v[128:131], v28 offset:36864\n"
  "ds_read_b128 v[132:135], v28 offset:37888\n"
  "ds_read_b128 v[136:139], v28 offset:38912\n"
  "ds_read_b128 v[140:143], v28 offset:39936\n"
  "ds_read_b128 v[144:147], v28 offset:40960\n"
  "s_mul_i32 s11, s37, 10\n"
  "s_add_u32 s11, s11, s26\n"
  "s_add_u32 m0, s9, 0xf000\n"
  "s_add_u32 s11, s11, 0x14000\n"
  "buffer_load_dwordx4 v21, s[12:15], s11 offen sc1 lds\n"
  "buffer_load_dwordx4 v21, s[12:15], s11 offen offset:1024 sc1 lds\n"
  "buffer_load_dwordx4 v21, s[12:15], s11 offen offset:2048 sc1 lds\n"
  "buffer_load_dwordx4 v21, s[12:15], s11 offen offset:3072 sc1 lds\n"
  "s_waitcnt lgkmcnt(4)\n"
  "v_mfma_f32_16x16x32_bf16 v[40:43], v[128:131], a[160:163], 0\n"
  "ds_read_b128 v[148:151], v28 offset:41984\n"
  "v_mfma_f32_16x16x32_bf16 v[84:87], v[128:131], a[164:167], 0\n"
  "ds_read_b128 v[152:155], v28 offset:43008\n"
  "s_waitcnt lgkmcnt(5)\n"
  "v_mfma_f32_16x16x32_bf16 v[56:59], v[132:135], a[160:163], 0\n"
  "ds_read_b128 v[156:159], v28 offset:44032\n"
  "v_mfma_f32_16x16x32_bf16 v[88:91], v[132:135], a[164:167], 0\n"
  "ds_read_b128 v[160:163], v28 offset:45056\n"
  "s_waitcnt lgkmcnt(6)\n"
  "v_mfma_f32_16x16x32_bf16 v[72:75], v[136:139], a[160:163], 0\n"
  "ds_read_b128 v[164:167], v28 offset:46080\n"
  "v_mfma_f32_16x16x32_bf16 v[92:95], v[136:139], a[164:167], 0\n"
  "s_add_u32 m0, m0, 0x1000\n"
  "s_add_u32 s11, s11, 0x1000\n"
  "buffer_load_dwordx4 v21, s[12:15], s11 offen sc1 lds\n"
  "s_waitcnt lgkmcnt(6)\n"
  "v_mfma_f32_16x16x32_bf16 v[76:79], v[140:143], a[160:163], 0\n"
  "s_mul_i32 s11, s38, 10\n"
  "s_add_u32 s11, s11, s26\n"
  "s_add_u32 m0, s9, 0x14000\n"
  "s_add_u32 s11, s11, 0x14000\n"
  "buffer_load_dwordx4 v21, s[12:15], s11 offen sc1 lds\n"
  "v_mfma_f32_16x16x32_bf16 v[176:179], v[140:143], a[164:167], 0\n"
  "s_waitcnt lgkmcnt(5)\n"
  "v_mfma_f32_16x16x32_bf16 v[80:83], v[144:147], a[160:163], 0\n"
  "buffer_load_dwordx4 v21, s[12:15], s11 offen offset:1024 sc1 lds\n"
  "v_mfma_f32_16x16x32_bf16 v[180:183], v[144:147], a[164:167], 0\n"
  "s_waitcnt lgkmcnt(0)\n"
  "s_barrier\n"
  "s_waitcnt vmcnt(7)\n"
  "v_cmp_ne_u64 vcc, s[72:73], v[64:65]\n"
  "s_nop 3\n"
  "s_cbranch_vccz .Lhh_done_22x\n"
  "s_mov_b32 s63, 0x10000\n"
  ".Lhh_poll_21:\n"
  "buffer_load_dwordx2 v[64:65], v30, s[12:15], s48 offen sc1\n"
  "s_waitcnt vmcnt(0)\n"
  "v_cmp_ne_u64 vcc, s[72:73], v[64:65]\n"
  "s_nop 4\n"
  "s_cbranch_vccz .Lhh_done_22\n"
  "s_sleep 1\n"
  "s_sub_u32 s63, s63, 1\n"
  "s_cbranch_scc0 .Lhh_poll_21\n"
  ".Lhh_done_22:\n"
  ".Lhh_done_22x:\n"
  "v_mfma_f32_16x16x32_bf16 v[40:43], v[148:151], a[168:171], v[40:43]\n"
  "ds_read_b128 v[128:131], v28 offset:57344\n"
  "v_mfma_f32_16x16x32_bf16 v[84:87], v[148:151], a[172:175], v[84:87]\n"
  "ds_read_b128 v[132:135], v28 offset:58368\n"
  "v_mfma_f32_16x16x32_bf16 v[56:59], v[152:155], a[168:171], v[56:59]\n"
  "ds_read_b128 v[136:139], v28 offset:59392\n"
  "v_mfma_f32_16x16x32_bf16 v[88:91], v[152:155], a[172:175], v[88:91]\n"
  "ds_read_b128 v[140:143], v28 offset:60416\n"
  "v_mfma_f32_16x16x32_bf16 v[72:75], v[156:159], a[168:171], v[72:75]\n"
  "ds_read_b128 v[144:147], v28 offset:61440\n"
  "v_mfma_f32_16x16x32_bf16 v[92:95], v[156:159], a[172:175], v[92:95]\n"
  "s_mul_i32 s58, s31, 3\n"
  "s_add_u32 s58, s58, s5\n"
  "buffer_load_dwordx4 a[0:3], v21, s[12:15], s58 offen offset:1024 sc1\n"
  "v_mfma_f32_16x16x32_bf16 v[76:79], v[160:163], a[168:171], v[76:79]\n"
  "s_mul_i32 s59, s31, 3\n"
  "s_add_u32 s59, s59, s6\n"
  "buffer_load_dwordx4 a[4:7], v21, s[12:15], s59 offen offset:1024 sc1\n"
  "v_mfma_f32_16x16x32_bf16 v[176:179], v[160:163], a[172:175], v[176:179]\n"
  "v_mfma_f32_16x16x32_bf16 v[80:83], v[164:167], a[168:171], v[80:83]\n"
  "s_add_u32 s58, s58, 0xc00\n"
  "buffer_load_dwordx4 a[8:11], v21, s[12:15], s58 offen offset:1024 sc1\n"
  "v_mfma_f32_16x16x32_bf16 v[180:183], v[164:167], a[172:175], v[180:183]\n"
  "s_waitcnt lgkmcnt(4)\n"
  "v_mfma_f32_16x16x32_bf16 v[40:43], v[128:131], a[176:179], v[40:43]\n"
  "ds_read_b128 v[148:151], v28 offset:62464\n"
  "v_mfma_f32_16x16x32_bf16 v[84:87], v[128:131], a[180:183], v[84:87]\n"
  "ds_read_b128 v[152:155], v28 offset:63488\n"
  "s_waitcnt lgkmcnt(5)\n"
  "v_mfma_f32_16x16x32_bf16 v[56:59], v[132:135], a[176:179], v[56:59]\n"
  "ds_read_b128 v[156:159], v28 offset:64512\n"
  "v_mfma_f32_16x16x32_bf16 v[88:91], v[132:135], a[180:183], v[88:91]\n"
  "ds_read_b128 v[160:163], v29\n"
  "s_waitcnt lgkmcnt(6)\n"
  "v_mfma_f32_16x16x32_bf16 v[72:75], v[136:139], a[176:179], v[72:75]\n"
  "ds_read_b128 v[164:167], v29 offset:1024\n"
  "v_mfma_f32_16x16x32_bf16 v[92:95], v[136:139], a[180:183], v[92:95]\n"
  "s_add_u32 s59, s59, 0xc00\n"
  "buffer_load_dwordx4 a[12:15], v21, s[12:15], s59 offen offset:1024 sc1\n"
  "s_waitcnt lgkmcnt(6)\n"
  "v_mfma_f32_16x16x32_bf16 v[76:79], v[140:143], a[176:179], v[76:79]\n"
  "s_mul_i32 s58, s36, 3\n"
  "s_add_u32 s58, s58, s5\n"
  "buffer_load_dwordx4 a[16:19], v21, s[12:15], s58 offen offset:1024 sc1\n"
  "v_mfma_f32_16x16x32_bf16 v[176:179], v[140:143], a[180:183], v[176:179]\n"
  "s_waitcnt lgkmcnt(5)\n"
  "v_mfma_f32_16x16x32_bf16 v[80:83], v[144:147], a[176:179], v[80:83]\n"
  "s_mul_i32 s59, s36, 3\n"
  "s_add_u32 s59, s59, s6\n"
  "buffer_load_dwordx4 a[20:23], v21, s[12:15], s59 offen offset:1024 sc1\n"
  "v_mfma_f32_16x16x32_bf16 v[180:183], v[144:147], a[180:183], v[180:183]\n"
  "s_waitcnt lgkmcnt(0)\n"
  "s_barrier\n"
  "v_mfma_f32_16x16x32_bf16 v[40:43], v[148:151], a[184:187], v[40:43]\n"
  "ds_read_b128 v[128:131], v27 offset:40960\n"
  "v_mfma_f32_16x16x32_bf16 v[84:87], v[148:151], a[188:191], v[84:87]\n"
  "ds_read_b128 v[132:135], v27 offset:41984\n"
  "v_mfma_f32_16x16x32_bf16 v[56:59], v[152:155], a[184:187], v[56:59]\n"
  "ds_read_b128 v[136:139], v27 offset:43008\n"
  "v_mfma_f32_16x16x32_bf16 v[88:91], v[152:155], a[188:191], v[88:91]\n"
  "ds_read_b128 v[140:143], v27 offset:44032\n"
  "v_mfma_f32_16x16x32_bf16 v[72:75], v[156:159], a[184:187], v[72:75]\n"
  "ds_read_b128 v[144:147], v27 offset:45056\n"
  "v_mfma_f32_16x16x32_bf16 v[92:95], v[156:159], a[188:191], v[92:95]\n"
  "s_add_u32 s58, s58, 0xc00\n"
  "buffer_load_dwordx4 a[24:27], v21, s[12:15], s58 offen offset:1024 sc1\n"
  "v_mfma_f32_16x16x32_bf16 v[76:79], v[160:163], a[184:187], v[76:79]\n"
  "s_add_u32 s59, s59, 0xc00\n"
  "buffer_load_dwordx4 a[28:31], v21, s[12:15], s59 offen offset:1024 sc1\n"
  "v_mfma_f32_16x16x32_bf16 v[176:179], v[160:163], a[188:191], v[176:179]\n"
  "v_mfma_f32_16x16x32_bf16 v[80:83], v[164:167], a[184:187], v[80:83]\n"
  "s_mul_i32 s58, s37, 3\n"
  "s_add_u32 s58, s58, s5\n"
  "buffer_load_dwordx4 a[32:35], v21, s[12:15], s58 offen offset:1024 sc1\n"
  "v_mfma_f32_16x16x32_bf16 v[180:183], v[164:167], a[188:191], v[180:183]\n"
  "s_add_u32 s64, s65, 0xd000\n"
  "s_mov_b64 exec, 1\n"
  "buffer_store_dwordx2 v[10:11], off, s[12:15], s64 sc1\n"
  "s_mov_b64 exec, -1\n"
  "s_waitcnt lgkmcnt(4)\n"
  "v_mfma_f32_16x16x32_bf16 v[40:43], v[128:131], a[192:195], v[40:43]\n"
  "ds_read_b128 v[148:151], v27 offset:46080\n"
  "v_mfma_f32_16x16x32_bf16 v[84:87], v[128:131], a[196:199], v[84:87]\n"
  "ds_read_b128 v[152:155], v27 offset:47104\n"
  "s_waitcnt lgkmcnt(5)\n"
  "v_mfma_f32_16x16x32_bf16 v[56:59], v[132:135], a[192:195], v[56:59]\n"
  "ds_read_b128 v[156:159], v27 offset:48128\n"
  "v_mfma_f32_16x16x32_bf16 v[88:91], v[132:135], a[196:199], v[88:91]\n"
  "ds_read_b128 v[160:163], v27 offset:49152\n"
  "s_waitcnt lgkmcnt(6)\n"
  "v_mfma_f32_16x16x32_bf16 v[72:75], v[136:139], a[192:195], v[72:75]\n"
  "ds_read_b128 v[164:167], v27 offset:50176\n"
  "v_mfma_f32_16x16x32_bf16 v[92:95], v[136:139], a[196:199], v[92:95]\n"
  "s_mul_i32 s59, s37, 3\n"
  "s_add_u32 s59, s59, s6\n"
  "buffer_load_dwordx4 a[36:39], v21, s[12:15], s59 offen offset:1024 sc1\n"
  "s_waitcnt lgkmcnt(6)\n"
  "v_mfma_f32_16x16x32_bf16 v[76:79], v[140:143], a[192:195], v[76:79]\n"
  "buffer_load_dwordx4 v21, s[12:15], s11 offen offset:2048 sc1 lds\n"
  "v_mfma_f32_16x16x32_bf16 v[176:179], v[140:143], a[196:199], v[176:179]\n"
  "s_waitcnt lgkmcnt(5)\n"
  "v_mfma_f32_16x16x32_bf16 v[80:83], v[144:147], a[192:195], v[80:83]\n"
  "buffer_load_dwordx4 v21, s[12:15], s11 offen offset:3072 sc1 lds\n"
  "v_mfma_f32_16x16x32_bf16 v[180:183], v[144:147], a[196:199], v[180:183]\n"
  "s_waitcnt lgkmcnt(0)\n"
  "s_barrier\n"
  "v_mfma_f32_16x16x32_bf16 v[40:43], v[148:151], a[200:203], v[40:43]\n"
  "ds_read_b128 v[128:131], v29 offset:12288\n"
  "v_mfma_f32_16x16x32_bf16 v[84:87], v[148:151], a[204:207], v[84:87]\n"
  "ds_read_b128 v[132:135], v29 offset:13312\n"
  "v_mfma_f32_16x16x32_bf16 v[56:59], v[152:155], a[200:203], v[56:59]\n"
  "ds_read_b128 v[136:139], v29 offset:14336\n"
  "v_mfma_f32_16x16x32_bf16 v[88:91], v[152:155], a[204:207], v[88:91]\n"
  "ds_read_b128 v[140:143], v29 offset:15360\n"
  "v_mfma_f32_16x16x32_bf16 v[72:75], v[156:159], a[200:203], v[72:75]\n"
  "ds_read_b128 v[144:147], v29 offset:16384\n"
  "v_mfma_f32_16x16x32_bf16 v[92:95], v[156:159], a[204:207], v[92:95]\n"
  "s_add_u32 m0, m0, 0x1000\n"
  "s_add_u32 s11, s11, 0x1000\n"
  "buffer_load_dwordx4 v21, s[12:15], s11 offen sc1 lds\n"
  "v_mfma_f32_16x16x32_bf16 v[76:79], v[160:163], a[200:203], v[76:79]\n"
  "s_add_u32 s58, s58, 0xc00\n"
  "buffer_load_dwordx4 a[40:43], v21, s[12:15], s58 offen offset:1024 sc1\n"
  "v_mfma_f32_16x16x32_bf16 v[176:179], v[160:163], a[204:207], v[176:179]\n"
  "v_mfma_f32_16x16x32_bf16 v[80:83], v[164:167], a[200:203], v[80:83]\n"
  "s_add_u32 s59, s59, 0xc00\n"
  "buffer_load_dwordx4 a[44:47], v21, s[12:15], s59 offen offset:1024 sc1\n"
  "v_mfma_f32_16x16x32_bf16 v[180:183], v[164:167], a[204:207], v[180:183]\n"
  "s_waitcnt lgkmcnt(4)\n"
  "v_mfma_f32_16x16x32_bf16 v[40:43], v[128:131], a[208:211], v[40:43]\n"
  "ds_read_b128 v[148:151], v29 offset:17408\n"
  "v_mfma_f32_16x16x32_bf16 v[84:87], v[128:131], a[212:215], v[84:87]\n"
  "ds_read_b128 v[152:155], v29 offset:18432\n"
  "s_waitcnt lgkmcnt(5)\n"
  "v_mfma_f32_16x16x32_bf16 v[56:59], v[132:135], a[208:211], v[56:59]\n"
  "ds_read_b128 v[156:159], v29 offset:19456\n"
  "v_mfma_f32_16x16x32_bf16 v[88:91], v[132:135], a[212:215], v[88:91]\n"
  "ds_read_b128 v[160:163], v29 offset:20480\n"
  "s_waitcnt lgkmcnt(6)\n"
  "v_mfma_f32_16x16x32_bf16 v[72:75], v[136:139], a[208:211], v[72:75]\n"
  "ds_read_b128 v[164:167], v29 offset:21504\n"
  "v_mfma_f32_16x16x32_bf16 v[92:95], v[136:139], a[212:215], v[92:95]\n"
  "s_mul_i32 s58, s38, 3\n"
  "s_add_u32 s58, s58, s5\n"
  "buffer_load_dwordx4 a[48:51], v21, s[12:15], s58 offen offset:1024 sc1\n"
  "s_waitcnt lgkmcnt(6)\n"
  "v_mfma_f32_16x16x32_bf16 v[76:79], v[140:143], a[208:211], v[76:79]\n"
  "s_mul_i32 s59, s38, 3\n"
  "s_add_u32 s59, s59, s6\n"
  "buffer_load_dwordx4 a[52:55], v21, s[12:15], s59 offen offset:1024 sc1\n"
  "v_mfma_f32_16x16x32_bf16 v[176:179], v[140:143], a[212:215], v[176:179]\n"
  "s_waitcnt lgkmcnt(5)\n"
  "v_mfma_f32_16x16x32_bf16 v[80:83], v[144:147], a[208:211], v[80:83]\n"
  "s_mul_i32 s11, s31, 10\n"
  "s_add_u32 s11, s11, s26\n"
  "s_add_u32 m0, s9, 0x19000\n"
  "s_add_u32 s11, s11, 0x28000\n"
  "buffer_load_dwordx4 v21, s[12:15], s11 offen sc1 lds\n"
  "v_mfma_f32_16x16x32_bf16 v[180:183], v[144:147], a[212:215], v[180:183]\n"
  "s_waitcnt lgkmcnt(0)\n"
  "s_barrier\n"
  "v_mfma_f32_16x16x32_bf16 v[40:43], v[148:151], a[216:219], v[40:43]\n"
  "ds_read_b128 v[128:131], v27\n"
  "v_mfma_f32_16x16x32_bf16 v[84:87], v[148:151], a[220:223], v[84:87]\n"
  "ds_read_b128 v[132:135], v27 offset:1024\n"
  "v_mfma_f32_16x16x32_bf16 v[56:59], v[152:155], a[216:219], v[56:59]\n"
  "ds_read_b128 v[136:139], v27 offset:2048\n"
  "v_mfma_f32_16x16x32_bf16 v[88:91], v[152:155], a[220:223], v[88:91]\n"
  "ds_read_b128 v[140:143], v27 offset:3072\n"
  "v_mfma_f32_16x16x32_bf16 v[72:75], v[156:159], a[216:219], v[72:75]\n"
  "ds_read_b128 v[144:147], v27 offset:4096\n"
  "v_mfma_f32_16x16x32_bf16 v[92:95], v[156:159], a[220:223], v[92:95]\n"
  "buffer_load_dwordx4 v21, s[12:15], s11 offen offset:1024 sc1 lds\n"
  "v_mfma_f32_16x16x32_bf16 v[76:79], v[160:163], a[216:219], v[76:79]\n"
  "buffer_load_dwordx4 v21, s[12:15], s11 offen offset:2048 sc1 lds\n"
  "v_mfma_f32_16x16x32_bf16 v[176:179], v[160:163], a[220:223], v[176:179]\n"
  "v_mfma_f32_16x16x32_bf16 v[80:83], v[164:167], a[216:219], v[80:83]\n"
  "buffer_load_dwordx4 v21, s[12:15], s11 offen offset:3072 sc1 lds\n"
  "v_mfma_f32_16x16x32_bf16 v[180:183], v[164:167], a[220:223], v[180:183]\n"
  "s_waitcnt vmcnt(21) lgkmcnt(4)\n"
  "v_mfma_f32_16x16x32_bf16 v[40:43], v[128:131], a[0:3], v[40:43]\n"
  "ds_read_b128 v[148:151], v27 offset:5120\n"
  "s_waitcnt vmcnt(20)\n"
  "v_mfma_f32_16x16x32_bf16 v[84:87], v[128:131], a[4:7], v[84:87]\n"
  "ds_read_b128 v[152:155], v27 offset:6144\n"
  "s_waitcnt lgkmcnt(5)\n"
  "v_mfma_f32_16x16x32_bf16 v[56:59], v[132:135], a[0:3], v[56:59]\n"
  "ds_read_b128 v[156:159], v27 offset:7168\n"
  "v_mfma_f32_16x16x32_bf16 v[88:91], v[132:135], a[4:7], v[88:91]\n"
  "ds_read_b128 v[160:163], v27 offset:8192\n"
  "s_waitcnt lgkmcnt(6)\n"
  "v_mfma_f32_16x16x32_bf16 v[72:75], v[136:139], a[0:3], v[72:75]\n"
  "ds_read_b128 v[164:167], v27 offset:9216\n"
  "v_mfma_f32_16x16x32_bf16 v[92:95], v[136:139], a[4:7], v[92:95]\n"
  "s_add_u32 m0, m0, 0x1000\n"
  "s_add_u32 s11, s11, 0x1000\n"
  "buffer_load_dwordx4 v21, s[12:15], s11 offen sc1 lds\n"
  "s_waitcnt lgkmcnt(6)\n"
  "v_mfma_f32_16x16x32_bf16 v[76:79], v[140:143], a[0:3], v[76:79]\n"
  "s_add_u32 s58, s58, 0xc00\n"
  "buffer_load_dwordx4 a[56:59], v21, s[12:15], s58 offen offset:1024 sc1\n"
  "v_mfma_f32_16x16x32_bf16 v[176:179], v[140:143], a[4:7], v[176:179]\n"
  "s_waitcnt lgkmcnt(5)\n"
  "v_mfma_f32_16x16x32_bf16 v[80:83], v[144:147], a[0:3], v[80:83]\n"
  "s_add_u32 s59, s59, 0xc00\n"
  "buffer_load_dwordx4 a[60:63], v21, s[12:15], s59 offen offset:1024 sc1\n"
  "v_mfma_f32_16x16x32_bf16 v[180:183], v[144:147], a[4:7], v[180:183]\n"
  "s_waitcnt lgkmcnt(0)\n"
  "s_barrier\n"
  "s_waitcnt vmcnt(22)\n"
  "v_mfma_f32_16x16x32_bf16 v[40:43], v[148:151], a[8:11], v[40:43]\n"
  "ds_read_b128 v[128:131], v27 offset:20480\n"
  "s_waitcnt vmcnt(21)\n"
  "v_mfma_f32_16x16x32_bf16 v[84:87], v[148:151], a[12:15], v[84:87]\n"
  "ds_read_b128 v[132:135], v27 offset:21504\n"
  "v_mfma_f32_16x16x32_bf16 v[56:59], v[152:155], a[8:11], v[56:59]\n"
  "ds_read_b128 v[136:139], v27 offset:22528\n"
  "v_mfma_f32_16x16x32_bf16 v[88:91], v[152:155], a[12:15], v[88:91]\n"
  "ds_read_b128 v[140:143], v27 offset:23552\n"
  "v_mfma_f32_16x16x32_bf16 v[72:75], v[156:159], a[8:11], v[72:75]\n"
  "ds_read_b128 v[144:147], v27 offset:24576\n"
  "v_mfma_f32_16x16x32_bf16 v[92:95], v[156:159], a[12:15], v[92:95]\n"
  "s_mul_i32 s11, s36, 10\n"
  "s_add_u32 s11, s11, s26\n"
  "s_add_u32 m0, s9, 0x1e000\n"
  "s_add_u32 s11, s11, 0x28000\n"
  "buffer_load_dwordx4 v21, s[12:15], s11 offen sc1 lds\n"
  "v_mfma_f32_16x16x32_bf16 v[76:79], v[160:163], a[8:11], v[76:79]\n"
  "buffer_load_dwordx4 v21, s[12:15], s11 offen offset:1024 sc1 lds\n"
  "v_mfma_f32_16x16x32_bf16 v[176:179], v[160:163], a[12:15], v[176:179]\n"
  "v_mfma_f32_16x16x32_bf16 v[80:83], v[164:167], a[8:11], v[80:83]\n"
  "buffer_load_dwordx4 v21, s[12:15], s11 offen offset:2048 sc1 lds\n"
  "v_mfma_f32_16x16x32_bf16 v[180:183], v[164:167], a[12:15], v[180:183]\n"
  "s_waitcnt vmcnt(23) lgkmcnt(4)\n"
  "v_mfma_f32_16x16x32_bf16 v[40:43], v[128:131], a[16:19], v[40:43]\n"
  "ds_read_b128 v[148:151], v27 offset:25600\n"
  "s_waitcnt vmcnt(22)\n"
  "v_mfma_f32_16x16x32_bf16 v[84:87], v[128:131], a[20:23], v[84:87]\n"
  "ds_read_b128 v[152:155], v27 offset:26624\n"
  "s_waitcnt lgkmcnt(5)\n"
  "v_mfma_f32_16x16x32_bf16 v[56:59], v[132:135], a[16:19], v[56:59]\n"
  "ds_read_b128 v[156:159], v27 offset:27648\n"
  "v_mfma_f32_16x16x32_bf16 v[88:91], v[132:135], a[20:23], v[88:91]\n"
  "ds_read_b128 v[160:163], v27 offset:28672\n"
  "s_waitcnt lgkmcnt(6)\n"
  "v_mfma_f32_16x16x32_bf16 v[72:75], v[136:139], a[16:19], v[72:75]\n"
  "ds_read_b128 v[164:167], v27 offset:29696\n"
  "v_mfma_f32_16x16x32_bf16 v[92:95], v[136:139], a[20:23], v[92:95]\n"
  "s_add_u32 s48, s4, 0xd000\n"
  "buffer_load_dwordx2 v[64:65], v30, s[12:15], s48 offen sc1\n"
  "s_waitcnt lgkmcnt(6)\n"
  "v_mfma_f32_16x16x32_bf16 v[76:79], v[140:143], a[16:19], v[76:79]\n"
  "buffer_load_dwordx4 v21, s[12:15], s11 offen offset:3072 sc1 lds\n"
  "v_mfma_f32_16x16x32_bf16 v[176:179], v[140:143], a[20:23], v[176:179]\n"
  "s_add_u32 m0, m0, 0x1000\n"
  "s_add_u32 s11, s11, 0x1000\n"
  "buffer_load_dwordx4 v21, s[12:15], s11 offen sc1 lds\n"
  "s_waitcnt lgkmcnt(5)\n"
  "v_mfma_f32_16x16x32_bf16 v[80:83], v[144:147], a[16:19], v[80:83]\n"
  "s_mul_i32 s11, s37, 10\n"
  "s_add_u32 s11, s11, s26\n"
  "s_add_u32 m0, s9, 0xa000\n"
  "s_add_u32 s11, s11, 0x28000\n"
  "buffer_load_dwordx4 v21, s[12:15], s11 offen sc1 lds\n"
  "v_mfma_f32_16x16x32_bf16 v[180:183], v[144:147], a[20:23], v[180:183]\n"
  "s_waitcnt lgkmcnt(0)\n"
  "s_barrier\n"
  "s_waitcnt vmcnt(25)\n"
  "v_mfma_f32_16x16x32_bf16 v[40:43], v[148:151], a[24:27], v[40:43]\n"
  "ds_read_b128 v[128:131], v27 offset:61440\n"
  "s_waitcnt vmcnt(24)\n"
  "v_mfma_f32_16x16x32_bf16 v[84:87], v[148:151], a[28:31], v[84:87]\n"
  "ds_read_b128 v[132:135], v27 offset:62464\n"
  "v_mfma_f32_16x16x32_bf16 v[56:59], v[152:155], a[24:27], v[56:59]\n"
  "ds_read_b128 v[136:139], v27 offset:63488\n"
  "v_mfma_f32_16x16x32_bf16 v[88:91], v[152:155], a[28:31], v[88:91]\n"
  "ds_read_b128 v[140:143], v27 offset:64512\n"
  "v_mfma_f32_16x16x32_bf16 v[72:75], v[156:159], a[24:27], v[72:75]\n"
  "ds_read_b128 v[144:147], v28\n"
  "v_mfma_f32_16x16x32_bf16 v[92:95], v[156:159], a[28:31], v[92:95]\n"
  "buffer_load_dwordx4 v21, s[12:15], s11 offen offset:1024 sc1 lds\n"
  "v_mfma_f32_16x16x32_bf16 v[76:79], v[160:163], a[24:27], v[76:79]\n"
  "buffer_load_dwordx4 v21, s[12:15], s11 offen offset:2048 sc1 lds\n"
  "v_mfma_f32_16x16x32_bf16 v[176:179], v[160:163], a[28:31], v[176:179]\n"
  "v_mfma_f32_16x16x32_bf16 v[80:83], v[164:167], a[24:27], v[80:83]\n"
  "buffer_load_dwordx4 v21, s[12:15], s11 offen offset:3072 sc1 lds\n"
  "v_mfma_f32_16x16x32_bf16 v[180:183], v[164:167], a[28:31], v[180:183]\n"
  "s_waitcnt vmcnt(6)\n"
  "v_cmp_ne_u64 vcc, s[72:73], v[64:65]\n"
  "s_nop 3\n"
  "s_cbranch_vccz .Lhh_done_24x\n"
  "s_mov_b32 s63, 0x10000\n"
  ".Lhh_poll_23:\n"
  "buffer_load_dwordx2 v[64:65], v30, s[12:15], s48 offen sc1\n"
  "s_waitcnt vmcnt(0)\n"
  "v_cmp_ne_u64 vcc, s[72:73], v[64:65]\n"
  "s_nop 4\n"
  "s_cbranch_vccz .Lhh_done_24\n"
  "s_sleep 1\n"
  "s_sub_u32 s63, s63, 1\n"
  "s_cbranch_scc0 .Lhh_poll_23\n"
  ".Lhh_done_24:\n"
  ".Lhh_done_24x:\n"
  "s_waitcnt lgkmcnt(4)\n"
  "v_mfma_f32_16x16x32_bf16 v[40:43], v[128:131], a[32:35], v[40:43]\n"
  "ds_read_b128 v[148:151], v28 offset:1024\n"
  "v_mfma_f32_16x16x32_bf16 v[84:87], v[128:131], a[36:39], v[84:87]\n"
  "ds_read_b128 v[152:155], v28 offset:2048\n"
  "s_waitcnt lgkmcnt(5)\n"
  "v_mfma_f32_16x16x32_bf16 v[56:59], v[132:135], a[32:35], v[56:59]\n"
  "ds_read_b128 v[156:159], v28 offset:3072\n"
  "v_mfma_f32_16x16x32_bf16 v[88:91], v[132:135], a[36:39], v[88:91]\n"
  "ds_read_b128 v[160:163], v28 offset:4096\n"
  "s_waitcnt lgkmcnt(6)\n"
  "v_mfma_f32_16x16x32_bf16 v[72:75], v[136:139], a[32:35], v[72:75]\n"
  "ds_read_b128 v[164:167], v28 offset:5120\n"
  "v_mfma_f32_16x16x32_bf16 v[92:95], v[136:139], a[36:39], v[92:95]\n"
  "s_mul_i32 s58, s31, 3\n"
  "s_add_u32 s58, s58, s5\n"
  "buffer_load_dwordx4 a[64:67], v21, s[12:15], s58 offen offset:2048 sc1\n"
  "s_waitcnt lgkmcnt(6)\n"
  "v_mfma_f32_16x16x32_bf16 v[76:79], v[140:143], a[32:35], v[76:79]\n"
  "s_mul_i32 s59, s31, 3\n"
  "s_add_u32 s59, s59, s6\n"
  "buffer_load_dwordx4 a[68:71], v21, s[12:15], s59 offen offset:2048 sc1\n"
  "v_mfma_f32_16x16x32_bf16 v[176:179], v[140:143], a[36:39], v[176:179]\n"
  "s_waitcnt lgkmcnt(5)\n"
  "v_mfma_f32_16x16x32_bf16 v[80:83], v[144:147], a[32:35], v[80:83]\n"
  "s_add_u32 s58, s58, 0xc00\n"
  "buffer_load_dwordx4 a[72:75], v21, s[12:15], s58 offen offset:2048 sc1\n"
  "v_mfma_f32_16x16x32_bf16 v[180:183], v[144:147], a[36:39], v[180:183]\n"
  "s_waitcnt lgkmcnt(0)\n"
  "s_barrier\n"
  "v_mfma_f32_16x16x32_bf16 v[40:43], v[148:151], a[40:43], v[40:43]\n"
  "ds_read_b128 v[128:131], v28 offset:16384\n"
  "v_mfma_f32_16x16x32_bf16 v[84:87], v[148:151], a[44:47], v[84:87]\n"
  "ds_read_b128 v[132:135], v28 offset:17408\n"
  "v_mfma_f32_16x16x32_bf16 v[56:59], v[152:155], a[40:43], v[56:59]\n"
  "ds_read_b128 v[136:139], v28 offset:18432\n"
  "v_mfma_f32_16x16x32_bf16 v[88:91], v[152:155], a[44:47], v[88:91]\n"
  "ds_read_b128 v[140:143], v28 offset:19456\n"
  "v_mfma_f32_16x16x32_bf16 v[72:75], v[156:159], a[40:43], v[72:75]\n"
  "ds_read_b128 v[144:147], v28 offset:20480\n"
  "v_mfma_f32_16x16x32_bf16 v[92:95], v[156:159], a[44:47], v[92:95]\n"
  "s_add_u32 s59, s59, 0xc00\n"
  "buffer_load_dwordx4 a[76:79], v21, s[12:15], s59 offen offset:2048 sc1\n"
  "v_mfma_f32_16x16x32_bf16 v[76:79], v[160:163], a[40:43], v[76:79]\n"
  "s_mul_i32 s58, s36, 3\n"
  "s_add_u32 s58, s58, s5\n"
  "buffer_load_dwordx4 a[80:83], v21, s[12:15], s58 offen offset:2048 sc1\n"
  "v_mfma_f32_16x16x32_bf16 v[176:179], v[160:163], a[44:47], v[176:179]\n"
  "v_mfma_f32_16x16x32_bf16 v[80:83], v[164:167], a[40:43], v[80:83]\n"
  "s_mul_i32 s59, s36, 3\n"
  "s_add_u32 s59, s59, s6\n"
  "buffer_load_dwordx4 a[84:87], v21, s[12:15], s59 offen offset:2048 sc1\n"
  "v_mfma_f32_16x16x32_bf16 v[180:183], v[164:167], a[44:47], v[180:183]\n"
  "s_waitcnt lgkmcnt(4)\n"
  "v_mfma_f32_16x16x32_bf16 v[40:43], v[128:131], a[48:51], v[40:43]\n"
  "ds_read_b128 v[148:151], v28 offset:21504\n"
  "v_mfma_f32_16x16x32_bf16 v[84:87], v[128:131], a[52:55], v[84:87]\n"
  "ds_read_b128 v[152:155], v28 offset:22528\n"
  "s_waitcnt lgkmcnt(5)\n"
  "v_mfma_f32_16x16x32_bf16 v[56:59], v[132:135], a[48:51], v[56:59]\n"
  "ds_read_b128 v[156:159], v28 offset:23552\n"
  "v_mfma_f32_16x16x32_bf16 v[88:91], v[132:135], a[52:55], v[88:91]\n"
  "ds_read_b128 v[160:163], v28 offset:24576\n"
  "s_waitcnt lgkmcnt(6)\n"
  "v_mfma_f32_16x16x32_bf16 v[72:75], v[136:139], a[48:51], v[72:75]\n"
  "ds_read_b128 v[164:167], v28 offset:25600\n"
  "v_mfma_f32_16x16x32_bf16 v[92:95], v[136:139], a[52:55], v[92:95]\n"
  "s_add_u32 m0, m0, 0x1000\n"
  "s_add_u32 s11, s11, 0x1000\n"
  "buffer_load_dwordx4 v21, s[12:15], s11 offen sc1 lds\n"
  "s_waitcnt lgkmcnt(6)\n"
  "v_mfma_f32_16x16x32_bf16 v[76:79], v[140:143], a[48:51], v[76:79]\n"
  "s_add_u32 s58, s58, 0xc00\n"
  "buffer_load_dwordx4 a[88:91], v21, s[12:15], s58 offen offset:2048 sc1\n"
  "v_mfma_f32_16x16x32_bf16 v[176:179], v[140:143], a[52:55], v[176:179]\n"
  "s_waitcnt lgkmcnt(5)\n"
  "v_mfma_f32_16x16x32_bf16 v[80:83], v[144:147], a[48:51], v[80:83]\n"
  "s_add_u32 s59, s59, 0xc00\n"
  "buffer_load_dwordx4 a[92:95], v21, s[12:15], s59 offen offset:2048 sc1\n"
  "v_mfma_f32_16x16x32_bf16 v[180:183], v[144:147], a[52:55], v[180:183]\n"
  "s_waitcnt lgkmcnt(0)\n"
  "s_barrier\n"
  "v_mfma_f32_16x16x32_bf16 v[40:43], v[148:151], a[56:59], v[40:43]\n"
  "ds_read_b128 v[128:131], v28 offset:36864\n"
  "v_mfma_f32_16x16x32_bf16 v[84:87], v[148:151], a[60:63], v[84:87]\n"
  "ds_read_b128 v[132:135], v28 offset:37888\n"
  "v_mfma_f32_16x16x32_bf16 v[56:59], v[152:155], a[56:59], v[56:59]\n"
  "ds_read_b128 v[136:139], v28 offset:38912\n"
  "v_mfma_f32_16x16x32_bf16 v[88:91], v[152:155], a[60:63], v[88:91]\n"
  "ds_read_b128 v[140:143], v28 offset:39936\n"
  "v_mfma_f32_16x16x32_bf16 v[72:75], v[156:159], a[56:59], v[72:75]\n"
  "ds_read_b128 v[144:147], v28 offset:40960\n"
  "v_mfma_f32_16x16x32_bf16 v[92:95], v[156:159], a[60:63], v[92:95]\n"
  "s_mul_i32 s58, s37, 3\n"
  "s_add_u32 s58, s58, s5\n"
  "buffer_load_dwordx4 a[96:99], v21, s[12:15], s58 offen offset:2048 sc1\n"
  "v_mfma_f32_16x16x32_bf16 v[76:79], v[160:163], a[56:59], v[76:79]\n"
  "s_mul_i32 s59, s37, 3\n"
  "s_add_u32 s59, s59, s6\n"
  "buffer_load_dwordx4 a[100:103], v21, s[12:15], s59 offen offset:2048 sc1\n"
  "v_mfma_f32_16x16x32_bf16 v[176:179], v[160:163], a[60:63], v[176:179]\n"
  "v_mfma_f32_16x16x32_bf16 v[80:83], v[164:167], a[56:59], v[80:83]\n"
  "s_mul_i32 s11, s38, 10\n"
  "s_add_u32 s11, s11, s26\n"
  "s_add_u32 m0, s9, 0x23000\n"
  "s_add_u32 s11, s11, 0x28000\n"
  "buffer_load_dwordx4 v21, s[12:15], s11 offen sc1 lds\n"
  "v_mfma_f32_16x16x32_bf16 v[180:183], v[164:167], a[60:63], v[180:183]\n"
  "s_waitcnt vmcnt(11) lgkmcnt(4)\n"
  "v_mfma_f32_16x16x32_bf16 v[40:43], v[128:131], a[64:67], v[40:43]\n"
  "ds_read_b128 v[148:151], v28 offset:41984\n"
  "s_waitcnt vmcnt(10)\n"
  "v_mfma_f32_16x16x32_bf16 v[84:87], v[128:131], a[68:71], v[84:87]\n"
  "ds_read_b128 v[152:155], v28 offset:43008\n"
  "s_waitcnt lgkmcnt(5)\n"
  "v_mfma_f32_16x16x32_bf16 v[56:59], v[132:135], a[64:67], v[56:59]\n"
  "ds_read_b128 v[156:159], v28 offset:44032\n"
  "v_mfma_f32_16x16x32_bf16 v[88:91], v[132:135], a[68:71], v[88:91]\n"
  "ds_read_b128 v[160:163], v28 offset:45056\n"
  "s_waitcnt lgkmcnt(6)\n"
  "v_mfma_f32_16x16x32_bf16 v[72:75], v[136:139], a[64:67], v[72:75]\n"
  "ds_read_b128 v[164:167], v28 offset:46080\n"
  "v_mfma_f32_16x16x32_bf16 v[92:95], v[136:139], a[68:71], v[92:95]\n"
  "buffer_load_dwordx4 v21, s[12:15], s11 offen offset:1024 sc1 lds\n"
  "s_waitcnt lgkmcnt(6)\n"
  "v_mfma_f32_16x16x32_bf16 v[76:79], v[140:143], a[64:67], v[76:79]\n"
  "buffer_load_dwordx4 v21, s[12:15], s11 offen offset:2048 sc1 lds\n"
  "v_mfma_f32_16x16x32_bf16 v[176:179], v[140:143], a[68:71], v[176:179]\n"
  "s_waitcnt lgkmcnt(5)\n"
  "v_mfma_f32_16x16x32_bf16 v[80:83], v[144:147], a[64:67], v[80:83]\n"
  "buffer_load_dwordx4 v21, s[12:15], s11 offen offset:3072 sc1 lds\n"
  "v_mfma_f32_16x16x32_bf16 v[180:183], v[144:147], a[68:71], v[180:183]\n"
  "s_waitcnt lgkmcnt(0)\n"
  "s_barrier\n"
  "s_waitcnt vmcnt(12)\n"
  "v_mfma_f32_16x16x32_bf16 v[40:43], v[148:151], a[72:75], v[40:43]\n"
  "ds_read_b128 v[128:131], v28 offset:57344\n"
  "s_waitcnt vmcnt(11)\n"
  "v_mfma_f32_16x16x32_bf16 v[84:87], v[148:151], a[76:79], v[84:87]\n"
  "ds_read_b128 v[132:135], v28 offset:58368\n"
  "v_mfma_f32_16x16x32_bf16 v[56:59], v[152:155], a[72:75], v[56:59]\n"
  "ds_read_b128 v[136:139], v28 offset:59392\n"
  "v_mfma_f32_16x16x32_bf16 v[88:91], v[152:155], a[76:79], v[88:91]\n"
  "ds_read_b128 v[140:143], v28 offset:60416\n"
  "v_mfma_f32_16x16x32_bf16 v[72:75], v[156:159], a[72:75], v[72:75]\n"
  "ds_read_b128 v[144:147], v28 offset:61440\n"
  "v_mfma_f32_16x16x32_bf16 v[92:95], v[156:159], a[76:79], v[92:95]\n"
  "s_add_u32 m0, m0, 0x1000\n"
  "s_add_u32 s11, s11, 0x1000\n"
  "buffer_load_dwordx4 v21, s[12:15], s11 offen sc1 lds\n"
  "v_mfma_f32_16x16x32_bf16 v[76:79], v[160:163], a[72:75], v[76:79]\n"
  "s_add_u32 s58, s58, 0xc00\n"
  "buffer_load_dwordx4 a[104:107], v21, s[12:15], s58 offen offset:2048 sc1\n"
  "v_mfma_f32_16x16x32_bf16 v[176:179], v[160:163], a[76:79], v[176:179]\n"
  "v_mfma_f32_16x16x32_bf16 v[80:83], v[164:167], a[72:75], v[80:83]\n"
  "s_add_u32 s59, s59, 0xc00\n"
  "buffer_load_dwordx4 a[108:111], v21, s[12:15], s59 offen offset:2048 sc1\n"
  "v_mfma_f32_16x16x32_bf16 v[180:183], v[164:167], a[76:79], v[180:183]\n"
  "s_waitcnt vmcnt(13) lgkmcnt(4)\n"
  "v_mfma_f32_16x16x32_bf16 v[40:43], v[128:131], a[80:83], v[40:43]\n"
  "ds_read_b128 v[148:151], v28 offset:62464\n"
  "s_waitcnt vmcnt(12)\n"
  "v_mfma_f32_16x16x32_bf16 v[84:87], v[128:131], a[84:87], v[84:87]\n"
  "ds_read_b128 v[152:155], v28 offset:63488\n"
  "s_waitcnt lgkmcnt(5)\n"
  "v_mfma_f32_16x16x32_bf16 v[56:59], v[132:135], a[80:83], v[56:59]\n"
  "ds_read_b128 v[156:159], v28 offset:64512\n"
  "v_mfma_f32_16x16x32_bf16 v[88:91], v[132:135], a[84:87], v[88:91]\n"
  "ds_read_b128 v[160:163], v29\n"
  "s_waitcnt lgkmcnt(6)\n"
  "v_mfma_f32_16x16x32_bf16 v[72:75], v[136:139], a[80:83], v[72:75]\n"
  "ds_read_b128 v[164:167], v29 offset:1024\n"
  "v_mfma_f32_16x16x32_bf16 v[92:95], v[136:139], a[84:87], v[92:95]\n"
  "s_mul_i32 s58, s38, 3\n"
  "s_add_u32 s58, s58, s5\n"
  "buffer_load_dwordx4 a[112:115], v21, s[12:15], s58 offen offset:2048 sc1\n"
  "s_waitcnt lgkmcnt(6)\n"
  "v_mfma_f32_16x16x32_bf16 v[76:79], v[140:143], a[80:83], v[76:79]\n"
  "s_mul_i32 s59, s38, 3\n"
  "s_add_u32 s59, s59, s6\n"
  "buffer_load_dwordx4 a[116:119], v21, s[12:15], s59 offen offset:2048 sc1\n"
  "v_mfma_f32_16x16x32_bf16 v[176:179], v[140:143], a[84:87], v[176:179]\n"
  "s_waitcnt lgkmcnt(5)\n"
  "v_mfma_f32_16x16x32_bf16 v[80:83], v[144:147], a[80:83], v[80:83]\n"
  "s_add_u32 s58, s58, 0xc00\n"
  "buffer_load_dwordx4 a[120:123], v21, s[12:15], s58 offen offset:2048 sc1\n"
  "v_mfma_f32_16x16x32_bf16 v[180:183], v[144:147], a[84:87], v[180:183]\n"
  "s_waitcnt lgkmcnt(0)\n"
  "s_waitcnt vmcnt(14)\n"
  "s_barrier\n"
  "s_waitcnt vmcnt(13)\n"
  "v_mfma_f32_16x16x32_bf16 v[40:43], v[148:151], a[88:91], v[40:43]\n"
  "ds_read_b128 v[128:131], v27 offset:40960\n"
  "s_waitcnt vmcnt(12)\n"
  "v_mfma_f32_16x16x32_bf16 v[84:87], v[148:151], a[92:95], v[84:87]\n"
  "ds_read_b128 v[132:135], v27 offset:41984\n"
  "v_mfma_f32_16x16x32_bf16 v[56:59], v[152:155], a[88:91], v[56:59]\n"
  "ds_read_b128 v[136:139], v27 offset:43008\n"
  "v_mfma_f32_16x16x32_bf16 v[88:91], v[152:155], a[92:95], v[88:91]\n"
  "ds_read_b128 v[140:143], v27 offset:44032\n"
  "v_mfma_f32_16x16x32_bf16 v[72:75], v[156:159], a[88:91], v[72:75]\n"
  "ds_read_b128 v[144:147], v27 offset:45056\n"
  "v_mfma_f32_16x16x32_bf16 v[92:95], v[156:159], a[92:95], v[92:95]\n"
  "s_add_u32 s59, s59, 0xc00\n"
  "buffer_load_dwordx4 a[124:127], v21, s[12:15], s59 offen offset:2048 sc1\n"
  "v_mfma_f32_16x16x32_bf16 v[76:79], v[160:163], a[88:91], v[76:79]\n"
  "v_mfma_f32_16x16x32_bf16 v[176:179], v[160:163], a[92:95], v[176:179]\n"
  "v_mfma_f32_16x16x32_bf16 v[80:83], v[164:167], a[88:91], v[80:83]\n"
  "v_mfma_f32_16x16x32_bf16 v[180:183], v[164:167], a[92:95], v[180:183]\n"
  "s_waitcnt vmcnt(12) lgkmcnt(4)\n"
  "v_mfma_f32_16x16x32_bf16 v[40:43], v[128:131], a[96:99], v[40:43]\n"
  "ds_read_b128 v[148:151], v27 offset:46080\n"
  "s_waitcnt vmcnt(11)\n"
  "v_mfma_f32_16x16x32_bf16 v[84:87], v[128:131], a[100:103], v[84:87]\n"
  "ds_read_b128 v[152:155], v27 offset:47104\n"
  "s_waitcnt lgkmcnt(5)\n"
  "v_mfma_f32_16x16x32_bf16 v[56:59], v[132:135], a[96:99], v[56:59]\n"
  "ds_read_b128 v[156:159], v27 offset:48128\n"
  "v_mfma_f32_16x16x32_bf16 v[88:91], v[132:135], a[100:103], v[88:91]\n"
  "ds_read_b128 v[160:163], v27 offset:49152\n"
  "s_waitcnt lgkmcnt(6)\n"
  "v_mfma_f32_16x16x32_bf16 v[72:75], v[136:139], a[96:99], v[72:75]\n"
  "ds_read_b128 v[164:167], v27 offset:50176\n"
  "v_mfma_f32_16x16x32_bf16 v[92:95], v[136:139], a[100:103], v[92:95]\n"
  "s_waitcnt lgkmcnt(6)\n"
  "v_mfma_f32_16x16x32_bf16 v[76:79], v[140:143], a[96:99], v[76:79]\n"
  "v_mfma_f32_16x16x32_bf16 v[176:179], v[140:143], a[100:103], v[176:179]\n"
  "s_waitcnt lgkmcnt(5)\n"
  "v_mfma_f32_16x16x32_bf16 v[80:83], v[144:147], a[96:99], v[80:83]\n"
  "v_mfma_f32_16x16x32_bf16 v[180:183], v[144:147], a[100:103], v[180:183]\n"
  "s_waitcnt lgkmcnt(0)\n"
  "s_waitcnt vmcnt(6)\n"
  "s_barrier\n"
  "s_waitcnt vmcnt(5)\n"
  "v_mfma_f32_16x16x32_bf16 v[40:43], v[148:151], a[104:107], v[40:43]\n"
  "ds_read_b128 v[128:131], v29 offset:12288\n"
  "s_waitcnt vmcnt(4)\n"
  "v_mfma_f32_16x16x32_bf16 v[84:87], v[148:151], a[108:111], v[84:87]\n"
  "ds_read_b128 v[132:135], v29 offset:13312\n"
  "v_mfma_f32_16x16x32_bf16 v[56:59], v[152:155], a[104:107], v[56:59]\n"
  "ds_read_b128 v[136:139], v29 offset:14336\n"
  "v_mfma_f32_16x16x32_bf16 v[88:91], v[152:155], a[108:111], v[88:91]\n"
  "ds_read_b128 v[140:143], v29 offset:15360\n"
  "v_mfma_f32_16x16x32_bf16 v[72:75], v[156:159], a[104:107], v[72:75]\n"
  "ds_read_b128 v[144:147], v29 offset:16384\n"
  "v_mfma_f32_16x16x32_bf16 v[92:95], v[156:159], a[108:111], v[92:95]\n"
  "v_mfma_f32_16x16x32_bf16 v[76:79], v[160:163], a[104:107], v[76:79]\n"
  "v_mfma_f32_16x16x32_bf16 v[176:179], v[160:163], a[108:111], v[176:179]\n"
  "v_mfma_f32_16x16x32_bf16 v[80:83], v[164:167], a[104:107], v[80:83]\n"
  "v_mfma_f32_16x16x32_bf16 v[180:183], v[164:167], a[108:111], v[180:183]\n"
  "s_waitcnt vmcnt(3) lgkmcnt(4)\n"
  "v_mfma_f32_16x16x32_bf16 v[40:43], v[128:131], a[112:115], v[40:43]\n"
  "ds_read_b128 v[148:151], v29 offset:17408\n"
  "s_waitcnt vmcnt(2)\n"
  "v_mfma_f32_16x16x32_bf16 v[84:87], v[128:131], a[116:119], v[84:87]\n"
  "ds_read_b128 v[152:155], v29 offset:18432\n"
  "s_waitcnt lgkmcnt(5)\n"
  "v_mfma_f32_16x16x32_bf16 v[56:59], v[132:135], a[112:115], v[56:59]\n"
  "ds_read_b128 v[156:159], v29 offset:19456\n"
  "v_mfma_f32_16x16x32_bf16 v[88:91], v[132:135], a[116:119], v[88:91]\n"
  "ds_read_b128 v[160:163], v29 offset:20480\n"
  "s_waitcnt lgkmcnt(6)\n"
  "v_mfma_f32_16x16x32_bf16 v[72:75], v[136:139], a[112:115], v[72:75]\n"
  "ds_read_b128 v[164:167], v29 offset:21504\n"
  "v_mfma_f32_16x16x32_bf16 v[92:95], v[136:139], a[116:119], v[92:95]\n"
  "s_waitcnt lgkmcnt(6)\n"
  "v_mfma_f32_16x16x32_bf16 v[76:79], v[140:143], a[112:115], v[76:79]\n"
  "v_mfma_f32_16x16x32_bf16 v[176:179], v[140:143], a[116:119], v[176:179]\n"
  "s_waitcnt lgkmcnt(5)\n"
  "v_mfma_f32_16x16x32_bf16 v[80:83], v[144:147], a[112:115], v[80:83]\n"
  "v_mfma_f32_16x16x32_bf16 v[180:183], v[144:147], a[116:119], v[180:183]\n"
  "s_waitcnt vmcnt(0) lgkmcnt(4)\n"
  "v_mfma_f32_16x16x32_bf16 v[84:87], v[148:151], a[124:127], v[84:87]\n"
  "s_waitcnt lgkmcnt(3)\n"
  "v_mfma_f32_16x16x32_bf16 v[88:91], v[152:155], a[124:127], v[88:91]\n"
  "s_waitcnt lgkmcnt(2)\n"
  "v_mfma_f32_16x16x32_bf16 v[92:95], v[156:159], a[124:127], v[92:95]\n"
  "s_waitcnt lgkmcnt(1)\n"
  "v_mfma_f32_16x16x32_bf16 v[176:179], v[160:163], a[124:127], v[176:179]\n"
  "s_waitcnt lgkmcnt(0)\n"
  "v_mfma_f32_16x16x32_bf16 v[180:183], v[164:167], a[124:127], v[180:183]\n"
  "v_mfma_f32_16x16x32_bf16 v[40:43], v[148:151], a[120:123], v[40:43]\n"
  "v_mfma_f32_16x16x32_bf16 v[56:59], v[152:155], a[120:123], v[56:59]\n"
  "v_mfma_f32_16x16x32_bf16 v[72:75], v[156:159], a[120:123], v[72:75]\n"
  "v_mfma_f32_16x16x32_bf16 v[76:79], v[160:163], a[120:123], v[76:79]\n"
  "v_mfma_f32_16x16x32_bf16 v[80:83], v[164:167], a[120:123], v[80:83]\n"
  "ds_write_b128 v35, v[84:87]\n"
  "ds_write_b128 v35, v[88:91] offset:1024\n"
  "ds_write_b128 v35, v[92:95] offset:2048\n"
  "ds_write_b128 v35, v[176:179] offset:3072\n"
  "ds_write_b128 v35, v[180:183] offset:4096\n"
  "v_pk_add_f32 v[108:109], v[108:109], v[40:41]\n"
  "v_pk_add_f32 v[110:111], v[110:111], v[42:43]\n"
  "v_pk_add_f32 v[112:113], v[112:113], v[56:57]\n"
  "v_pk_add_f32 v[114:115], v[114:115], v[58:59]\n"
  "v_pk_add_f32 v[116:117], v[116:117], v[72:73]\n"
  "v_pk_add_f32 v[118:119], v[118:119], v[74:75]\n"
  "v_pk_add_f32 v[120:121], v[120:121], v[76:77]\n"
  "v_pk_add_f32 v[122:123], v[122:123], v[78:79]\n"
  "v_pk_add_f32 v[124:125], v[124:125], v[80:81]\n"
  "v_pk_add_f32 v[126:127], v[126:127], v[82:83]\n"
  "s_waitcnt lgkmcnt(0)\n"
  "s_barrier\n"
  "ds_read_b128 v[128:131], v66\n"
  "ds_read_b128 v[132:135], v66 offset:1024\n"
  "ds_read_b128 v[136:139], v66 offset:2048\n"
  "ds_read_b128 v[140:143], v66 offset:3072\n"
  "ds_read_b128 v[144:147], v66 offset:4096\n"
  "s_waitcnt lgkmcnt(4)\n"
  "v_pk_add_f32 v[108:109], v[108:109], v[128:129]\n"
  "v_pk_add_f32 v[110:111], v[110:111], v[130:131]\n"
  "s_waitcnt lgkmcnt(3)\n"
  "v_pk_add_f32 v[112:113], v[112:113], v[132:133]\n"
  "v_pk_add_f32 v[114:115], v[114:115], v[134:135]\n"
  "v_cvt_pk_bf16_f32 v96, v108, v109\n"
  "v_cvt_pk_bf16_f32 v97, v110, v111\n"
  "v_cvt_pk_bf16_f32 v98, v112, v113\n"
  "v_cvt_pk_bf16_f32 v99, v114, v115\n"
  "buffer_store_dwordx4 v[96:99], v9, s[32:35], s18 offen\n"
  "s_waitcnt lgkmcnt(2)\n"
  "v_pk_add_f32 v[116:117], v[116:117], v[136:137]\n"
  "v_pk_add_f32 v[118:119], v[118:119], v[138:139]\n"
  "s_waitcnt lgkmcnt(1)\n"
  "v_pk_add_f32 v[120:121], v[120:121], v[140:141]\n"
  "v_pk_add_f32 v[122:123], v[122:123], v[142:143]\n"
  "v_cvt_pk_bf16_f32 v100, v116, v117\n"
  "v_cvt_pk_bf16_f32 v101, v118, v119\n"
  "v_cvt_pk_bf16_f32 v102, v120, v121\n"
  "v_cvt_pk_bf16_f32 v103, v122, v123\n"
  "buffer_store_dwordx4 v[100:103], v9, s[32:35], s18 offen offset:16\n"
  "s_waitcnt lgkmcnt(0)\n"
  "v_pk_add_f32 v[124:125], v[124:125], v[144:145]\n"
  "v_pk_add_f32 v[126:127], v[126:127], v[146:147]\n"
  "v_cvt_pk_bf16_f32 v104, v124, v125\n"
  "v_cvt_pk_bf16_f32 v105, v126, v127\n"
  "buffer_store_dwordx2 v[104:105], v9, s[32:35], s18 offen offset:32\n"
  "s_or_b32 s67, s24, s25\n"
  "s_cmp_lg_u32 s67, 0\n"
  "s_nop 1\n"
  "s_cbranch_scc1 .Lhh_end_25\n"
  "s_sub_u32 s72, s72, 0x7f4a7c14\n"
  "s_subb_u32 s73, s73, 0x9e3779b9\n"
  "v_mov_b32 v10, s72\n"
  "v_mov_b32 v11, s73\n"
  "s_mov_b64 exec, 1\n"
  "buffer_store_dwordx2 v[10:11], off, s[12:15], 0 sc1\n"
  ".Lhh_end_25:\n"
  "s_endpgm\n"
  ".Lhh_end:\n"
  : "+s"(ka), "+s"(blk), "+v"(tid)
  : "s"((unsigned)(size_t)lds)
  : "v1","v2","v3","v4","v5","v6","v7","v8","v9","v10","v11","v12","v13","v14","v15","v16","v17","v18","v19","v20","v21","v22","v23","v24","v25","v26","v27","v28","v29","v30","v31","v32","v33","v34","v35","v36","v37","v38","v39","v40","v41","v42","v43","v44","v45","v46","v47","v48","v49","v50","v51","v52","v53","v54","v55","v56","v57","v58","v59","v60","v61","v62","v63","v64","v65","v66","v67","v68","v69","v70","v71","v72","v73","v74","v75","v76","v77","v78","v79","v80","v81","v82","v83","v84","v85","v86","v87","v88","v89","v90","v91","v92","v93","v94","v95","v96","v97","v98","v99","v100","v101","v102","v103","v104","v105","v106","v107","v108","v109","v110","v111","v112","v113","v114","v115","v116","v117","v118","v119","v120","v121","v122","v123","v124","v125","v126","v127","v128","v129","v130","v131","v132","v133","v134","v135","v136","v137","v138","v139","v140","v141","v142","v143","v144","v145","v146","v147","v148","v149","v150","v151","v152","v153","v154","v155","v156","v157","v158","v159","v160","v161","v162","v163","v164","v165","v166","v167","v168","v169","v170","v171","v172","v173","v174","v175","v176","v177","v178","v179","v180","v181","v182","v183","v184","v185","v186","v187","v188","v189","v190","v191","v192","v193","v194","v195","v196","v197","v198","v199","v200","v201","v202","v203","v204","v205","v206","v207","v208","v209","v210","v211","v212","v213","v214","v215","v216","v217","v218","v219","v220","v221","v222","v223","v224","v225","v226","v227","v228","v229","v230","v231","v232","v233","v234","v235","v236","v237","v238","v239","v240","v241","v242","v243","v244","v245","v246","v247","v248","v249","v250","v251","v252","v253","v254","v255","a0","a1","a2","a3","a4","a5","a6","a7","a8","a9","a10","a11","a12","a13","a14","a15","a16","a17","a18","a19","a20","a21","a22","a23","a24","a25","a26","a27","a28","a29","a30","a31","a32","a33","a34","a35","a36","a37","a38","a39","a40","a41","a42","a43","a44","a45","a46","a47","a48","a49","a50","a51","a52","a53","a54","a55","a56","a57","a58","a59","a60","a61","a62","a63","a64","a65","a66","a67","a68","a69","a70","a71","a72","a73","a74","a75","a76","a77","a78","a79","a80","a81","a82","a83","a84","a85","a86","a87","a88","a89","a90","a91","a92","a93","a94","a95","a96","a97","a98","a99","a100","a101","a102","a103","a104","a105","a106","a107","a108","a109","a110","a111","a112","a113","a114","a115","a116","a117","a118","a119","a120","a121","a122","a123","a124","a125","a126","a127","a128","a129","a130","a131","a132","a133","a134","a135","a136","a137","a138","a139","a140","a141","a142","a143","a144","a145","a146","a147","a148","a149","a150","a151","a152","a153","a154","a155","a156","a157","a158","a159","a160","a161","a162","a163","a164","a165","a166","a167","a168","a169","a170","a171","a172","a173","a174","a175","a176","a177","a178","a179","a180","a181","a182","a183","a184","a185","a186","a187","a188","a189","a190","a191","a192","a193","a194","a195","a196","a197","a198","a199","a200","a201","a202","a203","a204","a205","a206","a207","a208","a209","a210","a211","a212","a213","a214","a215","a216","a217","a218","a219","a220","a221","a222","a223","a224","a225","a226","a227","a228","a229","a230","a231","a232","a233","a234","a235","a236","a237","a238","a239","a240","a241","a242","a243","a244","a245","a246","a247","a248","a249","a250","a251","a252","a253","a254","a255","s4","s5","s6","s7","s8","s9","s10","s11","s12","s13","s14","s15","s16","s17","s18","s19","s20","s21","s22","s23","s24","s25","s26","s27","s28","s29","s30","s31","s33","s34","s35","s36","s37","s38","s39","s40","s41","s42","s43","s44","s45","s46","s47","s48","s49","s50","s51","s52","s53","s54","s55","s56","s57","s58","s59","s60","s61","s62","s63","s64","s65","s66","s67","s68","s69","s70","s71","s72","s73","s74","s75","s76","s77","s78","s79","s80","s81","s82","s83","s84","s85","s86","s87","s88","s89","s90","s91","s92","s93","s94","s95","s96","s97","s98","s99","vcc","scc","memory");
}

extern "C" void kernel_launch(void* const* d_in, const int* in_sizes, int n_in, void* d_out, int out_size, void* d_ws, size_t ws_size,
                              hipStream_t stream) {
  (void)in_sizes; (void)n_in; (void)out_size; (void)ws_size;
  hh_block_kernel<<<256, 256, 0, stream>>>(d_in[0], d_in[1], d_in[2], d_in[3], d_in[5], d_in[6], d_in[7], d_out, (char*)d_ws + 0);
}
```
